```python
import torch

def _softmax(x, axis=-1):
    return torch.softmax(x, dim=axis)

def _gelu(x):
    return torch.nn.functional.gelu(x, approximate='tanh')

def _silu(x):
    return torch.nn.functional.silu(x)

def _sigmoid(x):
    return torch.sigmoid(x)

def _layernorm(x, axis=-1, eps=1e-05):
    m = x.mean(dim=axis, keepdim=True)
    v = x.var(dim=axis, keepdim=True, unbiased=False)
    return (x - m) / torch.sqrt(v + eps)

def _scatter_add(tgt, idx, src, axis=0):
    return tgt.clone().index_add_(axis, idx, src)

def _segment_sum(src, seg, n):
    out = torch.zeros((n,) + src.shape[1:], dtype=src.dtype)
    return out.index_add_(0, seg, src)

def _pad(x, pw):
    flat = tuple((v for pair in reversed(pw) for v in pair))
    return torch.nn.functional.pad(x, flat)
kernel_name = 'parallel_sum__ssm_conv_h3__mixer_gmlp__prompt'

def setup_inputs():

    _w = lambda a: (lambda t: t.to(torch.bfloat16) if t.is_floating_point() else t)(torch.as_tensor(a))

    def _A_setup():
        B, S, D = (1, 2048, 576)
        ks = 4
        return {'q': _w((lambda *s: torch.randn(*s, dtype=torch.bfloat16) * 0.05)(B, S, D)), 'k': _w((lambda *s: torch.randn(*s, dtype=torch.bfloat16) * 1.6)(B, S, D)), 'v': _w((lambda *s: torch.randn(*s, dtype=torch.bfloat16) * 0.4)(B, S, D)), 'shift_k': _w((lambda *s: torch.randn(*s, dtype=torch.bfloat16) * 1.6)(D, ks) * 0.5), 'ssm_k': _w((lambda *s: torch.randn(*s, dtype=torch.bfloat16) * 3.2)(D, S) * (1.0 / S ** 0.5)), 'ssm_D': _w((lambda *s: torch.randn(*s, dtype=torch.bfloat16) * 0.8)(D))}
    _A_in = _A_setup()

    def _B_setup():
        B, S, D, F = (1, 2048, 576, 4608)
        Fh = F // 2
        return {'x': _w((lambda *s: torch.randn(*s, dtype=torch.bfloat16) * 0.05)(B, S, D)), 'w_in': _w((lambda *s: torch.randn(*s, dtype=torch.bfloat16) * 0.4)(D, 2 * Fh)), 'w_sp': _w(0.01 * (lambda *s: torch.randn(*s, dtype=torch.bfloat16) * 0.4)(S, S)), 'b_sp': _w(1.0 + 0.01 * (lambda *s: torch.randn(*s, dtype=torch.bfloat16) * 0.05)(S)), 'w_out': _w((lambda *s: torch.randn(*s, dtype=torch.bfloat16) * 0.05)(Fh, D))}
    _B_in = _B_setup()
    return {'A_q': _A_in['q'], 'A_k': _A_in['k'], 'A_v': _A_in['v'], 'A_shift_k': _A_in['shift_k'], 'A_ssm_k': _A_in['ssm_k'], 'A_ssm_D': _A_in['ssm_D'], 'B_x': _B_in['x'], 'B_w_in': _B_in['w_in'], 'B_w_sp': _B_in['w_sp'], 'B_b_sp': _B_in['b_sp'], 'B_w_out': _B_in['w_out']}

def reference(A_q, A_k, A_v, A_shift_k, A_ssm_k, A_ssm_D, B_x, B_w_in, B_w_sp, B_b_sp, B_w_out):

    def _A_ref(q, k, v, shift_k, ssm_k, ssm_D):
        B, S, D = q.shape
        ks = shift_k.shape[-1]
        k_pad = _pad(k, ((0, 0), (ks - 1, 0), (0, 0)))
        k_shift = torch.zeros_like(k)
        for t in range(ks):
            k_shift = k_shift + k_pad[:, t:t + S, :] * shift_k[:, ks - 1 - t][None, None, :]
        kv = k_shift * v
        L = 2 * S
        kv_pad = _pad(kv, ((0, 0), (0, S), (0, 0)))
        kf_pad = _pad(ssm_k, ((0, 0), (0, S)))
        kv_f = (lambda x, axes=(-2, -1): torch.fft.rfft2(x.float(), dim=axes))(kv_pad[:, None, :, :], axes=(1, 2))
        kf_f = (lambda x, axes=(-2, -1): torch.fft.rfft2(x.float(), dim=axes))(kf_pad[:, None, :], axes=(1, 2))
        kf_f = (lambda x, *d: x.permute(*d))(kf_f, 1, 2, 0)
        y_f = kv_f * kf_f[None, :, :, :]
        y = (lambda x, s, axes=(-2, -1): torch.fft.irfft2(x, s=s, dim=axes).to(torch.bfloat16))(y_f, (1, L), axes=(1, 2))[:, 0, :S, :]
        y = y + kv * ssm_D[None, None, :]
        return q * y

    def _B_ref(x, w_in, w_sp, b_sp, w_out):
        B, S, D = x.shape
        F = w_in.shape[-1]
        Fh = F // 2
        z = _gelu(torch.einsum('bsd,df->bsf', x, w_in))
        z1 = z[..., :Fh].clone()
        z2 = _layernorm(z[..., Fh:], axis=-1)
        g = torch.einsum('ts,bsf->btf', w_sp, z2) + b_sp[None, :, None]
        return torch.einsum('bsf,fd->bsd', z1 * g, w_out)
    x = A_q
    xn = _layernorm(x, axis=-1)
    return x + _A_ref(xn, A_k, A_v, A_shift_k, A_ssm_k, A_ssm_D) + _B_ref(xn, B_w_in, B_w_sp, B_b_sp, B_w_out)
```

```cpp
#include <hip/hip_runtime.h>
#include <hip/hip_bf16.h>
typedef unsigned short u16;
constexpr int S=2048, D=576, F=4608, FH=2304;
__device__ inline float b2f(u16 b){ return __uint_as_float(((unsigned)b)<<16); }
__device__ inline u16 f2b(float f){ unsigned u=__float_as_uint(f); u += 0x7fffu + ((u>>16)&1u); return (u16)(u>>16); }
__device__ inline float rb(float f){ return b2f(f2b(f)); }

__global__ void k_ln(const u16* in, int ldi, u16* out, int ldo, int n){
  int r=blockIdx.x; __shared__ float red[256];
  float s=0; for(int i=threadIdx.x;i<n;i+=256) s+=b2f(in[(size_t)r*ldi+i]);
  red[threadIdx.x]=s; __syncthreads();
  for(int o=128;o>0;o>>=1){ if(threadIdx.x<o) red[threadIdx.x]+=red[threadIdx.x+o]; __syncthreads(); }
  float m=red[0]/n; __syncthreads();
  float q=0; for(int i=threadIdx.x;i<n;i+=256){ float d=b2f(in[(size_t)r*ldi+i])-m; q+=d*d; }
  red[threadIdx.x]=q; __syncthreads();
  for(int o=128;o>0;o>>=1){ if(threadIdx.x<o) red[threadIdx.x]+=red[threadIdx.x+o]; __syncthreads(); }
  float v=red[0]/n;
  float rs=1.0f/sqrtf(v+1e-5f);
  for(int i=threadIdx.x;i<n;i+=256) out[(size_t)r*ldo+i]=f2b((b2f(in[(size_t)r*ldi+i])-m)*rs);
}
__global__ void k_kv(const u16* k,const u16* v,const u16* sk,u16* kv){
  int idx=blockIdx.x*256+threadIdx.x; int s=idx/D,d=idx%D;
  float a=0;
  for(int t=0;t<4;t++){ int ss=s+t-3; float kk= ss>=0? b2f(k[ss*D+d]):0.f; a=rb(a+rb(kk*b2f(sk[d*4+3-t]))); }
  kv[idx]=f2b(a*b2f(v[idx]));
}
__global__ void k_conv(const u16* kv,const u16* h,float* y){
  int idx=blockIdx.x*256+threadIdx.x; int s=idx/D,d=idx%D;
  float a=0; for(int j=0;j<=s;j++) a+=b2f(h[d*S+j])*b2f(kv[(s-j)*D+d]);
  y[idx]=a;
}
template<int MODE>
__global__ void k_gemm(const u16* A,int lda,const u16* B,int ldb,int K,u16* C,int ldc,
   const u16* e0,const u16* e1,const u16* e2,const u16* e3,const float* e4,const u16* e5){
  __shared__ float As[64][17], Bs[16][65];
  int tx=threadIdx.x%16, ty=threadIdx.x/16; int m0=blockIdx.y*64,n0=blockIdx.x*64;
  float acc[4][4]={};
  for(int k0=0;k0<K;k0+=16){
    for(int i=threadIdx.x;i<1024;i+=256){ As[i/16][i%16]=b2f(A[(size_t)(m0+i/16)*lda+k0+i%16]); Bs[i/64][i%64]=b2f(B[(size_t)(k0+i/64)*ldb+n0+i%64]); }
    __syncthreads();
    for(int k=0;k<16;k++){ float a[4],b[4]; for(int i=0;i<4;i++){a[i]=As[ty*4+i][k]; b[i]=Bs[k][tx*4+i];}
      for(int i=0;i<4;i++)for(int j=0;j<4;j++) acc[i][j]+=a[i]*b[j]; }
    __syncthreads();
  }
  for(int i=0;i<4;i++)for(int j=0;j<4;j++){ int m=m0+ty*4+i,n=n0+tx*4+j; float x=acc[i][j];
    if(MODE==0){ float u=0.7978845608f*(x+0.044715f*x*x*x); C[(size_t)m*ldc+n]=f2b(0.5f*x*(1.f+tanhf(u))); }
    if(MODE==1){ float g=x+b2f(e0[m]); C[(size_t)m*ldc+n]=f2b(g*b2f(e1[(size_t)m*F+n])); }
    if(MODE==2){ int id=m*D+n; float xx=b2f(e0[id]), xn=b2f(e1[id]), kv=b2f(e2[id]); float y=e4[id]+kv*b2f(e3[n]); C[id]=f2b(xx+xn*y+x); }
  }
}
extern "C" void kernel_launch(void* const* d_in, const int* in_sizes, int n_in, void* d_out, int out_size, void* d_ws, size_t ws_size, hipStream_t stream){
  const u16 *q=(const u16*)d_in[0],*k=(const u16*)d_in[1],*v=(const u16*)d_in[2],*sk=(const u16*)d_in[3],*hk=(const u16*)d_in[4],*sd=(const u16*)d_in[5];
  const u16 *win=(const u16*)d_in[7],*wsp=(const u16*)d_in[8],*bsp=(const u16*)d_in[9],*wout=(const u16*)d_in[10];
  char* ws=(char*)d_ws;
  u16* xn=(u16*)ws; u16* kv=(u16*)(ws+(4<<20)); float* y=(float*)(ws+(8<<20)); u16* z=(u16*)(ws+(16<<20)); u16* z2=(u16*)(ws+(48<<20)); u16* h=(u16*)(ws+(64<<20));
  k_ln<<<S,256,0,stream>>>(q,D,xn,D,D);
  k_kv<<<S*D/256,256,0,stream>>>(k,v,sk,kv);
  k_conv<<<S*D/256,256,0,stream>>>(kv,hk,y);
  k_gemm<0><<<dim3(F/64,S/64),256,0,stream>>>(xn,D,win,F,D,z,F,0,0,0,0,0,0);
  k_ln<<<S,256,0,stream>>>(z+FH,F,z2,FH,FH);
  k_gemm<1><<<dim3(FH/64,S/64),256,0,stream>>>(wsp,S,z2,FH,S,h,FH,bsp,z,0,0,0,0);
  k_gemm<2><<<dim3(D/64,S/64),256,0,stream>>>(h,FH,wout,D,FH,(u16*)d_out,D,q,xn,kv,sd,y,0);
}
```

```cpp
#include <hip/hip_runtime.h>
#include <hip/hip_bf16.h>
typedef unsigned short u16;
constexpr int S=2048, D=576, F=4608, FH=2304;
__device__ inline float b2f(u16 b){ return __uint_as_float(((unsigned)b)<<16); }
__device__ inline u16 f2b(float f){ unsigned u=__float_as_uint(f); u += 0x7fffu + ((u>>16)&1u); return (u16)(u>>16); }
__device__ inline float rb(float f){ return b2f(f2b(f)); }

__global__ void k_ln(const u16* in, int ldi, u16* out, int ldo, int n){
  int r=blockIdx.x; __shared__ float red[256];
  float s=0; for(int i=threadIdx.x;i<n;i+=256) s+=b2f(in[(size_t)r*ldi+i]);
  red[threadIdx.x]=s; __syncthreads();
  for(int o=128;o>0;o>>=1){ if(threadIdx.x<o) red[threadIdx.x]+=red[threadIdx.x+o]; __syncthreads(); }
  float m=red[0]/n; __syncthreads();
  float q=0; for(int i=threadIdx.x;i<n;i+=256){ float d=b2f(in[(size_t)r*ldi+i])-m; q+=d*d; }
  red[threadIdx.x]=q; __syncthreads();
  for(int o=128;o>0;o>>=1){ if(threadIdx.x<o) red[threadIdx.x]+=red[threadIdx.x+o]; __syncthreads(); }
  float v=red[0]/n;
  float rs=1.0f/sqrtf(v+1e-5f);
  for(int i=threadIdx.x;i<n;i+=256) out[(size_t)r*ldo+i]=f2b((b2f(in[(size_t)r*ldi+i])-m)*rs);
}
__global__ void k_kv(const u16* k,const u16* v,const u16* sk,u16* kv){
  int idx=blockIdx.x*256+threadIdx.x; int s=idx/D,d=idx%D;
  float a=0;
  for(int t=0;t<4;t++){ int ss=s+t-3; float kk= ss>=0? b2f(k[ss*D+d]):0.f; a=rb(a+rb(kk*b2f(sk[d*4+3-t]))); }
  kv[idx]=f2b(a*b2f(v[idx]));
}
__global__ void k_conv(const u16* kv,const u16* h,float* y){
  int idx=blockIdx.x*256+threadIdx.x; int s=idx/D,d=idx%D;
  float a=0; for(int j=0;j<=s;j++) a+=b2f(h[d*S+j])*b2f(kv[(s-j)*D+d]);
  y[idx]=a;
}
template<int MODE>
__global__ void k_gemm(const u16* A,int lda,const u16* B,int ldb,int K,u16* C,int ldc,
   const u16* e0,const u16* e1,const u16* e2,const u16* e3,const float* e4,const u16* e5){
  __shared__ float As[64][17], Bs[16][65];
  int tx=threadIdx.x%16, ty=threadIdx.x/16; int m0=blockIdx.y*64,n0=blockIdx.x*64;
  float acc[4][4]={};
  for(int k0=0;k0<K;k0+=16){
    for(int i=threadIdx.x;i<1024;i+=256){ As[i/16][i%16]=b2f(A[(size_t)(m0+i/16)*lda+k0+i%16]); Bs[i/64][i%64]=b2f(B[(size_t)(k0+i/64)*ldb+n0+i%64]); }
    __syncthreads();
    for(int k=0;k<16;k++){ float a[4],b[4]; for(int i=0;i<4;i++){a[i]=As[ty*4+i][k]; b[i]=Bs[k][tx*4+i];}
      for(int i=0;i<4;i++)for(int j=0;j<4;j++) acc[i][j]+=a[i]*b[j]; }
    __syncthreads();
  }
  for(int i=0;i<4;i++)for(int j=0;j<4;j++){ int m=m0+ty*4+i,n=n0+tx*4+j; float x=acc[i][j];
    if(MODE==0){ float u=0.7978845608f*(x+0.044715f*x*x*x); C[(size_t)m*ldc+n]=f2b(0.5f*x*(1.f+tanhf(u))); }
    if(MODE==1){ float g=x+b2f(e0[m]); C[(size_t)m*ldc+n]=f2b(g*b2f(e1[(size_t)m*F+n])); }
    if(MODE==2){ int id=m*D+n; float xx=b2f(e0[id]), xn=b2f(e1[id]), kv=b2f(e2[id]); float y=e4[id]+kv*b2f(e3[n]); C[id]=f2b(xx+xn*y+x); }
  }
}

__global__ void k_probe_lat(const unsigned* p, unsigned stride_dw, unsigned* sink){
  unsigned long long t0=__builtin_amdgcn_s_memrealtime(); unsigned idx=0, acc=0;
  for(int i=0;i<32;i++){ unsigned x; const unsigned* q=p+idx; asm volatile("global_load_dword %0, %1, off\n s_waitcnt vmcnt(0)":"=&v"(x):"v"(q):"memory"); acc+=x; idx+=stride_dw+(x&0); }
  unsigned long long t1=__builtin_amdgcn_s_memrealtime();
  unsigned long long d=(t1-t0)*200; while(__builtin_amdgcn_s_memrealtime()-t1<d) __builtin_amdgcn_s_sleep(8);
  if(acc==0x12345678) *sink=acc;
}
typedef unsigned u32x4 __attribute__((ext_vector_type(4)));
template<int NT> __global__ void k_probe_bw(const u32x4* p, unsigned* sink){
  unsigned long long t0=__builtin_amdgcn_s_memrealtime(); u32x4 r[8]; unsigned acc=0;
  const u32x4* q=p+blockIdx.x*2048+threadIdx.x;
  for(int i=0;i<8;i++){ if(NT) r[i]=__builtin_nontemporal_load(q+i*256); else r[i]=q[i*256]; }
  for(int i=0;i<8;i++) acc+=r[i][0]^r[i][1]^r[i][2]^r[i][3];
  if(acc==0x12345678) *sink=acc;
  __syncthreads();
  unsigned long long t1=__builtin_amdgcn_s_memrealtime();
  unsigned long long d=(t1-t0)*200; while(__builtin_amdgcn_s_memrealtime()-t1<d) __builtin_amdgcn_s_sleep(8);
}
extern "C" void kernel_launch(void* const* d_in, const int* in_sizes, int n_in, void* d_out, int out_size, void* d_ws, size_t ws_size, hipStream_t stream){
  const u16 *q=(const u16*)d_in[0],*k=(const u16*)d_in[1],*v=(const u16*)d_in[2],*sk=(const u16*)d_in[3],*hk=(const u16*)d_in[4],*sd=(const u16*)d_in[5];
  const u16 *win=(const u16*)d_in[7],*wsp=(const u16*)d_in[8],*bsp=(const u16*)d_in[9],*wout=(const u16*)d_in[10];
  char* ws=(char*)d_ws;
  u16* xn=(u16*)ws; u16* kv=(u16*)(ws+(4<<20)); float* y=(float*)(ws+(8<<20)); u16* z=(u16*)(ws+(16<<20)); u16* z2=(u16*)(ws+(48<<20)); u16* h=(u16*)(ws+(64<<20));

  k_ln<<<S,256,0,stream>>>(q,D,xn,D,D);
  k_kv<<<S*D/256,256,0,stream>>>(k,v,sk,kv);
  k_conv<<<S*D/256,256,0,stream>>>(kv,hk,y);
  k_gemm<0><<<dim3(F/64,S/64),256,0,stream>>>(xn,D,win,F,D,z,F,0,0,0,0,0,0);
  k_ln<<<S,256,0,stream>>>(z+FH,F,z2,FH,FH);
  k_gemm<1><<<dim3(FH/64,S/64),256,0,stream>>>(wsp,S,z2,FH,S,h,FH,bsp,z,0,0,0,0);
  k_gemm<2><<<dim3(D/64,S/64),256,0,stream>>>(h,FH,wout,D,FH,(u16*)d_out,D,q,xn,kv,sd,y,0);
}
```

```cpp
#include <hip/hip_runtime.h>

__global__ __launch_bounds__(256, 1) void fused_h3_gmlp(const void* x, const void* k, const void* v, const void* sk, const void* hk, const void* sd,
                                                        const void* win, const void* wsp, const void* bsp, const void* wout, void* out, void* ws) {
  __shared__ __attribute__((aligned(16))) char smem[163840];
  asm volatile(
"s_load_dwordx16 s[4:19], %0, 0x0\n"
"s_load_dwordx8 s[20:27], %0, 0x40\n"
"s_mov_b32 s64, %1\n"
"v_mov_b32 v0, %2\n"
"v_and_b32 v1, 63, v0\n"
"v_lshrrev_b32 v2, 6, v0\n"
"s_nop 0\n"
"v_readfirstlane_b32 s65, v2\n"
"s_and_b32 s71, s64, 7\n"
"s_lshr_b32 s72, s64, 3\n"
"s_and_b32 s66, s71, 1\n"
"s_lshl_b32 s66, s66, 3\n"
"s_and_b32 s73, s72, 7\n"
"s_add_u32 s66, s66, s73\n"
"s_lshr_b32 s67, s71, 1\n"
"s_lshl_b32 s67, s67, 2\n"
"s_lshr_b32 s73, s72, 3\n"
"s_add_u32 s67, s67, s73\n"
"v_lshrrev_b32 v156, 5, v1\n"
"s_lshl_b32 s71, s64, 3\n"
"s_lshl_b32 s72, s65, 1\n"
"s_add_u32 s71, s71, s72\n"
"v_add_u32 v156, s71, v156\n"
"v_and_b32 v159, 31, v1\n"
"v_lshlrev_b32 v157, 4, v159\n"
"s_movk_i32 s72, 1152\n"
"v_mad_u32_u24 v157, v156, s72, v157\n"
"v_cmp_gt_u32 vcc, 8, v159\n"
"v_mov_b32 v158, 2146435072\n"
"v_mov_b32 v160, 0\n"
"v_cndmask_b32 v158, v158, v157, vcc\n"
"v_cndmask_b32_e64 v160, v160, 1.0, vcc\n"
"v_lshrrev_b32 v6, 4, v1\n"
"v_xor_b32 v6, v6, v1\n"
"v_and_b32 v6, 7, v6\n"
"v_lshlrev_b32 v6, 4, v6\n"
"v_lshrrev_b32 v7, 3, v1\n"
"s_lshl_b32 s71, s66, 7\n"
"s_lshl_b32 s72, s65, 5\n"
"s_add_u32 s71, s71, s72\n"
"s_mul_i32 s71, s71, 1152\n"
"s_movk_i32 s72, 1152\n"
"v_mad_u32_u24 v6, v7, s72, v6\n"
"v_add_u32 v2, s71, v6\n"
"v_xor_b32 v6, 64, v2\n"
"v_add_u32 v3, 8192, v6\n"
"v_add_u32 v4, 16384, v2\n"
"v_add_u32 v5, 24576, v6\n"
"s_lshl_b32 s77, s65, 12\n"
"s_mul_i32 s78, s65, 9216\n"
"s_add_u32 s78, s78, 49152\n"
"v_mov_b32 v6, 0\n"
"s_waitcnt lgkmcnt(0)\n"
"s_mov_b32 s28, s4\n"
"s_and_b32 s29, s5, 0xffff\n"
"s_mov_b32 s30, 2359296\n"
"s_mov_b32 s31, 0x20000\n"
"s_mov_b32 s4, 0\n"
"buffer_load_dwordx4 v[120:123], v157, s[28:31], 0 offen offset:0\n"
"buffer_load_dwordx4 v[124:127], v157, s[28:31], 0 offen offset:512\n"
"buffer_load_dwordx4 v[128:131], v158, s[28:31], 0 offen offset:1024\n"
"s_mov_b32 s36, s26\n"
"s_and_b32 s37, s27, 0xffff\n"
"s_mov_b32 s38, 31457280\n"
"s_mov_b32 s39, 0x20000\n"
"buffer_load_dwordx2 v[202:203], v6, s[36:39], 0 offen sc0 sc1\n"
"s_getpc_b64 s[74:75]\n"
"pcbase_%=:\n"
"s_and_b32 s74, s74, 0xffffff00\n"
"s_mov_b32 s76, kend_%=-pcbase_%=-4\n"
"s_lshr_b32 s71, s64, 3\n"
"s_lshl_b32 s71, s71, 11\n"
"s_lshl_b32 s72, s65, 9\n"
"s_add_u32 s71, s71, s72\n"
"v_lshrrev_b32 v112, 4, v1\n"
"v_lshl_add_u32 v112, v112, 7, s71\n"
"v_min_u32 v112, s76, v112\n"
"global_load_dword a254, v112, s[74:75]\n"
"s_add_u32 m0, s77, 0\n"
"s_nop 0\n"
"buffer_load_dwordx4 v2, s[28:31], s4 offen offset:0 lds\n"
"buffer_load_dwordx4 v3, s[28:31], s4 offen offset:1024 lds\n"
"buffer_load_dwordx4 v4, s[28:31], s4 offen offset:2048 lds\n"
"buffer_load_dwordx4 v5, s[28:31], s4 offen offset:3072 lds\n"
"s_add_u32 s4, s4, 128\n"
"s_lshr_b32 s71, s65, 1\n"
"s_mul_i32 s71, s71, 294912\n"
"s_and_b32 s72, s65, 1\n"
"s_xor_b32 s72, s72, 1\n"
"s_mul_i32 s72, s72, 4608\n"
"s_add_u32 s71, s71, s72\n"
"s_lshl_b32 s73, s67, 5\n"
"s_add_u32 s73, s73, s71\n"
"s_lshl_b32 s72, s67, 8\n"
"s_add_u32 s71, s71, s72\n"
"s_movk_i32 s72, 9216\n"
"v_lshrrev_b32 v6, 4, v1\n"
"v_lshlrev_b32 v7, 1, v6\n"
"v_xor_b32 v7, v7, v1\n"
"v_and_b32 v7, 15, v7\n"
"v_lshlrev_b32 v7, 4, v7\n"
"v_mul_u32_u24 v6, s72, v6\n"
"v_add_u32 v6, s71, v6\n"
"v_add_u32 v193, v6, v7\n"
"v_xor_b32 v7, 128, v7\n"
"v_add_u32 v7, v6, v7\n"
"s_mov_b32 s32, s16\n"
"s_and_b32 s33, s17, 0xffff\n"
"s_mov_b32 s34, 5308416\n"
"s_mov_b32 s35, 0x20000\n"
"s_mov_b32 s5, 0\n"
"v_add_u32 v194, 35840, v193\n"
"v_add_u32 v195, 71680, v7\n"
"v_add_u32 v196, 107520, v7\n"
"v_add_u32 v197, 147456, v193\n"
"v_add_u32 v198, 183296, v193\n"
"v_add_u32 v199, 219136, v7\n"
"v_add_u32 v200, 254976, v7\n"
"v_lshrrev_b32 v6, 1, v1\n"
"v_and_b32 v7, 0x13, v6\n"
"v_and_b32 v201, 4, v6\n"
"v_lshl_or_b32 v7, v201, 1, v7\n"
"v_bfe_u32 v6, v6, 3, 1\n"
"v_lshl_or_b32 v7, v6, 2, v7\n"
"v_and_b32 v6, 1, v1\n"
"v_lshl_add_u32 v6, v6, 4, s73\n"
"v_mad_u32_u24 v6, v7, s72, v6\n"
"v_add_u32 v201, 4096, v6\n"
"v_min_u32 v114, 161, v0\n"
"v_lshlrev_b32 v114, 7, v114\n"
"s_mul_i32 s79, s64, 20736\n"
"buffer_load_dword a250, v114, s[32:35], s79 offen\n"
"s_add_u32 m0, s78, 0\n"
"s_nop 0\n"
"buffer_load_dwordx4 v193, s[32:35], s5 offen offset:0 lds\n"
"buffer_load_dwordx4 v194, s[32:35], s5 offen offset:1024 lds\n"
"s_cmp_lt_u32 s65, 2\n"
"s_cselect_b32 s40, s6, s8\n"
"s_cselect_b32 s41, s7, s9\n"
"s_and_b32 s41, s41, 0xffff\n"
"s_mov_b32 s42, 2359296\n"
"s_mov_b32 s43, 0x20000\n"
"s_mov_b32 s48, s18\n"
"s_and_b32 s49, s19, 0xffff\n"
"s_mov_b32 s50, 8388608\n"
"s_mov_b32 s51, 0x20000\n"
"s_mov_b32 s52, s12\n"
"s_and_b32 s53, s13, 0xffff\n"
"s_mov_b32 s54, 2359296\n"
"s_mov_b32 s55, 0x20000\n"
"s_mov_b32 s56, s22\n"
"s_and_b32 s57, s23, 0xffff\n"
"s_mov_b32 s58, 2654208\n"
"s_mov_b32 s59, 0x20000\n"
"s_mov_b32 s60, s24\n"
"s_and_b32 s61, s25, 0xffff\n"
"s_mov_b32 s62, 2359296\n"
"s_mov_b32 s63, 0x20000\n"
"s_mov_b32 s70, 100000\n"
"s_mov_b32 s6, s77\n"
"s_mov_b32 s7, s78\n"
"s_add_u32 s44, s65, 0\n"
"s_and_b32 s44, s44, 3\n"
"s_add_u32 s45, s65, 1\n"
"s_and_b32 s45, s45, 3\n"
"s_add_u32 s46, s65, 2\n"
"s_and_b32 s46, s46, 3\n"
"s_add_u32 s47, s65, 3\n"
"s_and_b32 s47, s47, 3\n"
"v_and_b32 v108, 15, v1\n"
"v_lshrrev_b32 v109, 4, v1\n"
"v_lshrrev_b32 v110, 1, v108\n"
"v_xor_b32 v109, v109, v110\n"
"v_lshlrev_b32 v109, 4, v109\n"
"v_lshl_add_u32 v109, v108, 7, v109\n"
"s_lshl_b32 s71, s44, 12\n"
"v_add_u32 v176, s71, v109\n"
"s_lshl_b32 s71, s45, 12\n"
"v_add_u32 v177, s71, v109\n"
"s_lshl_b32 s71, s46, 12\n"
"v_add_u32 v178, s71, v109\n"
"s_lshl_b32 s71, s47, 12\n"
"v_add_u32 v179, s71, v109\n"
"v_xor_b32 v180, 64, v176\n"
"v_xor_b32 v181, 64, v177\n"
"v_xor_b32 v182, 64, v178\n"
"v_xor_b32 v183, 64, v179\n"
"v_bfe_u32 v108, v1, 2, 3\n"
"v_lshrrev_b32 v109, 4, v1\n"
"v_bfe_u32 v110, v1, 2, 2\n"
"v_lshl_add_u32 v109, v109, 3, v110\n"
"v_lshlrev_b32 v109, 8, v109\n"
"s_lshl_b32 s71, s65, 1\n"
"v_xor_b32 v110, s71, v108\n"
"v_lshl_add_u32 v109, v110, 5, v109\n"
"v_and_b32 v110, 3, v1\n"
"v_lshl_add_u32 v109, v110, 3, v109\n"
"s_lshl_b32 s71, s65, 4\n"
"s_add_u32 s71, s71, s66\n"
"s_min_u32 s71, s71, 17\n"
"s_cmp_ge_u32 s71, 9\n"
"s_cselect_b32 s72, 9, 0\n"
"s_sub_u32 s71, s71, s72\n"
"s_lshl_b32 s72, s72, 10\n"
"v_xor_b32 v112, s71, v108\n"
"v_lshlrev_b32 v112, 5, v112\n"
"v_and_b32 v113, 0xffffff1f, v109\n"
"v_add_u32 v112, v112, v113\n"
"v_lshrrev_b32 v111, 5, v1\n"
"v_lshl_add_u32 v108, v111, 4, v108\n"
"v_lshlrev_b32 v108, 5, v108\n"
"v_lshl_add_u32 v108, v110, 3, v108\n"
"v_add_u32 v184, 49152, v109\n"
"v_xor_b32 v185, 32, v184\n"
"v_add_u32 v186, 57344, v108\n"
"v_add_u32 v187, 86016, v109\n"
"v_xor_b32 v188, 32, v187\n"
"v_add_u32 v189, 94208, v108\n"
"v_add_u32 v190, 122880, v109\n"
"v_xor_b32 v191, 32, v190\n"
"v_add_u32 v192, 131072, v108\n"
"v_add_u32 v113, 8192, v108\n"
"s_cmp_eq_u32 s71, 8\n"
"s_cselect_b64 vcc, -1, 0\n"
"s_cselect_b32 s73, 1, 4\n"
"s_lshl_b32 s73, s73, 8\n"
"v_cndmask_b32 v112, v112, v113, vcc\n"
"v_add_u32 v112, s72, v112\n"
"v_add_u32 v164, 49152, v112\n"
"v_add_u32 v165, s73, v164\n"
"v_add_u32 v166, 86016, v112\n"
"v_add_u32 v167, s73, v166\n"
"v_add_u32 v168, 122880, v112\n"
"v_add_u32 v169, s73, v168\n"
"v_mov_b32 v108, 0x3f803f80\n"
"v_accvgpr_write_b32 a112, v108\n"
"v_accvgpr_write_b32 a113, v108\n"
"v_accvgpr_write_b32 a114, v108\n"
"v_accvgpr_write_b32 a115, v108\n"
"s_waitcnt vmcnt(8)\n"
"v_add_u32 v202, 1, v202\n"
"v_add_u32 v203, 1, v203\n"
"v_readfirstlane_b32 s68, v202\n"
"v_readfirstlane_b32 s69, v203\n"
"v_lshlrev_b32 v132, 16, v120\n"
"v_and_b32 v133, 0xffff0000, v120\n"
"v_lshlrev_b32 v134, 16, v121\n"
"v_and_b32 v135, 0xffff0000, v121\n"
"v_lshlrev_b32 v136, 16, v122\n"
"v_and_b32 v137, 0xffff0000, v122\n"
"v_lshlrev_b32 v138, 16, v123\n"
"v_and_b32 v139, 0xffff0000, v123\n"
"v_lshlrev_b32 v140, 16, v124\n"
"buffer_load_dwordx4 v195, s[32:35], s5 offen offset:2048 lds\n"
"v_and_b32 v141, 0xffff0000, v124\n"
"v_lshlrev_b32 v142, 16, v125\n"
"v_and_b32 v143, 0xffff0000, v125\n"
"v_lshlrev_b32 v144, 16, v126\n"
"v_and_b32 v145, 0xffff0000, v126\n"
"v_lshlrev_b32 v146, 16, v127\n"
"v_and_b32 v147, 0xffff0000, v127\n"
"v_lshlrev_b32 v148, 16, v128\n"
"v_and_b32 v149, 0xffff0000, v128\n"
"buffer_load_dwordx4 v196, s[32:35], s5 offen offset:3072 lds\n"
"v_lshlrev_b32 v150, 16, v129\n"
"v_and_b32 v151, 0xffff0000, v129\n"
"v_lshlrev_b32 v152, 16, v130\n"
"v_and_b32 v153, 0xffff0000, v130\n"
"v_lshlrev_b32 v154, 16, v131\n"
"v_and_b32 v155, 0xffff0000, v131\n"
"v_pk_add_f32 v[120:121], v[132:133], v[134:135]\n"
"v_pk_add_f32 v[122:123], v[136:137], v[138:139]\n"
"v_pk_add_f32 v[120:121], v[120:121], v[140:141]\n"
"s_add_u32 m0, s78, 4096\n"
"s_nop 0\n"
"buffer_load_dwordx4 v197, s[32:35], s5 offen offset:0 lds\n"
"v_pk_add_f32 v[122:123], v[122:123], v[142:143]\n"
"v_pk_add_f32 v[120:121], v[120:121], v[144:145]\n"
"v_pk_add_f32 v[122:123], v[122:123], v[146:147]\n"
"v_pk_add_f32 v[120:121], v[120:121], v[148:149]\n"
"v_pk_add_f32 v[122:123], v[122:123], v[150:151]\n"
"v_pk_add_f32 v[120:121], v[120:121], v[152:153]\n"
"v_pk_add_f32 v[122:123], v[122:123], v[154:155]\n"
"v_pk_add_f32 v[120:121], v[120:121], v[122:123]\n"
"v_add_f32 v120, v120, v121\n"
"buffer_load_dwordx4 v198, s[32:35], s5 offen offset:1024 lds\n"
"s_nop 0\n"
"v_add_f32_dpp v120, v120, v120 row_ror:8 row_mask:0xf bank_mask:0xf\n"
"s_nop 1\n"
"v_add_f32_dpp v120, v120, v120 row_ror:4 row_mask:0xf bank_mask:0xf\n"
"s_nop 1\n"
"v_add_f32_dpp v120, v120, v120 row_ror:2 row_mask:0xf bank_mask:0xf\n"
"s_nop 1\n"
"v_add_f32_dpp v120, v120, v120 row_ror:1 row_mask:0xf bank_mask:0xf\n"
"v_mov_b32 v126, v120\n"
"s_nop 1\n"
"v_permlane16_swap_b32 v120, v126\n"
"v_add_f32 v120, v120, v126\n"
"v_mul_f32 v124, 0x3ae38e39, v120\n"
"v_pk_add_f32 v[132:133], v[132:133], v[124:125] op_sel_hi:[1,0] neg_lo:[0,1] neg_hi:[0,1]\n"
"buffer_load_dwordx4 v199, s[32:35], s5 offen offset:2048 lds\n"
"v_pk_add_f32 v[134:135], v[134:135], v[124:125] op_sel_hi:[1,0] neg_lo:[0,1] neg_hi:[0,1]\n"
"v_pk_add_f32 v[136:137], v[136:137], v[124:125] op_sel_hi:[1,0] neg_lo:[0,1] neg_hi:[0,1]\n"
"v_pk_add_f32 v[138:139], v[138:139], v[124:125] op_sel_hi:[1,0] neg_lo:[0,1] neg_hi:[0,1]\n"
"v_pk_add_f32 v[140:141], v[140:141], v[124:125] op_sel_hi:[1,0] neg_lo:[0,1] neg_hi:[0,1]\n"
"v_pk_add_f32 v[142:143], v[142:143], v[124:125] op_sel_hi:[1,0] neg_lo:[0,1] neg_hi:[0,1]\n"
"v_pk_add_f32 v[144:145], v[144:145], v[124:125] op_sel_hi:[1,0] neg_lo:[0,1] neg_hi:[0,1]\n"
"v_pk_add_f32 v[146:147], v[146:147], v[124:125] op_sel_hi:[1,0] neg_lo:[0,1] neg_hi:[0,1]\n"
"v_pk_add_f32 v[148:149], v[148:149], v[124:125] op_sel_hi:[1,0] neg_lo:[0,1] neg_hi:[0,1]\n"
"v_pk_add_f32 v[150:151], v[150:151], v[124:125] op_sel_hi:[1,0] neg_lo:[0,1] neg_hi:[0,1]\n"
"buffer_load_dwordx4 v200, s[32:35], s5 offen offset:3072 lds\n"
"v_pk_add_f32 v[152:153], v[152:153], v[124:125] op_sel_hi:[1,0] neg_lo:[0,1] neg_hi:[0,1]\n"
"v_pk_add_f32 v[154:155], v[154:155], v[124:125] op_sel_hi:[1,0] neg_lo:[0,1] neg_hi:[0,1]\n"
"v_pk_mul_f32 v[148:149], v[148:149], v[160:161] op_sel_hi:[1,0]\n"
"v_pk_mul_f32 v[150:151], v[150:151], v[160:161] op_sel_hi:[1,0]\n"
"v_pk_mul_f32 v[152:153], v[152:153], v[160:161] op_sel_hi:[1,0]\n"
"v_pk_mul_f32 v[154:155], v[154:155], v[160:161] op_sel_hi:[1,0]\n"
"v_pk_mul_f32 v[120:121], v[132:133], v[132:133]\n"
"v_pk_mul_f32 v[122:123], v[134:135], v[134:135]\n"
"v_pk_fma_f32 v[120:121], v[136:137], v[136:137], v[120:121]\n"
"s_add_u32 m0, s78, 8192\n"
"s_nop 0\n"
"buffer_load_dwordx4 v201, s[32:35], s5 offen offset:0 lds\n"
"s_add_u32 s5, s5, 589824\n"
"v_pk_fma_f32 v[122:123], v[138:139], v[138:139], v[122:123]\n"
"v_pk_fma_f32 v[120:121], v[140:141], v[140:141], v[120:121]\n"
"v_pk_fma_f32 v[122:123], v[142:143], v[142:143], v[122:123]\n"
"v_pk_fma_f32 v[120:121], v[144:145], v[144:145], v[120:121]\n"
"v_pk_fma_f32 v[122:123], v[146:147], v[146:147], v[122:123]\n"
"v_pk_fma_f32 v[120:121], v[148:149], v[148:149], v[120:121]\n"
"v_pk_fma_f32 v[122:123], v[150:151], v[150:151], v[122:123]\n"
"v_pk_fma_f32 v[120:121], v[152:153], v[152:153], v[120:121]\n"
"v_pk_fma_f32 v[122:123], v[154:155], v[154:155], v[122:123]\n"
"s_add_u32 m0, s6, 16384\n"
"s_nop 0\n"
"buffer_load_dwordx4 v2, s[28:31], s4 offen offset:0 lds\n"
"v_pk_add_f32 v[120:121], v[120:121], v[122:123]\n"
"v_add_f32 v120, v120, v121\n"
"s_nop 1\n"
"v_add_f32_dpp v120, v120, v120 row_ror:8 row_mask:0xf bank_mask:0xf\n"
"s_nop 1\n"
"v_add_f32_dpp v120, v120, v120 row_ror:4 row_mask:0xf bank_mask:0xf\n"
"s_nop 1\n"
"v_add_f32_dpp v120, v120, v120 row_ror:2 row_mask:0xf bank_mask:0xf\n"
"s_nop 1\n"
"v_add_f32_dpp v120, v120, v120 row_ror:1 row_mask:0xf bank_mask:0xf\n"
"v_mov_b32 v126, v120\n"
"s_nop 1\n"
"v_permlane16_swap_b32 v120, v126\n"
"v_add_f32 v120, v120, v126\n"
"buffer_load_dwordx4 v3, s[28:31], s4 offen offset:1024 lds\n"
"v_mov_b32 v126, 0x3727c5ac\n"
"v_fmac_f32 v126, 0x3ae38e39, v120\n"
"v_rsq_f32 v130, v126\n"
"v_mov_b32 v128, v124\n"
"v_mov_b32 v129, s68\n"
"v_mov_b32 v131, s69\n"
"v_lshlrev_b32 v156, 4, v156\n"
"v_min_u32 v126, 1, v159\n"
"v_lshl_or_b32 v156, v126, 30, v156\n"
"buffer_load_dwordx4 v4, s[28:31], s4 offen offset:2048 lds\n"
"s_mov_b32 s71, 81920\n"
"buffer_load_dwordx4 v5, s[28:31], s4 offen offset:3072 lds\n"
"s_add_u32 s4, s4, 128\n"
"s_add_u32 m0, s7, 36864\n"
"s_nop 0\n"
"buffer_load_dwordx4 v193, s[32:35], s5 offen offset:0 lds\n"
"buffer_load_dwordx4 v194, s[32:35], s5 offen offset:1024 lds\n"
"buffer_load_dwordx4 v195, s[32:35], s5 offen offset:2048 lds\n"
"buffer_load_dwordx4 v196, s[32:35], s5 offen offset:3072 lds\n"
"s_add_u32 m0, s7, 40960\n"
"s_nop 0\n"
"buffer_load_dwordx4 v197, s[32:35], s5 offen offset:0 lds\n"
"buffer_load_dwordx4 v198, s[32:35], s5 offen offset:1024 lds\n"
"buffer_load_dwordx4 v199, s[32:35], s5 offen offset:2048 lds\n"
"buffer_load_dwordx4 v200, s[32:35], s5 offen offset:3072 lds\n"
"s_add_u32 m0, s7, 45056\n"
"s_nop 0\n"
"buffer_load_dwordx4 v201, s[32:35], s5 offen offset:0 lds\n"
"s_add_u32 s5, s5, 589824\n"
"buffer_store_dwordx4 v[128:131], v156, s[36:39], s71 offen sc1\n"
"s_waitcnt vmcnt(14)\n"
"s_barrier\n"
"ds_read_b64_tr_b16 a[64:65], v184 offset:0\n"
"ds_read_b128 a[0:3], v176 offset:0\n"
"ds_read_b64_tr_b16 a[66:67], v184 offset:1024\n"
"ds_read_b128 a[4:7], v176 offset:2048\n"
"ds_read_b64_tr_b16 a[68:69], v185 offset:0\n"
"ds_read_b128 a[8:11], v177 offset:0\n"
"ds_read_b64_tr_b16 a[70:71], v185 offset:1024\n"
"ds_read_b128 a[12:15], v177 offset:2048\n"
"ds_read_b64_tr_b16 a[72:73], v186 offset:0\n"
"ds_read_b128 a[16:19], v178 offset:0\n"
"ds_read_b64_tr_b16 a[74:75], v186 offset:256\n"
"ds_read_b128 a[20:23], v178 offset:2048\n"
"ds_read_b64_tr_b16 a[76:77], v184 offset:9216\n"
"ds_read_b128 a[24:27], v179 offset:0\n"
"ds_read_b64_tr_b16 a[78:79], v184 offset:10240\n"
"ds_read_b128 a[28:31], v179 offset:2048\n"
"ds_read_b64_tr_b16 a[80:81], v185 offset:9216\n"
"ds_read_b64_tr_b16 a[82:83], v185 offset:10240\n"
"ds_read_b64_tr_b16 a[84:85], v186 offset:9216\n"
"ds_read_b64_tr_b16 a[86:87], v186 offset:9472\n"
"ds_read_b64_tr_b16 a[120:121], v164 offset:0\n"
"ds_read_b64_tr_b16 a[122:123], v165 offset:0\n"
"s_waitcnt lgkmcnt(0)\n"
"v_mfma_f32_16x16x32_bf16 v[8:11], a[64:67], a[0:3], 0\n"
"ds_read_b64_tr_b16 a[88:89], v184 offset:18432\n"
"v_mfma_f32_16x16x32_bf16 v[40:43], a[68:71], a[0:3], 0\n"
"ds_read_b128 a[32:35], v180 offset:0\n"
"v_mfma_f32_16x16x32_bf16 v[44:47], a[68:71], a[4:7], 0\n"
"ds_read_b64_tr_b16 a[90:91], v184 offset:19456\n"
"v_mfma_f32_16x16x32_bf16 v[12:15], a[64:67], a[4:7], 0\n"
"s_add_u32 m0, s6, 32768\n"
"s_nop 0\n"
"buffer_load_dwordx4 v2, s[28:31], s4 offen offset:0 lds\n"
"v_mfma_f32_16x16x32_bf16 v[16:19], a[64:67], a[8:11], 0\n"
"ds_read_b128 a[36:39], v180 offset:2048\n"
"v_mfma_f32_16x16x32_bf16 v[48:51], a[68:71], a[8:11], 0\n"
"ds_read_b64_tr_b16 a[92:93], v185 offset:18432\n"
"v_mfma_f32_16x16x32_bf16 v[52:55], a[68:71], a[12:15], 0\n"
"ds_read_b128 a[40:43], v181 offset:0\n"
"v_mfma_f32_16x16x32_bf16 v[20:23], a[64:67], a[12:15], 0\n"
"v_mfma_f32_16x16x32_bf16 v[24:27], a[64:67], a[16:19], 0\n"
"ds_read_b64_tr_b16 a[94:95], v185 offset:19456\n"
"v_mfma_f32_16x16x32_bf16 v[56:59], a[68:71], a[16:19], 0\n"
"ds_read_b128 a[44:47], v181 offset:2048\n"
"v_mfma_f32_16x16x32_bf16 v[60:63], a[68:71], a[20:23], 0\n"
"ds_read_b64_tr_b16 a[96:97], v186 offset:18432\n"
"v_mfma_f32_16x16x32_bf16 v[28:31], a[64:67], a[20:23], 0\n"
"buffer_load_dwordx4 v3, s[28:31], s4 offen offset:1024 lds\n"
"v_mfma_f32_16x16x32_bf16 v[32:35], a[64:67], a[24:27], 0\n"
"ds_read_b128 a[48:51], v182 offset:0\n"
"v_mfma_f32_16x16x32_bf16 v[64:67], a[68:71], a[24:27], 0\n"
"ds_read_b64_tr_b16 a[98:99], v186 offset:18688\n"
"v_mfma_f32_16x16x32_bf16 v[68:71], a[68:71], a[28:31], 0\n"
"ds_read_b128 a[52:55], v182 offset:2048\n"
"v_mfma_f32_16x16x32_bf16 v[36:39], a[64:67], a[28:31], 0\n"
"buffer_load_dwordx4 v4, s[28:31], s4 offen offset:2048 lds\n"
"v_mfma_f32_16x16x32_bf16 v[72:75], a[72:75], a[0:3], 0\n"
"ds_read_b64_tr_b16 a[100:101], v184 offset:27648\n"
"v_mfma_f32_16x16x32_bf16 v[76:79], a[72:75], a[4:7], 0\n"
"ds_read_b128 a[56:59], v183 offset:0\n"
"v_mfma_f32_16x16x32_bf16 v[204:207], a[120:123], a[112:115], 0\n"
"ds_read_b64_tr_b16 a[102:103], v184 offset:28672\n"
"v_mfma_f32_16x16x32_bf16 v[80:83], a[76:79], a[0:3], 0\n"
"v_mfma_f32_16x16x32_bf16 v[112:115], a[80:83], a[0:3], 0\n"
"ds_read_b128 a[60:63], v183 offset:2048\n"
"v_mfma_f32_16x16x32_bf16 v[116:119], a[80:83], a[4:7], 0\n"
"ds_read_b64_tr_b16 a[104:105], v185 offset:27648\n"
"v_mfma_f32_16x16x32_bf16 v[84:87], a[76:79], a[4:7], 0\n"
"ds_read_b64_tr_b16 a[106:107], v185 offset:28672\n"
"v_mfma_f32_16x16x32_bf16 v[88:91], a[76:79], a[8:11], 0\n"
"buffer_load_dwordx4 v5, s[28:31], s4 offen offset:3072 lds\n"
"s_add_u32 s4, s4, 128\n"
"v_mfma_f32_16x16x32_bf16 v[120:123], a[80:83], a[8:11], 0\n"
"ds_read_b64_tr_b16 a[108:109], v186 offset:27648\n"
"v_mfma_f32_16x16x32_bf16 v[124:127], a[80:83], a[12:15], 0\n"
"ds_read_b64_tr_b16 a[110:111], v186 offset:27904\n"
"v_mfma_f32_16x16x32_bf16 v[92:95], a[76:79], a[12:15], 0\n"
"ds_read_b64_tr_b16 a[124:125], v164 offset:18432\n"
"v_mfma_f32_16x16x32_bf16 v[96:99], a[76:79], a[16:19], 0\n"
"v_mfma_f32_16x16x32_bf16 v[128:131], a[80:83], a[16:19], 0\n"
"ds_read_b64_tr_b16 a[126:127], v165 offset:18432\n"
"v_mfma_f32_16x16x32_bf16 v[132:135], a[80:83], a[20:23], 0\n"
"s_add_u32 m0, s7, 73728\n"
"s_nop 0\n"
"buffer_load_dwordx4 v193, s[32:35], s5 offen offset:0 lds\n"
"v_mfma_f32_16x16x32_bf16 v[100:103], a[76:79], a[20:23], 0\n"
"v_mfma_f32_16x16x32_bf16 v[104:107], a[76:79], a[24:27], 0\n"
"v_mfma_f32_16x16x32_bf16 v[136:139], a[80:83], a[24:27], 0\n"
"v_mfma_f32_16x16x32_bf16 v[140:143], a[80:83], a[28:31], 0\n"
"buffer_load_dwordx4 v194, s[32:35], s5 offen offset:1024 lds\n"
"v_mfma_f32_16x16x32_bf16 v[108:111], a[76:79], a[28:31], 0\n"
"v_mfma_f32_16x16x32_bf16 v[144:147], a[84:87], a[0:3], 0\n"
"v_mfma_f32_16x16x32_bf16 v[148:151], a[84:87], a[4:7], 0\n"
"s_waitcnt vmcnt(7) lgkmcnt(0)\n"
"s_barrier\n"
"v_mfma_f32_16x16x32_bf16 v[8:11], a[88:91], a[32:35], v[8:11]\n"
"ds_read_b64_tr_b16 a[64:65], v187 offset:0\n"
"v_mfma_f32_16x16x32_bf16 v[40:43], a[92:95], a[32:35], v[40:43]\n"
"ds_read_b128 a[0:3], v176 offset:16384\n"
"v_mfma_f32_16x16x32_bf16 v[44:47], a[92:95], a[36:39], v[44:47]\n"
"ds_read_b64_tr_b16 a[66:67], v187 offset:1024\n"
"v_mfma_f32_16x16x32_bf16 v[12:15], a[88:91], a[36:39], v[12:15]\n"
"buffer_load_dwordx4 v195, s[32:35], s5 offen offset:2048 lds\n"
"v_mfma_f32_16x16x32_bf16 v[16:19], a[88:91], a[40:43], v[16:19]\n"
"ds_read_b128 a[4:7], v176 offset:18432\n"
"v_mfma_f32_16x16x32_bf16 v[48:51], a[92:95], a[40:43], v[48:51]\n"
"ds_read_b64_tr_b16 a[68:69], v188 offset:0\n"
"v_mfma_f32_16x16x32_bf16 v[52:55], a[92:95], a[44:47], v[52:55]\n"
"ds_read_b128 a[8:11], v177 offset:16384\n"
"v_mfma_f32_16x16x32_bf16 v[20:23], a[88:91], a[44:47], v[20:23]\n"
"buffer_load_dwordx4 v196, s[32:35], s5 offen offset:3072 lds\n"
"v_mfma_f32_16x16x32_bf16 v[24:27], a[88:91], a[48:51], v[24:27]\n"
"ds_read_b64_tr_b16 a[70:71], v188 offset:1024\n"
"v_mfma_f32_16x16x32_bf16 v[56:59], a[92:95], a[48:51], v[56:59]\n"
"ds_read_b128 a[12:15], v177 offset:18432\n"
"v_mfma_f32_16x16x32_bf16 v[60:63], a[92:95], a[52:55], v[60:63]\n"
"ds_read_b64_tr_b16 a[72:73], v189 offset:0\n"
"v_mfma_f32_16x16x32_bf16 v[28:31], a[88:91], a[52:55], v[28:31]\n"
"s_add_u32 m0, s7, 77824\n"
"s_nop 0\n"
"buffer_load_dwordx4 v197, s[32:35], s5 offen offset:0 lds\n"
"v_mfma_f32_16x16x32_bf16 v[32:35], a[88:91], a[56:59], v[32:35]\n"
"ds_read_b128 a[16:19], v178 offset:16384\n"
"v_mfma_f32_16x16x32_bf16 v[64:67], a[92:95], a[56:59], v[64:67]\n"
"ds_read_b64_tr_b16 a[74:75], v189 offset:256\n"
"v_mfma_f32_16x16x32_bf16 v[68:71], a[92:95], a[60:63], v[68:71]\n"
"ds_read_b128 a[20:23], v178 offset:18432\n"
"v_mfma_f32_16x16x32_bf16 v[36:39], a[88:91], a[60:63], v[36:39]\n"
"buffer_load_dwordx4 v198, s[32:35], s5 offen offset:1024 lds\n"
"v_mfma_f32_16x16x32_bf16 v[72:75], a[96:99], a[32:35], v[72:75]\n"
"ds_read_b64_tr_b16 a[76:77], v187 offset:9216\n"
"v_mfma_f32_16x16x32_bf16 v[76:79], a[96:99], a[36:39], v[76:79]\n"
"ds_read_b128 a[24:27], v179 offset:16384\n"
"v_mfma_f32_16x16x32_bf16 v[204:207], a[124:127], a[112:115], v[204:207]\n"
"ds_read_b64_tr_b16 a[78:79], v187 offset:10240\n"
"v_mfma_f32_16x16x32_bf16 v[80:83], a[100:103], a[32:35], v[80:83]\n"
"buffer_load_dwordx4 v199, s[32:35], s5 offen offset:2048 lds\n"
"v_mfma_f32_16x16x32_bf16 v[112:115], a[104:107], a[32:35], v[112:115]\n"
"ds_read_b128 a[28:31], v179 offset:18432\n"
"v_mfma_f32_16x16x32_bf16 v[116:119], a[104:107], a[36:39], v[116:119]\n"
"ds_read_b64_tr_b16 a[80:81], v188 offset:9216\n"
"v_mfma_f32_16x16x32_bf16 v[84:87], a[100:103], a[36:39], v[84:87]\n"
"ds_read_b64_tr_b16 a[82:83], v188 offset:10240\n"
"v_mfma_f32_16x16x32_bf16 v[88:91], a[100:103], a[40:43], v[88:91]\n"
"buffer_load_dwordx4 v200, s[32:35], s5 offen offset:3072 lds\n"
"v_mfma_f32_16x16x32_bf16 v[120:123], a[104:107], a[40:43], v[120:123]\n"
"ds_read_b64_tr_b16 a[84:85], v189 offset:9216\n"
"v_mfma_f32_16x16x32_bf16 v[124:127], a[104:107], a[44:47], v[124:127]\n"
"ds_read_b64_tr_b16 a[86:87], v189 offset:9472\n"
"v_mfma_f32_16x16x32_bf16 v[92:95], a[100:103], a[44:47], v[92:95]\n"
"ds_read_b64_tr_b16 a[120:121], v166 offset:0\n"
"v_mfma_f32_16x16x32_bf16 v[96:99], a[100:103], a[48:51], v[96:99]\n"
"v_mfma_f32_16x16x32_bf16 v[128:131], a[104:107], a[48:51], v[128:131]\n"
"ds_read_b64_tr_b16 a[122:123], v167 offset:0\n"
"v_mfma_f32_16x16x32_bf16 v[132:135], a[104:107], a[52:55], v[132:135]\n"
"s_add_u32 m0, s7, 81920\n"
"s_nop 0\n"
"buffer_load_dwordx4 v201, s[32:35], s5 offen offset:0 lds\n"
"s_add_u32 s5, s5, 589824\n"
"v_mfma_f32_16x16x32_bf16 v[100:103], a[100:103], a[52:55], v[100:103]\n"
"v_mfma_f32_16x16x32_bf16 v[104:107], a[100:103], a[56:59], v[104:107]\n"
"v_mfma_f32_16x16x32_bf16 v[136:139], a[104:107], a[56:59], v[136:139]\n"
"v_mfma_f32_16x16x32_bf16 v[140:143], a[104:107], a[60:63], v[140:143]\n"
"s_add_u32 m0, s6, 0\n"
"s_nop 0\n"
"buffer_load_dwordx4 v2, s[28:31], s4 offen offset:0 lds\n"
"v_mfma_f32_16x16x32_bf16 v[108:111], a[100:103], a[60:63], v[108:111]\n"
"v_mfma_f32_16x16x32_bf16 v[144:147], a[108:111], a[32:35], v[144:147]\n"
"v_mfma_f32_16x16x32_bf16 v[148:151], a[108:111], a[36:39], v[148:151]\n"
"s_waitcnt lgkmcnt(0)\n"
"v_mfma_f32_16x16x32_bf16 v[8:11], a[64:67], a[0:3], v[8:11]\n"
"ds_read_b64_tr_b16 a[88:89], v187 offset:18432\n"
"v_mfma_f32_16x16x32_bf16 v[40:43], a[68:71], a[0:3], v[40:43]\n"
"ds_read_b128 a[32:35], v180 offset:16384\n"
"v_mfma_f32_16x16x32_bf16 v[44:47], a[68:71], a[4:7], v[44:47]\n"
"ds_read_b64_tr_b16 a[90:91], v187 offset:19456\n"
"v_mfma_f32_16x16x32_bf16 v[12:15], a[64:67], a[4:7], v[12:15]\n"
"buffer_load_dwordx4 v3, s[28:31], s4 offen offset:1024 lds\n"
"v_mfma_f32_16x16x32_bf16 v[16:19], a[64:67], a[8:11], v[16:19]\n"
"ds_read_b128 a[36:39], v180 offset:18432\n"
"v_mfma_f32_16x16x32_bf16 v[48:51], a[68:71], a[8:11], v[48:51]\n"
"ds_read_b64_tr_b16 a[92:93], v188 offset:18432\n"
"v_mfma_f32_16x16x32_bf16 v[52:55], a[68:71], a[12:15], v[52:55]\n"
"ds_read_b128 a[40:43], v181 offset:16384\n"
"v_mfma_f32_16x16x32_bf16 v[20:23], a[64:67], a[12:15], v[20:23]\n"
"v_mfma_f32_16x16x32_bf16 v[24:27], a[64:67], a[16:19], v[24:27]\n"
"ds_read_b64_tr_b16 a[94:95], v188 offset:19456\n"
"v_mfma_f32_16x16x32_bf16 v[56:59], a[68:71], a[16:19], v[56:59]\n"
"ds_read_b128 a[44:47], v181 offset:18432\n"
"v_mfma_f32_16x16x32_bf16 v[60:63], a[68:71], a[20:23], v[60:63]\n"
"ds_read_b64_tr_b16 a[96:97], v189 offset:18432\n"
"v_mfma_f32_16x16x32_bf16 v[28:31], a[64:67], a[20:23], v[28:31]\n"
"buffer_load_dwordx4 v4, s[28:31], s4 offen offset:2048 lds\n"
"v_mfma_f32_16x16x32_bf16 v[32:35], a[64:67], a[24:27], v[32:35]\n"
"ds_read_b128 a[48:51], v182 offset:16384\n"
"v_mfma_f32_16x16x32_bf16 v[64:67], a[68:71], a[24:27], v[64:67]\n"
"ds_read_b64_tr_b16 a[98:99], v189 offset:18688\n"
"v_mfma_f32_16x16x32_bf16 v[68:71], a[68:71], a[28:31], v[68:71]\n"
"ds_read_b128 a[52:55], v182 offset:18432\n"
"v_mfma_f32_16x16x32_bf16 v[36:39], a[64:67], a[28:31], v[36:39]\n"
"buffer_load_dwordx4 v5, s[28:31], s4 offen offset:3072 lds\n"
"s_add_u32 s4, s4, 128\n"
"v_mfma_f32_16x16x32_bf16 v[72:75], a[72:75], a[0:3], v[72:75]\n"
"ds_read_b64_tr_b16 a[100:101], v187 offset:27648\n"
"v_mfma_f32_16x16x32_bf16 v[76:79], a[72:75], a[4:7], v[76:79]\n"
"ds_read_b128 a[56:59], v183 offset:16384\n"
"v_mfma_f32_16x16x32_bf16 v[204:207], a[120:123], a[112:115], v[204:207]\n"
"ds_read_b64_tr_b16 a[102:103], v187 offset:28672\n"
"v_mfma_f32_16x16x32_bf16 v[80:83], a[76:79], a[0:3], v[80:83]\n"
"v_mfma_f32_16x16x32_bf16 v[112:115], a[80:83], a[0:3], v[112:115]\n"
"ds_read_b128 a[60:63], v183 offset:18432\n"
"v_mfma_f32_16x16x32_bf16 v[116:119], a[80:83], a[4:7], v[116:119]\n"
"ds_read_b64_tr_b16 a[104:105], v188 offset:27648\n"
"v_mfma_f32_16x16x32_bf16 v[84:87], a[76:79], a[4:7], v[84:87]\n"
"ds_read_b64_tr_b16 a[106:107], v188 offset:28672\n"
"v_mfma_f32_16x16x32_bf16 v[88:91], a[76:79], a[8:11], v[88:91]\n"
"s_add_u32 m0, s7, 0\n"
"s_nop 0\n"
"buffer_load_dwordx4 v193, s[32:35], s5 offen offset:0 lds\n"
"v_mfma_f32_16x16x32_bf16 v[120:123], a[80:83], a[8:11], v[120:123]\n"
"ds_read_b64_tr_b16 a[108:109], v189 offset:27648\n"
"v_mfma_f32_16x16x32_bf16 v[124:127], a[80:83], a[12:15], v[124:127]\n"
"ds_read_b64_tr_b16 a[110:111], v189 offset:27904\n"
"v_mfma_f32_16x16x32_bf16 v[92:95], a[76:79], a[12:15], v[92:95]\n"
"ds_read_b64_tr_b16 a[124:125], v166 offset:18432\n"
"v_mfma_f32_16x16x32_bf16 v[96:99], a[76:79], a[16:19], v[96:99]\n"
"v_mfma_f32_16x16x32_bf16 v[128:131], a[80:83], a[16:19], v[128:131]\n"
"ds_read_b64_tr_b16 a[126:127], v167 offset:18432\n"
"v_mfma_f32_16x16x32_bf16 v[132:135], a[80:83], a[20:23], v[132:135]\n"
"buffer_load_dwordx4 v194, s[32:35], s5 offen offset:1024 lds\n"
"v_mfma_f32_16x16x32_bf16 v[100:103], a[76:79], a[20:23], v[100:103]\n"
"v_mfma_f32_16x16x32_bf16 v[104:107], a[76:79], a[24:27], v[104:107]\n"
"v_mfma_f32_16x16x32_bf16 v[136:139], a[80:83], a[24:27], v[136:139]\n"
"v_mfma_f32_16x16x32_bf16 v[140:143], a[80:83], a[28:31], v[140:143]\n"
"buffer_load_dwordx4 v195, s[32:35], s5 offen offset:2048 lds\n"
"v_mfma_f32_16x16x32_bf16 v[108:111], a[76:79], a[28:31], v[108:111]\n"
"v_mfma_f32_16x16x32_bf16 v[144:147], a[84:87], a[0:3], v[144:147]\n"
"v_mfma_f32_16x16x32_bf16 v[148:151], a[84:87], a[4:7], v[148:151]\n"
"s_waitcnt vmcnt(7) lgkmcnt(0)\n"
"s_barrier\n"
"v_mfma_f32_16x16x32_bf16 v[8:11], a[88:91], a[32:35], v[8:11]\n"
"ds_read_b64_tr_b16 a[64:65], v190 offset:0\n"
"s_lshl_b32 s71, s65, 8\n"
"v_mfma_f32_16x16x32_bf16 v[40:43], a[92:95], a[32:35], v[40:43]\n"
"ds_read_b128 a[0:3], v176 offset:32768\n"
"s_add_u32 s71, s71, s64\n"
"v_mfma_f32_16x16x32_bf16 v[44:47], a[92:95], a[36:39], v[44:47]\n"
"ds_read_b64_tr_b16 a[66:67], v190 offset:1024\n"
"v_mfma_f32_16x16x32_bf16 v[12:15], a[88:91], a[36:39], v[12:15]\n"
"buffer_load_dwordx4 v196, s[32:35], s5 offen offset:3072 lds\n"
"v_mfma_f32_16x16x32_bf16 v[16:19], a[88:91], a[40:43], v[16:19]\n"
"ds_read_b128 a[4:7], v176 offset:34816\n"
"v_mfma_f32_16x16x32_bf16 v[48:51], a[92:95], a[40:43], v[48:51]\n"
"ds_read_b64_tr_b16 a[68:69], v191 offset:0\n"
"v_mfma_f32_16x16x32_bf16 v[52:55], a[92:95], a[44:47], v[52:55]\n"
"ds_read_b128 a[8:11], v177 offset:32768\n"
"v_mfma_f32_16x16x32_bf16 v[20:23], a[88:91], a[44:47], v[20:23]\n"
"s_add_u32 m0, s7, 4096\n"
"s_nop 0\n"
"buffer_load_dwordx4 v197, s[32:35], s5 offen offset:0 lds\n"
"v_mfma_f32_16x16x32_bf16 v[24:27], a[88:91], a[48:51], v[24:27]\n"
"ds_read_b64_tr_b16 a[70:71], v191 offset:1024\n"
"v_mfma_f32_16x16x32_bf16 v[56:59], a[92:95], a[48:51], v[56:59]\n"
"ds_read_b128 a[12:15], v177 offset:34816\n"
"v_mfma_f32_16x16x32_bf16 v[60:63], a[92:95], a[52:55], v[60:63]\n"
"ds_read_b64_tr_b16 a[72:73], v192 offset:0\n"
"v_mfma_f32_16x16x32_bf16 v[28:31], a[88:91], a[52:55], v[28:31]\n"
"s_cmp_lt_u32 s71, 576\n"
"s_cselect_b32 s13, 0, 0x40000000\n"
"s_cselect_b32 s71, s71, 0\n"
"v_mfma_f32_16x16x32_bf16 v[32:35], a[88:91], a[56:59], v[32:35]\n"
"ds_read_b128 a[16:19], v178 offset:32768\n"
"s_lshr_b32 s72, s71, 3\n"
"v_mfma_f32_16x16x32_bf16 v[64:67], a[92:95], a[56:59], v[64:67]\n"
"ds_read_b64_tr_b16 a[74:75], v192 offset:256\n"
"s_and_b32 s73, s71, 7\n"
"v_mfma_f32_16x16x32_bf16 v[68:71], a[92:95], a[60:63], v[68:71]\n"
"ds_read_b128 a[20:23], v178 offset:34816\n"
"s_mul_i32 s74, s72, 7282\n"
"v_mfma_f32_16x16x32_bf16 v[36:39], a[88:91], a[60:63], v[36:39]\n"
"buffer_load_dwordx4 v198, s[32:35], s5 offen offset:1024 lds\n"
"s_lshr_b32 s74, s74, 16\n"
"v_mfma_f32_16x16x32_bf16 v[72:75], a[96:99], a[32:35], v[72:75]\n"
"ds_read_b64_tr_b16 a[76:77], v190 offset:9216\n"
"s_mul_i32 s75, s74, 9\n"
"v_mfma_f32_16x16x32_bf16 v[76:79], a[96:99], a[36:39], v[76:79]\n"
"ds_read_b128 a[24:27], v179 offset:32768\n"
"s_sub_u32 s75, s72, s75\n"
"v_mfma_f32_16x16x32_bf16 v[204:207], a[124:127], a[112:115], v[204:207]\n"
"ds_read_b64_tr_b16 a[78:79], v190 offset:10240\n"
"s_lshl_b32 s76, s74, 8\n"
"v_mfma_f32_16x16x32_bf16 v[80:83], a[100:103], a[32:35], v[80:83]\n"
"buffer_load_dwordx4 v199, s[32:35], s5 offen offset:2048 lds\n"
"s_lshl_b32 s77, s75, 5\n"
"v_mfma_f32_16x16x32_bf16 v[112:115], a[104:107], a[32:35], v[112:115]\n"
"ds_read_b128 a[28:31], v179 offset:34816\n"
"s_add_u32 s76, s76, s77\n"
"v_mfma_f32_16x16x32_bf16 v[116:119], a[104:107], a[36:39], v[116:119]\n"
"ds_read_b64_tr_b16 a[80:81], v191 offset:9216\n"
"s_lshl_b32 s77, s74, 5\n"
"v_mfma_f32_16x16x32_bf16 v[84:87], a[100:103], a[36:39], v[84:87]\n"
"ds_read_b64_tr_b16 a[82:83], v191 offset:10240\n"
"s_add_u32 s77, s77, 2048\n"
"v_mfma_f32_16x16x32_bf16 v[88:91], a[100:103], a[40:43], v[88:91]\n"
"buffer_load_dwordx4 v200, s[32:35], s5 offen offset:3072 lds\n"
"v_mfma_f32_16x16x32_bf16 v[120:123], a[104:107], a[40:43], v[120:123]\n"
"ds_read_b64_tr_b16 a[84:85], v192 offset:9216\n"
"v_mfma_f32_16x16x32_bf16 v[124:127], a[104:107], a[44:47], v[124:127]\n"
"ds_read_b64_tr_b16 a[86:87], v192 offset:9472\n"
"v_mfma_f32_16x16x32_bf16 v[92:95], a[100:103], a[44:47], v[92:95]\n"
"ds_read_b64_tr_b16 a[120:121], v168 offset:0\n"
"v_mfma_f32_16x16x32_bf16 v[96:99], a[100:103], a[48:51], v[96:99]\n"
"s_cmp_eq_u32 s75, 8\n"
"s_cselect_b32 s74, s77, s76\n"
"v_mfma_f32_16x16x32_bf16 v[128:131], a[104:107], a[48:51], v[128:131]\n"
"ds_read_b64_tr_b16 a[122:123], v169 offset:0\n"
"s_mul_i32 s74, s74, 1152\n"
"v_mfma_f32_16x16x32_bf16 v[132:135], a[104:107], a[52:55], v[132:135]\n"
"s_add_u32 m0, s7, 8192\n"
"s_nop 0\n"
"buffer_load_dwordx4 v201, s[32:35], s5 offen offset:0 lds\n"
"s_add_u32 s5, s5, 589824\n"
"v_mfma_f32_16x16x32_bf16 v[100:103], a[100:103], a[52:55], v[100:103]\n"
"s_mul_i32 s76, s73, 144\n"
"s_add_u32 s78, s74, s76\n"
"v_mfma_f32_16x16x32_bf16 v[104:107], a[100:103], a[56:59], v[104:107]\n"
"s_add_u32 s79, s78, 18432\n"
"s_mul_i32 s76, s73, 36\n"
"v_mfma_f32_16x16x32_bf16 v[136:139], a[104:107], a[56:59], v[136:139]\n"
"s_lshr_b32 s77, s72, 1\n"
"s_add_u32 s76, s76, s77\n"
"v_mfma_f32_16x16x32_bf16 v[140:143], a[104:107], a[60:63], v[140:143]\n"
"s_add_u32 m0, s6, 16384\n"
"s_nop 0\n"
"buffer_load_dwordx4 v2, s[28:31], s4 offen offset:0 lds\n"
"v_mfma_f32_16x16x32_bf16 v[108:111], a[100:103], a[60:63], v[108:111]\n"
"s_mul_i32 s76, s76, 9216\n"
"s_add_u32 s76, s76, 7340032\n"
"v_mfma_f32_16x16x32_bf16 v[144:147], a[108:111], a[32:35], v[144:147]\n"
"s_and_b32 s77, s72, 1\n"
"s_mul_i32 s74, s77, 5120\n"
"v_mfma_f32_16x16x32_bf16 v[148:151], a[108:111], a[36:39], v[148:151]\n"
"s_add_u32 s19, s76, s74\n"
"s_lshl_b32 s77, s77, 7\n"
"s_waitcnt lgkmcnt(0)\n"
"v_mfma_f32_16x16x32_bf16 v[8:11], a[64:67], a[0:3], v[8:11]\n"
"ds_read_b64_tr_b16 a[88:89], v190 offset:18432\n"
"s_add_u32 s76, s76, s77\n"
"v_mfma_f32_16x16x32_bf16 v[40:43], a[68:71], a[0:3], v[40:43]\n"
"ds_read_b128 a[32:35], v180 offset:32768\n"
"s_add_u32 s9, s76, 4096\n"
"v_mfma_f32_16x16x32_bf16 v[44:47], a[68:71], a[4:7], v[44:47]\n"
"ds_read_b64_tr_b16 a[90:91], v190 offset:19456\n"
"s_lshl_b32 s73, s73, 7\n"
"v_mfma_f32_16x16x32_bf16 v[12:15], a[64:67], a[4:7], v[12:15]\n"
"buffer_load_dwordx4 v3, s[28:31], s4 offen offset:1024 lds\n"
"s_add_u32 s73, s73, s72\n"
"v_mfma_f32_16x16x32_bf16 v[16:19], a[64:67], a[8:11], v[16:19]\n"
"ds_read_b128 a[36:39], v180 offset:34816\n"
"s_lshl_b32 s73, s73, 3\n"
"v_mfma_f32_16x16x32_bf16 v[48:51], a[68:71], a[8:11], v[48:51]\n"
"ds_read_b64_tr_b16 a[92:93], v191 offset:18432\n"
"s_add_u32 s22, s73, 40960\n"
"v_mfma_f32_16x16x32_bf16 v[52:55], a[68:71], a[12:15], v[52:55]\n"
"ds_read_b128 a[40:43], v181 offset:32768\n"
"s_mov_b32 s96, s64\n"
"v_mfma_f32_16x16x32_bf16 v[20:23], a[64:67], a[12:15], v[20:23]\n"
"s_add_u32 s97, s64, 256\n"
"s_lshr_b32 s98, s64, 2\n"
"v_mfma_f32_16x16x32_bf16 v[24:27], a[64:67], a[16:19], v[24:27]\n"
"ds_read_b64_tr_b16 a[94:95], v191 offset:19456\n"
"s_add_u32 s98, s98, 512\n"
"v_mfma_f32_16x16x32_bf16 v[56:59], a[68:71], a[16:19], v[56:59]\n"
"ds_read_b128 a[44:47], v181 offset:34816\n"
"v_mfma_f32_16x16x32_bf16 v[60:63], a[68:71], a[20:23], v[60:63]\n"
"ds_read_b64_tr_b16 a[96:97], v192 offset:18432\n"
"v_mfma_f32_16x16x32_bf16 v[28:31], a[64:67], a[20:23], v[28:31]\n"
"buffer_load_dwordx4 v4, s[28:31], s4 offen offset:2048 lds\n"
"v_mfma_f32_16x16x32_bf16 v[32:35], a[64:67], a[24:27], v[32:35]\n"
"ds_read_b128 a[48:51], v182 offset:32768\n"
"v_mfma_f32_16x16x32_bf16 v[64:67], a[68:71], a[24:27], v[64:67]\n"
"ds_read_b64_tr_b16 a[98:99], v192 offset:18688\n"
"v_mfma_f32_16x16x32_bf16 v[68:71], a[68:71], a[28:31], v[68:71]\n"
"ds_read_b128 a[52:55], v182 offset:34816\n"
"v_mfma_f32_16x16x32_bf16 v[36:39], a[64:67], a[28:31], v[36:39]\n"
"buffer_load_dwordx4 v5, s[28:31], s4 offen offset:3072 lds\n"
"s_add_u32 s4, s4, 128\n"
"v_mfma_f32_16x16x32_bf16 v[72:75], a[72:75], a[0:3], v[72:75]\n"
"ds_read_b64_tr_b16 a[100:101], v190 offset:27648\n"
"v_mfma_f32_16x16x32_bf16 v[76:79], a[72:75], a[4:7], v[76:79]\n"
"ds_read_b128 a[56:59], v183 offset:32768\n"
"v_mfma_f32_16x16x32_bf16 v[204:207], a[120:123], a[112:115], v[204:207]\n"
"ds_read_b64_tr_b16 a[102:103], v190 offset:28672\n"
"v_mfma_f32_16x16x32_bf16 v[80:83], a[76:79], a[0:3], v[80:83]\n"
"v_mfma_f32_16x16x32_bf16 v[112:115], a[80:83], a[0:3], v[112:115]\n"
"ds_read_b128 a[60:63], v183 offset:34816\n"
"v_mfma_f32_16x16x32_bf16 v[116:119], a[80:83], a[4:7], v[116:119]\n"
"ds_read_b64_tr_b16 a[104:105], v191 offset:27648\n"
"v_mfma_f32_16x16x32_bf16 v[84:87], a[76:79], a[4:7], v[84:87]\n"
"ds_read_b64_tr_b16 a[106:107], v191 offset:28672\n"
"v_mfma_f32_16x16x32_bf16 v[88:91], a[76:79], a[8:11], v[88:91]\n"
"s_add_u32 m0, s7, 36864\n"
"s_nop 0\n"
"buffer_load_dwordx4 v193, s[32:35], s5 offen offset:0 lds\n"
"v_mfma_f32_16x16x32_bf16 v[120:123], a[80:83], a[8:11], v[120:123]\n"
"ds_read_b64_tr_b16 a[108:109], v192 offset:27648\n"
"v_mfma_f32_16x16x32_bf16 v[124:127], a[80:83], a[12:15], v[124:127]\n"
"ds_read_b64_tr_b16 a[110:111], v192 offset:27904\n"
"v_mfma_f32_16x16x32_bf16 v[92:95], a[76:79], a[12:15], v[92:95]\n"
"ds_read_b64_tr_b16 a[124:125], v168 offset:18432\n"
"v_mfma_f32_16x16x32_bf16 v[96:99], a[76:79], a[16:19], v[96:99]\n"
"v_mfma_f32_16x16x32_bf16 v[128:131], a[80:83], a[16:19], v[128:131]\n"
"ds_read_b64_tr_b16 a[126:127], v169 offset:18432\n"
"v_mfma_f32_16x16x32_bf16 v[132:135], a[80:83], a[20:23], v[132:135]\n"
"buffer_load_dwordx4 v194, s[32:35], s5 offen offset:1024 lds\n"
"v_mfma_f32_16x16x32_bf16 v[100:103], a[76:79], a[20:23], v[100:103]\n"
"v_mfma_f32_16x16x32_bf16 v[104:107], a[76:79], a[24:27], v[104:107]\n"
"v_mfma_f32_16x16x32_bf16 v[136:139], a[80:83], a[24:27], v[136:139]\n"
"v_mfma_f32_16x16x32_bf16 v[140:143], a[80:83], a[28:31], v[140:143]\n"
"buffer_load_dwordx4 v195, s[32:35], s5 offen offset:2048 lds\n"
"v_mfma_f32_16x16x32_bf16 v[108:111], a[76:79], a[28:31], v[108:111]\n"
"v_mfma_f32_16x16x32_bf16 v[144:147], a[84:87], a[0:3], v[144:147]\n"
"v_mfma_f32_16x16x32_bf16 v[148:151], a[84:87], a[4:7], v[148:151]\n"
"s_waitcnt vmcnt(7) lgkmcnt(0)\n"
"s_barrier\n"
"v_mfma_f32_16x16x32_bf16 v[8:11], a[88:91], a[32:35], v[8:11]\n"
"ds_read_b64_tr_b16 a[64:65], v184 offset:0\n"
"v_and_b32 v242, 15, v1\n"
"v_mfma_f32_16x16x32_bf16 v[40:43], a[92:95], a[32:35], v[40:43]\n"
"ds_read_b128 a[0:3], v176 offset:0\n"
"v_lshrrev_b32 v240, 4, v1\n"
"v_mfma_f32_16x16x32_bf16 v[44:47], a[92:95], a[36:39], v[44:47]\n"
"ds_read_b64_tr_b16 a[66:67], v184 offset:1024\n"
"v_lshlrev_b32 v240, 5, v240\n"
"v_mfma_f32_16x16x32_bf16 v[12:15], a[88:91], a[36:39], v[12:15]\n"
"buffer_load_dwordx4 v196, s[32:35], s5 offen offset:3072 lds\n"
"v_and_b32 v241, 1, v1\n"
"v_mfma_f32_16x16x32_bf16 v[16:19], a[88:91], a[40:43], v[16:19]\n"
"ds_read_b128 a[4:7], v176 offset:2048\n"
"v_lshl_add_u32 v240, v241, 4, v240\n"
"v_mfma_f32_16x16x32_bf16 v[48:51], a[92:95], a[40:43], v[48:51]\n"
"ds_read_b64_tr_b16 a[68:69], v185 offset:0\n"
"v_lshrrev_b32 v242, 1, v242\n"
"v_mfma_f32_16x16x32_bf16 v[52:55], a[92:95], a[44:47], v[52:55]\n"
"ds_read_b128 a[8:11], v177 offset:0\n"
"v_min_u32 v241, 1, v242\n"
"v_mfma_f32_16x16x32_bf16 v[20:23], a[88:91], a[44:47], v[20:23]\n"
"s_add_u32 m0, s7, 40960\n"
"s_nop 0\n"
"buffer_load_dwordx4 v197, s[32:35], s5 offen offset:0 lds\n"
"v_mfma_f32_16x16x32_bf16 v[24:27], a[88:91], a[48:51], v[24:27]\n"
"ds_read_b64_tr_b16 a[70:71], v185 offset:1024\n"
"v_lshl_or_b32 v250, v241, 30, v240\n"
"v_mfma_f32_16x16x32_bf16 v[56:59], a[92:95], a[48:51], v[56:59]\n"
"ds_read_b128 a[12:15], v177 offset:2048\n"
"s_lshl_b32 s71, s65, 4\n"
"v_mfma_f32_16x16x32_bf16 v[60:63], a[92:95], a[52:55], v[60:63]\n"
"ds_read_b64_tr_b16 a[72:73], v186 offset:0\n"
"s_add_u32 s71, s71, s66\n"
"v_mfma_f32_16x16x32_bf16 v[28:31], a[88:91], a[52:55], v[28:31]\n"
"s_cmp_lt_u32 s71, 18\n"
"s_cselect_b32 s72, 0, 0x40000000\n"
"v_mfma_f32_16x16x32_bf16 v[32:35], a[88:91], a[56:59], v[32:35]\n"
"ds_read_b128 a[16:19], v178 offset:0\n"
"s_lshl_b32 s71, s71, 7\n"
"v_mfma_f32_16x16x32_bf16 v[64:67], a[92:95], a[56:59], v[64:67]\n"
"ds_read_b64_tr_b16 a[74:75], v186 offset:256\n"
"s_or_b32 s71, s71, s72\n"
"v_mfma_f32_16x16x32_bf16 v[68:71], a[92:95], a[60:63], v[68:71]\n"
"ds_read_b128 a[20:23], v178 offset:2048\n"
"v_or_b32 v250, s71, v250\n"
"v_mfma_f32_16x16x32_bf16 v[36:39], a[88:91], a[60:63], v[36:39]\n"
"buffer_load_dwordx4 v198, s[32:35], s5 offen offset:1024 lds\n"
"v_min_u32 v242, 5, v242\n"
"v_mfma_f32_16x16x32_bf16 v[72:75], a[96:99], a[32:35], v[72:75]\n"
"ds_read_b64_tr_b16 a[76:77], v184 offset:9216\n"
"s_lshl_b32 s71, s65, 1\n"
"v_mfma_f32_16x16x32_bf16 v[76:79], a[96:99], a[36:39], v[76:79]\n"
"ds_read_b128 a[24:27], v179 offset:0\n"
"v_mfma_f32_16x16x32_bf16 v[204:207], a[124:127], a[112:115], v[204:207]\n"
"ds_read_b64_tr_b16 a[78:79], v184 offset:10240\n"
"v_mfma_f32_16x16x32_bf16 v[80:83], a[100:103], a[32:35], v[80:83]\n"
"buffer_load_dwordx4 v199, s[32:35], s5 offen offset:2048 lds\n"
"v_mfma_f32_16x16x32_bf16 v[112:115], a[104:107], a[32:35], v[112:115]\n"
"ds_read_b128 a[28:31], v179 offset:2048\n"
"v_mfma_f32_16x16x32_bf16 v[116:119], a[104:107], a[36:39], v[116:119]\n"
"ds_read_b64_tr_b16 a[80:81], v185 offset:9216\n"
"v_mfma_f32_16x16x32_bf16 v[84:87], a[100:103], a[36:39], v[84:87]\n"
"ds_read_b64_tr_b16 a[82:83], v185 offset:10240\n"
"v_mfma_f32_16x16x32_bf16 v[88:91], a[100:103], a[40:43], v[88:91]\n"
"buffer_load_dwordx4 v200, s[32:35], s5 offen offset:3072 lds\n"
"v_mfma_f32_16x16x32_bf16 v[120:123], a[104:107], a[40:43], v[120:123]\n"
"ds_read_b64_tr_b16 a[84:85], v186 offset:9216\n"
"v_mfma_f32_16x16x32_bf16 v[124:127], a[104:107], a[44:47], v[124:127]\n"
"ds_read_b64_tr_b16 a[86:87], v186 offset:9472\n"
"v_mfma_f32_16x16x32_bf16 v[92:95], a[100:103], a[44:47], v[92:95]\n"
"ds_read_b64_tr_b16 a[120:121], v164 offset:0\n"
"v_mfma_f32_16x16x32_bf16 v[96:99], a[100:103], a[48:51], v[96:99]\n"
"v_cmp_lt_u32 vcc, 2, v242\n"
"v_mov_b32 v241, 3\n"
"s_nop 0\n"
"v_cndmask_b32 v241, 0, v241, vcc\n"
"v_mfma_f32_16x16x32_bf16 v[128:131], a[104:107], a[48:51], v[128:131]\n"
"ds_read_b64_tr_b16 a[122:123], v165 offset:0\n"
"v_sub_u32 v242, v242, v241\n"
"v_mfma_f32_16x16x32_bf16 v[132:135], a[104:107], a[52:55], v[132:135]\n"
"s_add_u32 m0, s7, 45056\n"
"s_nop 0\n"
"buffer_load_dwordx4 v201, s[32:35], s5 offen offset:0 lds\n"
"s_add_u32 s5, s5, 589824\n"
"v_mfma_f32_16x16x32_bf16 v[100:103], a[100:103], a[52:55], v[100:103]\n"
"v_mul_u32_u24 v241, 3, v241\n"
"v_mfma_f32_16x16x32_bf16 v[104:107], a[100:103], a[56:59], v[104:107]\n"
"v_cmp_eq_u32 vcc, 2, v242\n"
"v_add_u32 v242, s71, v242\n"
"s_nop 0\n"
"v_cndmask_b32_e64 v242, v242, 8, vcc\n"
"v_mfma_f32_16x16x32_bf16 v[136:139], a[104:107], a[56:59], v[136:139]\n"
"v_add_u32 v242, v242, v241\n"
"v_mfma_f32_16x16x32_bf16 v[140:143], a[104:107], a[60:63], v[140:143]\n"
"s_add_u32 m0, s6, 32768\n"
"s_nop 0\n"
"buffer_load_dwordx4 v2, s[28:31], s4 offen offset:0 lds\n"
"v_mfma_f32_16x16x32_bf16 v[108:111], a[100:103], a[60:63], v[108:111]\n"
"v_lshl_add_u32 v251, v242, 7, v240\n"
"s_mul_i32 s17, s67, 2304\n"
"v_mfma_f32_16x16x32_bf16 v[144:147], a[108:111], a[32:35], v[144:147]\n"
"s_add_u32 s17, s17, 3538944\n"
"v_min_u32 v240, 251, v0\n"
"v_mfma_f32_16x16x32_bf16 v[148:151], a[108:111], a[36:39], v[148:151]\n"
"v_mul_u32_u24 v241, 1821, v240\n"
"s_waitcnt lgkmcnt(0)\n"
"v_mfma_f32_16x16x32_bf16 v[8:11], a[64:67], a[0:3], v[8:11]\n"
"ds_read_b64_tr_b16 a[88:89], v184 offset:18432\n"
"v_lshrrev_b32 v241, 16, v241\n"
"v_mfma_f32_16x16x32_bf16 v[40:43], a[68:71], a[0:3], v[40:43]\n"
"ds_read_b128 a[32:35], v180 offset:0\n"
"v_mul_u32_u24 v242, 36, v241\n"
"v_mfma_f32_16x16x32_bf16 v[44:47], a[68:71], a[4:7], v[44:47]\n"
"ds_read_b64_tr_b16 a[90:91], v184 offset:19456\n"
"v_sub_u32 v240, v240, v242\n"
"v_mfma_f32_16x16x32_bf16 v[12:15], a[64:67], a[4:7], v[12:15]\n"
"buffer_load_dwordx4 v3, s[28:31], s4 offen offset:1024 lds\n"
"v_mul_u32_u24 v241, 10, v241\n"
"v_mfma_f32_16x16x32_bf16 v[16:19], a[64:67], a[8:11], v[16:19]\n"
"ds_read_b128 a[36:39], v180 offset:2048\n"
"v_min_u32 v241, 54, v241\n"
"v_mfma_f32_16x16x32_bf16 v[48:51], a[68:71], a[8:11], v[48:51]\n"
"ds_read_b64_tr_b16 a[92:93], v185 offset:18432\n"
"s_and_b32 s76, s64, 7\n"
"v_mfma_f32_16x16x32_bf16 v[52:55], a[68:71], a[12:15], v[52:55]\n"
"ds_read_b128 a[40:43], v181 offset:0\n"
"s_mul_i32 s76, s76, 36\n"
"v_mfma_f32_16x16x32_bf16 v[20:23], a[64:67], a[12:15], v[20:23]\n"
"v_add_u32 v242, s76, v240\n"
"v_mfma_f32_16x16x32_bf16 v[24:27], a[64:67], a[16:19], v[24:27]\n"
"ds_read_b64_tr_b16 a[94:95], v185 offset:19456\n"
"v_lshlrev_b32 v242, 4, v242\n"
"v_mfma_f32_16x16x32_bf16 v[56:59], a[68:71], a[16:19], v[56:59]\n"
"ds_read_b128 a[44:47], v181 offset:2048\n"
"global_load_dwordx4 a[116:119], v242, s[10:11]\n"
"v_mfma_f32_16x16x32_bf16 v[60:63], a[68:71], a[20:23], v[60:63]\n"
"ds_read_b64_tr_b16 a[96:97], v186 offset:18432\n"
"v_mul_u32_u24 v252, 144, v241\n"
"v_mfma_f32_16x16x32_bf16 v[28:31], a[64:67], a[20:23], v[28:31]\n"
"buffer_load_dwordx4 v4, s[28:31], s4 offen offset:2048 lds\n"
"v_lshl_add_u32 v252, v240, 2, v252\n"
"v_mfma_f32_16x16x32_bf16 v[32:35], a[64:67], a[24:27], v[32:35]\n"
"ds_read_b128 a[48:51], v182 offset:0\n"
"v_mul_u32_u24 v253, 264, v240\n"
"v_mfma_f32_16x16x32_bf16 v[64:67], a[68:71], a[24:27], v[64:67]\n"
"ds_read_b64_tr_b16 a[98:99], v186 offset:18688\n"
"v_lshlrev_b32 v241, 1, v241\n"
"v_mfma_f32_16x16x32_bf16 v[68:71], a[68:71], a[28:31], v[68:71]\n"
"ds_read_b128 a[52:55], v182 offset:2048\n"
"v_sub_u32 v253, v253, v241\n"
"v_mfma_f32_16x16x32_bf16 v[36:39], a[64:67], a[28:31], v[36:39]\n"
"buffer_load_dwordx4 v5, s[28:31], s4 offen offset:3072 lds\n"
"s_add_u32 s4, s4, 128\n"
"v_mfma_f32_16x16x32_bf16 v[72:75], a[72:75], a[0:3], v[72:75]\n"
"ds_read_b64_tr_b16 a[100:101], v184 offset:27648\n"
"v_add_u32 v253, 16492, v253\n"
"v_mfma_f32_16x16x32_bf16 v[76:79], a[72:75], a[4:7], v[76:79]\n"
"ds_read_b128 a[56:59], v183 offset:0\n"
"v_lshrrev_b32 v240, 3, v0\n"
"v_mfma_f32_16x16x32_bf16 v[204:207], a[120:123], a[112:115], v[204:207]\n"
"ds_read_b64_tr_b16 a[102:103], v184 offset:28672\n"
"v_and_b32 v241, 7, v0\n"
"v_mfma_f32_16x16x32_bf16 v[80:83], a[76:79], a[0:3], v[80:83]\n"
"v_lshlrev_b32 v241, 4, v241\n"
"v_mul_u32_u24 v254, 132, v240\n"
"v_mfma_f32_16x16x32_bf16 v[112:115], a[80:83], a[0:3], v[112:115]\n"
"ds_read_b128 a[60:63], v183 offset:2048\n"
"v_add_u32 v254, v254, v241\n"
"v_mfma_f32_16x16x32_bf16 v[116:119], a[80:83], a[4:7], v[116:119]\n"
"ds_read_b64_tr_b16 a[104:105], v185 offset:27648\n"
"v_add_u32 v254, 16384, v254\n"
"v_mfma_f32_16x16x32_bf16 v[84:87], a[76:79], a[4:7], v[84:87]\n"
"ds_read_b64_tr_b16 a[106:107], v185 offset:28672\n"
"v_lshl_add_u32 v255, v240, 12, v241\n"
"v_mfma_f32_16x16x32_bf16 v[88:91], a[76:79], a[8:11], v[88:91]\n"
"s_add_u32 m0, s7, 73728\n"
"s_nop 0\n"
"buffer_load_dwordx4 v193, s[32:35], s5 offen offset:0 lds\n"
"v_mfma_f32_16x16x32_bf16 v[120:123], a[80:83], a[8:11], v[120:123]\n"
"ds_read_b64_tr_b16 a[108:109], v186 offset:27648\n"
"v_and_b32 v248, 15, v1\n"
"v_mfma_f32_16x16x32_bf16 v[124:127], a[80:83], a[12:15], v[124:127]\n"
"ds_read_b64_tr_b16 a[110:111], v186 offset:27904\n"
"v_lshlrev_b32 v248, 4, v248\n"
"v_mfma_f32_16x16x32_bf16 v[92:95], a[76:79], a[12:15], v[92:95]\n"
"ds_read_b64_tr_b16 a[124:125], v164 offset:18432\n"
"s_lshl_b32 s12, s66, 11\n"
"v_mfma_f32_16x16x32_bf16 v[96:99], a[76:79], a[16:19], v[96:99]\n"
"s_add_u32 s12, s12, 81920\n"
"s_lshl_b32 s71, s44, 9\n"
"v_mfma_f32_16x16x32_bf16 v[128:131], a[80:83], a[16:19], v[128:131]\n"
"ds_read_b64_tr_b16 a[126:127], v165 offset:18432\n"
"v_add_u32 v244, s71, v248\n"
"v_mfma_f32_16x16x32_bf16 v[132:135], a[80:83], a[20:23], v[132:135]\n"
"buffer_load_dwordx4 v194, s[32:35], s5 offen offset:1024 lds\n"
"s_lshl_b32 s71, s45, 9\n"
"v_mfma_f32_16x16x32_bf16 v[100:103], a[76:79], a[20:23], v[100:103]\n"
"v_add_u32 v245, s71, v248\n"
"s_lshl_b32 s71, s46, 9\n"
"v_mfma_f32_16x16x32_bf16 v[104:107], a[76:79], a[24:27], v[104:107]\n"
"v_add_u32 v246, s71, v248\n"
"s_lshl_b32 s71, s47, 9\n"
"v_mfma_f32_16x16x32_bf16 v[136:139], a[80:83], a[24:27], v[136:139]\n"
"v_add_u32 v247, s71, v248\n"
"v_mfma_f32_16x16x32_bf16 v[140:143], a[80:83], a[28:31], v[140:143]\n"
"buffer_load_dwordx4 v195, s[32:35], s5 offen offset:2048 lds\n"
"v_mfma_f32_16x16x32_bf16 v[108:111], a[76:79], a[28:31], v[108:111]\n"
"v_mfma_f32_16x16x32_bf16 v[144:147], a[84:87], a[0:3], v[144:147]\n"
"v_mfma_f32_16x16x32_bf16 v[148:151], a[84:87], a[4:7], v[148:151]\n"
"s_waitcnt vmcnt(8) lgkmcnt(0)\n"
"s_barrier\n"
"v_mfma_f32_16x16x32_bf16 v[8:11], a[88:91], a[32:35], v[8:11]\n"
"ds_read_b64_tr_b16 a[64:65], v187 offset:0\n"
"v_mfma_f32_16x16x32_bf16 v[40:43], a[92:95], a[32:35], v[40:43]\n"
"ds_read_b128 a[0:3], v176 offset:16384\n"
"v_mfma_f32_16x16x32_bf16 v[44:47], a[92:95], a[36:39], v[44:47]\n"
"ds_read_b64_tr_b16 a[66:67], v187 offset:1024\n"
"v_mfma_f32_16x16x32_bf16 v[12:15], a[88:91], a[36:39], v[12:15]\n"
"buffer_load_dwordx4 v196, s[32:35], s5 offen offset:3072 lds\n"
"v_mfma_f32_16x16x32_bf16 v[16:19], a[88:91], a[40:43], v[16:19]\n"
"ds_read_b128 a[4:7], v176 offset:18432\n"
"v_mfma_f32_16x16x32_bf16 v[48:51], a[92:95], a[40:43], v[48:51]\n"
"ds_read_b64_tr_b16 a[68:69], v188 offset:0\n"
"v_mfma_f32_16x16x32_bf16 v[52:55], a[92:95], a[44:47], v[52:55]\n"
"ds_read_b128 a[8:11], v177 offset:16384\n"
"v_mfma_f32_16x16x32_bf16 v[20:23], a[88:91], a[44:47], v[20:23]\n"
"s_add_u32 m0, s7, 77824\n"
"s_nop 0\n"
"buffer_load_dwordx4 v197, s[32:35], s5 offen offset:0 lds\n"
"v_mfma_f32_16x16x32_bf16 v[24:27], a[88:91], a[48:51], v[24:27]\n"
"ds_read_b64_tr_b16 a[70:71], v188 offset:1024\n"
"v_mfma_f32_16x16x32_bf16 v[56:59], a[92:95], a[48:51], v[56:59]\n"
"ds_read_b128 a[12:15], v177 offset:18432\n"
"v_mfma_f32_16x16x32_bf16 v[60:63], a[92:95], a[52:55], v[60:63]\n"
"ds_read_b64_tr_b16 a[72:73], v189 offset:0\n"
"v_mfma_f32_16x16x32_bf16 v[28:31], a[88:91], a[52:55], v[28:31]\n"
"v_mfma_f32_16x16x32_bf16 v[32:35], a[88:91], a[56:59], v[32:35]\n"
"ds_read_b128 a[16:19], v178 offset:16384\n"
"v_mfma_f32_16x16x32_bf16 v[64:67], a[92:95], a[56:59], v[64:67]\n"
"ds_read_b64_tr_b16 a[74:75], v189 offset:256\n"
"v_mfma_f32_16x16x32_bf16 v[68:71], a[92:95], a[60:63], v[68:71]\n"
"ds_read_b128 a[20:23], v178 offset:18432\n"
"v_mfma_f32_16x16x32_bf16 v[36:39], a[88:91], a[60:63], v[36:39]\n"
"buffer_load_dwordx4 v198, s[32:35], s5 offen offset:1024 lds\n"
"v_mfma_f32_16x16x32_bf16 v[72:75], a[96:99], a[32:35], v[72:75]\n"
"ds_read_b64_tr_b16 a[76:77], v187 offset:9216\n"
"v_mfma_f32_16x16x32_bf16 v[76:79], a[96:99], a[36:39], v[76:79]\n"
"ds_read_b128 a[24:27], v179 offset:16384\n"
"v_mfma_f32_16x16x32_bf16 v[204:207], a[124:127], a[112:115], v[204:207]\n"
"ds_read_b64_tr_b16 a[78:79], v187 offset:10240\n"
"v_mfma_f32_16x16x32_bf16 v[80:83], a[100:103], a[32:35], v[80:83]\n"
"buffer_load_dwordx4 v199, s[32:35], s5 offen offset:2048 lds\n"
"v_mfma_f32_16x16x32_bf16 v[112:115], a[104:107], a[32:35], v[112:115]\n"
"ds_read_b128 a[28:31], v179 offset:18432\n"
"v_mfma_f32_16x16x32_bf16 v[116:119], a[104:107], a[36:39], v[116:119]\n"
"ds_read_b64_tr_b16 a[80:81], v188 offset:9216\n"
"v_mfma_f32_16x16x32_bf16 v[84:87], a[100:103], a[36:39], v[84:87]\n"
"ds_read_b64_tr_b16 a[82:83], v188 offset:10240\n"
"v_mfma_f32_16x16x32_bf16 v[88:91], a[100:103], a[40:43], v[88:91]\n"
"buffer_load_dwordx4 v200, s[32:35], s5 offen offset:3072 lds\n"
"v_mfma_f32_16x16x32_bf16 v[120:123], a[104:107], a[40:43], v[120:123]\n"
"ds_read_b64_tr_b16 a[84:85], v189 offset:9216\n"
"v_mfma_f32_16x16x32_bf16 v[124:127], a[104:107], a[44:47], v[124:127]\n"
"ds_read_b64_tr_b16 a[86:87], v189 offset:9472\n"
"v_mfma_f32_16x16x32_bf16 v[92:95], a[100:103], a[44:47], v[92:95]\n"
"ds_read_b64_tr_b16 a[120:121], v166 offset:0\n"
"v_mfma_f32_16x16x32_bf16 v[96:99], a[100:103], a[48:51], v[96:99]\n"
"v_mfma_f32_16x16x32_bf16 v[128:131], a[104:107], a[48:51], v[128:131]\n"
"ds_read_b64_tr_b16 a[122:123], v167 offset:0\n"
"v_mfma_f32_16x16x32_bf16 v[132:135], a[104:107], a[52:55], v[132:135]\n"
"s_add_u32 m0, s7, 81920\n"
"s_nop 0\n"
"buffer_load_dwordx4 v201, s[32:35], s5 offen offset:0 lds\n"
"s_add_u32 s5, s5, 589824\n"
"v_mfma_f32_16x16x32_bf16 v[100:103], a[100:103], a[52:55], v[100:103]\n"
"v_mfma_f32_16x16x32_bf16 v[104:107], a[100:103], a[56:59], v[104:107]\n"
"v_mfma_f32_16x16x32_bf16 v[136:139], a[104:107], a[56:59], v[136:139]\n"
"v_mfma_f32_16x16x32_bf16 v[140:143], a[104:107], a[60:63], v[140:143]\n"
"s_add_u32 m0, s6, 0\n"
"s_nop 0\n"
"buffer_load_dwordx4 v2, s[28:31], s4 offen offset:0 lds\n"
"v_mfma_f32_16x16x32_bf16 v[108:111], a[100:103], a[60:63], v[108:111]\n"
"v_mfma_f32_16x16x32_bf16 v[144:147], a[108:111], a[32:35], v[144:147]\n"
"v_mfma_f32_16x16x32_bf16 v[148:151], a[108:111], a[36:39], v[148:151]\n"
"s_waitcnt lgkmcnt(0)\n"
"v_mfma_f32_16x16x32_bf16 v[8:11], a[64:67], a[0:3], v[8:11]\n"
"ds_read_b64_tr_b16 a[88:89], v187 offset:18432\n"
"v_mfma_f32_16x16x32_bf16 v[40:43], a[68:71], a[0:3], v[40:43]\n"
"ds_read_b128 a[32:35], v180 offset:16384\n"
"v_mfma_f32_16x16x32_bf16 v[44:47], a[68:71], a[4:7], v[44:47]\n"
"ds_read_b64_tr_b16 a[90:91], v187 offset:19456\n"
"v_mfma_f32_16x16x32_bf16 v[12:15], a[64:67], a[4:7], v[12:15]\n"
"buffer_load_dwordx4 v3, s[28:31], s4 offen offset:1024 lds\n"
"v_mfma_f32_16x16x32_bf16 v[16:19], a[64:67], a[8:11], v[16:19]\n"
"ds_read_b128 a[36:39], v180 offset:18432\n"
"v_mfma_f32_16x16x32_bf16 v[48:51], a[68:71], a[8:11], v[48:51]\n"
"ds_read_b64_tr_b16 a[92:93], v188 offset:18432\n"
"v_mfma_f32_16x16x32_bf16 v[52:55], a[68:71], a[12:15], v[52:55]\n"
"ds_read_b128 a[40:43], v181 offset:16384\n"
"v_mfma_f32_16x16x32_bf16 v[20:23], a[64:67], a[12:15], v[20:23]\n"
"v_mfma_f32_16x16x32_bf16 v[24:27], a[64:67], a[16:19], v[24:27]\n"
"ds_read_b64_tr_b16 a[94:95], v188 offset:19456\n"
"v_mfma_f32_16x16x32_bf16 v[56:59], a[68:71], a[16:19], v[56:59]\n"
"ds_read_b128 a[44:47], v181 offset:18432\n"
"v_mfma_f32_16x16x32_bf16 v[60:63], a[68:71], a[20:23], v[60:63]\n"
"ds_read_b64_tr_b16 a[96:97], v189 offset:18432\n"
"v_mfma_f32_16x16x32_bf16 v[28:31], a[64:67], a[20:23], v[28:31]\n"
"buffer_load_dwordx4 v4, s[28:31], s4 offen offset:2048 lds\n"
"v_mfma_f32_16x16x32_bf16 v[32:35], a[64:67], a[24:27], v[32:35]\n"
"ds_read_b128 a[48:51], v182 offset:16384\n"
"v_mfma_f32_16x16x32_bf16 v[64:67], a[68:71], a[24:27], v[64:67]\n"
"ds_read_b64_tr_b16 a[98:99], v189 offset:18688\n"
"v_mfma_f32_16x16x32_bf16 v[68:71], a[68:71], a[28:31], v[68:71]\n"
"ds_read_b128 a[52:55], v182 offset:18432\n"
"v_mfma_f32_16x16x32_bf16 v[36:39], a[64:67], a[28:31], v[36:39]\n"
"buffer_load_dwordx4 v5, s[28:31], s4 offen offset:3072 lds\n"
"s_add_u32 s4, s4, 128\n"
"v_mfma_f32_16x16x32_bf16 v[72:75], a[72:75], a[0:3], v[72:75]\n"
"ds_read_b64_tr_b16 a[100:101], v187 offset:27648\n"
"v_mfma_f32_16x16x32_bf16 v[76:79], a[72:75], a[4:7], v[76:79]\n"
"ds_read_b128 a[56:59], v183 offset:16384\n"
"v_mfma_f32_16x16x32_bf16 v[204:207], a[120:123], a[112:115], v[204:207]\n"
"ds_read_b64_tr_b16 a[102:103], v187 offset:28672\n"
"v_mfma_f32_16x16x32_bf16 v[80:83], a[76:79], a[0:3], v[80:83]\n"
"v_mfma_f32_16x16x32_bf16 v[112:115], a[80:83], a[0:3], v[112:115]\n"
"ds_read_b128 a[60:63], v183 offset:18432\n"
"v_mfma_f32_16x16x32_bf16 v[116:119], a[80:83], a[4:7], v[116:119]\n"
"ds_read_b64_tr_b16 a[104:105], v188 offset:27648\n"
"v_mfma_f32_16x16x32_bf16 v[84:87], a[76:79], a[4:7], v[84:87]\n"
"ds_read_b64_tr_b16 a[106:107], v188 offset:28672\n"
"v_mfma_f32_16x16x32_bf16 v[88:91], a[76:79], a[8:11], v[88:91]\n"
"s_add_u32 m0, s7, 0\n"
"s_nop 0\n"
"buffer_load_dwordx4 v193, s[32:35], s5 offen offset:0 lds\n"
"v_mfma_f32_16x16x32_bf16 v[120:123], a[80:83], a[8:11], v[120:123]\n"
"ds_read_b64_tr_b16 a[108:109], v189 offset:27648\n"
"v_mfma_f32_16x16x32_bf16 v[124:127], a[80:83], a[12:15], v[124:127]\n"
"ds_read_b64_tr_b16 a[110:111], v189 offset:27904\n"
"v_mfma_f32_16x16x32_bf16 v[92:95], a[76:79], a[12:15], v[92:95]\n"
"ds_read_b64_tr_b16 a[124:125], v166 offset:18432\n"
"v_mfma_f32_16x16x32_bf16 v[96:99], a[76:79], a[16:19], v[96:99]\n"
"v_mfma_f32_16x16x32_bf16 v[128:131], a[80:83], a[16:19], v[128:131]\n"
"ds_read_b64_tr_b16 a[126:127], v167 offset:18432\n"
"v_mfma_f32_16x16x32_bf16 v[132:135], a[80:83], a[20:23], v[132:135]\n"
"buffer_load_dwordx4 v194, s[32:35], s5 offen offset:1024 lds\n"
"v_mfma_f32_16x16x32_bf16 v[100:103], a[76:79], a[20:23], v[100:103]\n"
"v_mfma_f32_16x16x32_bf16 v[104:107], a[76:79], a[24:27], v[104:107]\n"
"v_mfma_f32_16x16x32_bf16 v[136:139], a[80:83], a[24:27], v[136:139]\n"
"v_mfma_f32_16x16x32_bf16 v[140:143], a[80:83], a[28:31], v[140:143]\n"
"buffer_load_dwordx4 v195, s[32:35], s5 offen offset:2048 lds\n"
"v_mfma_f32_16x16x32_bf16 v[108:111], a[76:79], a[28:31], v[108:111]\n"
"v_mfma_f32_16x16x32_bf16 v[144:147], a[84:87], a[0:3], v[144:147]\n"
"v_mfma_f32_16x16x32_bf16 v[148:151], a[84:87], a[4:7], v[148:151]\n"
"s_waitcnt vmcnt(7) lgkmcnt(0)\n"
"s_barrier\n"
"v_mfma_f32_16x16x32_bf16 v[8:11], a[88:91], a[32:35], v[8:11]\n"
"ds_read_b64_tr_b16 a[64:65], v190 offset:0\n"
"v_mfma_f32_16x16x32_bf16 v[40:43], a[92:95], a[32:35], v[40:43]\n"
"ds_read_b128 a[0:3], v176 offset:32768\n"
"v_mfma_f32_16x16x32_bf16 v[44:47], a[92:95], a[36:39], v[44:47]\n"
"ds_read_b64_tr_b16 a[66:67], v190 offset:1024\n"
"v_mfma_f32_16x16x32_bf16 v[12:15], a[88:91], a[36:39], v[12:15]\n"
"buffer_load_dwordx4 v196, s[32:35], s5 offen offset:3072 lds\n"
"v_mfma_f32_16x16x32_bf16 v[16:19], a[88:91], a[40:43], v[16:19]\n"
"ds_read_b128 a[4:7], v176 offset:34816\n"
"v_mfma_f32_16x16x32_bf16 v[48:51], a[92:95], a[40:43], v[48:51]\n"
"ds_read_b64_tr_b16 a[68:69], v191 offset:0\n"
"v_mfma_f32_16x16x32_bf16 v[52:55], a[92:95], a[44:47], v[52:55]\n"
"ds_read_b128 a[8:11], v177 offset:32768\n"
"v_mfma_f32_16x16x32_bf16 v[20:23], a[88:91], a[44:47], v[20:23]\n"
"s_add_u32 m0, s7, 4096\n"
"s_nop 0\n"
"buffer_load_dwordx4 v197, s[32:35], s5 offen offset:0 lds\n"
"v_mfma_f32_16x16x32_bf16 v[24:27], a[88:91], a[48:51], v[24:27]\n"
"ds_read_b64_tr_b16 a[70:71], v191 offset:1024\n"
"v_mfma_f32_16x16x32_bf16 v[56:59], a[92:95], a[48:51], v[56:59]\n"
"ds_read_b128 a[12:15], v177 offset:34816\n"
"v_mfma_f32_16x16x32_bf16 v[60:63], a[92:95], a[52:55], v[60:63]\n"
"ds_read_b64_tr_b16 a[72:73], v192 offset:0\n"
"v_mfma_f32_16x16x32_bf16 v[28:31], a[88:91], a[52:55], v[28:31]\n"
"v_mfma_f32_16x16x32_bf16 v[32:35], a[88:91], a[56:59], v[32:35]\n"
"ds_read_b128 a[16:19], v178 offset:32768\n"
"v_mfma_f32_16x16x32_bf16 v[64:67], a[92:95], a[56:59], v[64:67]\n"
"ds_read_b64_tr_b16 a[74:75], v192 offset:256\n"
"v_mfma_f32_16x16x32_bf16 v[68:71], a[92:95], a[60:63], v[68:71]\n"
"ds_read_b128 a[20:23], v178 offset:34816\n"
"v_mfma_f32_16x16x32_bf16 v[36:39], a[88:91], a[60:63], v[36:39]\n"
"buffer_load_dwordx4 v198, s[32:35], s5 offen offset:1024 lds\n"
"v_mfma_f32_16x16x32_bf16 v[72:75], a[96:99], a[32:35], v[72:75]\n"
"ds_read_b64_tr_b16 a[76:77], v190 offset:9216\n"
"v_mfma_f32_16x16x32_bf16 v[76:79], a[96:99], a[36:39], v[76:79]\n"
"ds_read_b128 a[24:27], v179 offset:32768\n"
"v_mfma_f32_16x16x32_bf16 v[204:207], a[124:127], a[112:115], v[204:207]\n"
"ds_read_b64_tr_b16 a[78:79], v190 offset:10240\n"
"v_mfma_f32_16x16x32_bf16 v[80:83], a[100:103], a[32:35], v[80:83]\n"
"buffer_load_dwordx4 v199, s[32:35], s5 offen offset:2048 lds\n"
"v_mfma_f32_16x16x32_bf16 v[112:115], a[104:107], a[32:35], v[112:115]\n"
"ds_read_b128 a[28:31], v179 offset:34816\n"
"v_mfma_f32_16x16x32_bf16 v[116:119], a[104:107], a[36:39], v[116:119]\n"
"ds_read_b64_tr_b16 a[80:81], v191 offset:9216\n"
"v_mfma_f32_16x16x32_bf16 v[84:87], a[100:103], a[36:39], v[84:87]\n"
"ds_read_b64_tr_b16 a[82:83], v191 offset:10240\n"
"v_mfma_f32_16x16x32_bf16 v[88:91], a[100:103], a[40:43], v[88:91]\n"
"buffer_load_dwordx4 v200, s[32:35], s5 offen offset:3072 lds\n"
"v_mfma_f32_16x16x32_bf16 v[120:123], a[104:107], a[40:43], v[120:123]\n"
"ds_read_b64_tr_b16 a[84:85], v192 offset:9216\n"
"v_mfma_f32_16x16x32_bf16 v[124:127], a[104:107], a[44:47], v[124:127]\n"
"ds_read_b64_tr_b16 a[86:87], v192 offset:9472\n"
"v_mfma_f32_16x16x32_bf16 v[92:95], a[100:103], a[44:47], v[92:95]\n"
"ds_read_b64_tr_b16 a[120:121], v168 offset:0\n"
"v_mfma_f32_16x16x32_bf16 v[96:99], a[100:103], a[48:51], v[96:99]\n"
"v_mfma_f32_16x16x32_bf16 v[128:131], a[104:107], a[48:51], v[128:131]\n"
"ds_read_b64_tr_b16 a[122:123], v169 offset:0\n"
"v_mfma_f32_16x16x32_bf16 v[132:135], a[104:107], a[52:55], v[132:135]\n"
"s_add_u32 m0, s7, 8192\n"
"s_nop 0\n"
"buffer_load_dwordx4 v201, s[32:35], s5 offen offset:0 lds\n"
"s_add_u32 s5, s5, 589824\n"
"v_mfma_f32_16x16x32_bf16 v[100:103], a[100:103], a[52:55], v[100:103]\n"
"v_mfma_f32_16x16x32_bf16 v[104:107], a[100:103], a[56:59], v[104:107]\n"
"v_mfma_f32_16x16x32_bf16 v[136:139], a[104:107], a[56:59], v[136:139]\n"
"v_mfma_f32_16x16x32_bf16 v[140:143], a[104:107], a[60:63], v[140:143]\n"
"s_add_u32 m0, s6, 16384\n"
"s_nop 0\n"
"buffer_load_dwordx4 v2, s[28:31], s4 offen offset:0 lds\n"
"v_mfma_f32_16x16x32_bf16 v[108:111], a[100:103], a[60:63], v[108:111]\n"
"v_mfma_f32_16x16x32_bf16 v[144:147], a[108:111], a[32:35], v[144:147]\n"
"v_mfma_f32_16x16x32_bf16 v[148:151], a[108:111], a[36:39], v[148:151]\n"
"s_waitcnt lgkmcnt(0)\n"
"v_mfma_f32_16x16x32_bf16 v[8:11], a[64:67], a[0:3], v[8:11]\n"
"ds_read_b64_tr_b16 a[88:89], v190 offset:18432\n"
"v_mfma_f32_16x16x32_bf16 v[40:43], a[68:71], a[0:3], v[40:43]\n"
"ds_read_b128 a[32:35], v180 offset:32768\n"
"v_mfma_f32_16x16x32_bf16 v[44:47], a[68:71], a[4:7], v[44:47]\n"
"ds_read_b64_tr_b16 a[90:91], v190 offset:19456\n"
"v_mfma_f32_16x16x32_bf16 v[12:15], a[64:67], a[4:7], v[12:15]\n"
"buffer_load_dwordx4 v3, s[28:31], s4 offen offset:1024 lds\n"
"v_mfma_f32_16x16x32_bf16 v[16:19], a[64:67], a[8:11], v[16:19]\n"
"ds_read_b128 a[36:39], v180 offset:34816\n"
"v_mfma_f32_16x16x32_bf16 v[48:51], a[68:71], a[8:11], v[48:51]\n"
"ds_read_b64_tr_b16 a[92:93], v191 offset:18432\n"
"v_mfma_f32_16x16x32_bf16 v[52:55], a[68:71], a[12:15], v[52:55]\n"
"ds_read_b128 a[40:43], v181 offset:32768\n"
"v_mfma_f32_16x16x32_bf16 v[20:23], a[64:67], a[12:15], v[20:23]\n"
"v_mfma_f32_16x16x32_bf16 v[24:27], a[64:67], a[16:19], v[24:27]\n"
"ds_read_b64_tr_b16 a[94:95], v191 offset:19456\n"
"v_mfma_f32_16x16x32_bf16 v[56:59], a[68:71], a[16:19], v[56:59]\n"
"ds_read_b128 a[44:47], v181 offset:34816\n"
"v_mfma_f32_16x16x32_bf16 v[60:63], a[68:71], a[20:23], v[60:63]\n"
"ds_read_b64_tr_b16 a[96:97], v192 offset:18432\n"
"v_mfma_f32_16x16x32_bf16 v[28:31], a[64:67], a[20:23], v[28:31]\n"
"buffer_load_dwordx4 v4, s[28:31], s4 offen offset:2048 lds\n"
"v_mfma_f32_16x16x32_bf16 v[32:35], a[64:67], a[24:27], v[32:35]\n"
"ds_read_b128 a[48:51], v182 offset:32768\n"
"v_mfma_f32_16x16x32_bf16 v[64:67], a[68:71], a[24:27], v[64:67]\n"
"ds_read_b64_tr_b16 a[98:99], v192 offset:18688\n"
"v_mfma_f32_16x16x32_bf16 v[68:71], a[68:71], a[28:31], v[68:71]\n"
"ds_read_b128 a[52:55], v182 offset:34816\n"
"v_mfma_f32_16x16x32_bf16 v[36:39], a[64:67], a[28:31], v[36:39]\n"
"buffer_load_dwordx4 v5, s[28:31], s4 offen offset:3072 lds\n"
"s_add_u32 s4, s4, 128\n"
"v_mfma_f32_16x16x32_bf16 v[72:75], a[72:75], a[0:3], v[72:75]\n"
"ds_read_b64_tr_b16 a[100:101], v190 offset:27648\n"
"v_mfma_f32_16x16x32_bf16 v[76:79], a[72:75], a[4:7], v[76:79]\n"
"ds_read_b128 a[56:59], v183 offset:32768\n"
"v_mfma_f32_16x16x32_bf16 v[204:207], a[120:123], a[112:115], v[204:207]\n"
"ds_read_b64_tr_b16 a[102:103], v190 offset:28672\n"
"v_mfma_f32_16x16x32_bf16 v[80:83], a[76:79], a[0:3], v[80:83]\n"
"v_mfma_f32_16x16x32_bf16 v[112:115], a[80:83], a[0:3], v[112:115]\n"
"ds_read_b128 a[60:63], v183 offset:34816\n"
"v_mfma_f32_16x16x32_bf16 v[116:119], a[80:83], a[4:7], v[116:119]\n"
"ds_read_b64_tr_b16 a[104:105], v191 offset:27648\n"
"v_mfma_f32_16x16x32_bf16 v[84:87], a[76:79], a[4:7], v[84:87]\n"
"ds_read_b64_tr_b16 a[106:107], v191 offset:28672\n"
"v_mfma_f32_16x16x32_bf16 v[88:91], a[76:79], a[8:11], v[88:91]\n"
"s_add_u32 m0, s7, 36864\n"
"s_nop 0\n"
"buffer_load_dwordx4 v193, s[32:35], s5 offen offset:0 lds\n"
"v_mfma_f32_16x16x32_bf16 v[120:123], a[80:83], a[8:11], v[120:123]\n"
"ds_read_b64_tr_b16 a[108:109], v192 offset:27648\n"
"v_mfma_f32_16x16x32_bf16 v[124:127], a[80:83], a[12:15], v[124:127]\n"
"ds_read_b64_tr_b16 a[110:111], v192 offset:27904\n"
"v_mfma_f32_16x16x32_bf16 v[92:95], a[76:79], a[12:15], v[92:95]\n"
"ds_read_b64_tr_b16 a[124:125], v168 offset:18432\n"
"v_mfma_f32_16x16x32_bf16 v[96:99], a[76:79], a[16:19], v[96:99]\n"
"v_mfma_f32_16x16x32_bf16 v[128:131], a[80:83], a[16:19], v[128:131]\n"
"ds_read_b64_tr_b16 a[126:127], v169 offset:18432\n"
"v_mfma_f32_16x16x32_bf16 v[132:135], a[80:83], a[20:23], v[132:135]\n"
"buffer_load_dwordx4 v194, s[32:35], s5 offen offset:1024 lds\n"
"v_mfma_f32_16x16x32_bf16 v[100:103], a[76:79], a[20:23], v[100:103]\n"
"v_mfma_f32_16x16x32_bf16 v[104:107], a[76:79], a[24:27], v[104:107]\n"
"v_mfma_f32_16x16x32_bf16 v[136:139], a[80:83], a[24:27], v[136:139]\n"
"v_mfma_f32_16x16x32_bf16 v[140:143], a[80:83], a[28:31], v[140:143]\n"
"buffer_load_dwordx4 v195, s[32:35], s5 offen offset:2048 lds\n"
"v_mfma_f32_16x16x32_bf16 v[108:111], a[76:79], a[28:31], v[108:111]\n"
"v_mfma_f32_16x16x32_bf16 v[144:147], a[84:87], a[0:3], v[144:147]\n"
"v_mfma_f32_16x16x32_bf16 v[148:151], a[84:87], a[4:7], v[148:151]\n"
"s_waitcnt vmcnt(7) lgkmcnt(0)\n"
"s_barrier\n"
"v_mfma_f32_16x16x32_bf16 v[8:11], a[88:91], a[32:35], v[8:11]\n"
"ds_read_b64_tr_b16 a[64:65], v184 offset:0\n"
"v_mfma_f32_16x16x32_bf16 v[40:43], a[92:95], a[32:35], v[40:43]\n"
"ds_read_b128 a[0:3], v176 offset:0\n"
"v_mfma_f32_16x16x32_bf16 v[44:47], a[92:95], a[36:39], v[44:47]\n"
"ds_read_b64_tr_b16 a[66:67], v184 offset:1024\n"
"v_mfma_f32_16x16x32_bf16 v[12:15], a[88:91], a[36:39], v[12:15]\n"
"buffer_load_dwordx4 v196, s[32:35], s5 offen offset:3072 lds\n"
"v_mfma_f32_16x16x32_bf16 v[16:19], a[88:91], a[40:43], v[16:19]\n"
"ds_read_b128 a[4:7], v176 offset:2048\n"
"v_mfma_f32_16x16x32_bf16 v[48:51], a[92:95], a[40:43], v[48:51]\n"
"ds_read_b64_tr_b16 a[68:69], v185 offset:0\n"
"v_mfma_f32_16x16x32_bf16 v[52:55], a[92:95], a[44:47], v[52:55]\n"
"ds_read_b128 a[8:11], v177 offset:0\n"
"v_mfma_f32_16x16x32_bf16 v[20:23], a[88:91], a[44:47], v[20:23]\n"
"s_add_u32 m0, s7, 40960\n"
"s_nop 0\n"
"buffer_load_dwordx4 v197, s[32:35], s5 offen offset:0 lds\n"
"v_mfma_f32_16x16x32_bf16 v[24:27], a[88:91], a[48:51], v[24:27]\n"
"ds_read_b64_tr_b16 a[70:71], v185 offset:1024\n"
"v_mfma_f32_16x16x32_bf16 v[56:59], a[92:95], a[48:51], v[56:59]\n"
"ds_read_b128 a[12:15], v177 offset:2048\n"
"v_mfma_f32_16x16x32_bf16 v[60:63], a[92:95], a[52:55], v[60:63]\n"
"ds_read_b64_tr_b16 a[72:73], v186 offset:0\n"
"v_mfma_f32_16x16x32_bf16 v[28:31], a[88:91], a[52:55], v[28:31]\n"
"v_mfma_f32_16x16x32_bf16 v[32:35], a[88:91], a[56:59], v[32:35]\n"
"ds_read_b128 a[16:19], v178 offset:0\n"
"v_mfma_f32_16x16x32_bf16 v[64:67], a[92:95], a[56:59], v[64:67]\n"
"ds_read_b64_tr_b16 a[74:75], v186 offset:256\n"
"v_mfma_f32_16x16x32_bf16 v[68:71], a[92:95], a[60:63], v[68:71]\n"
"ds_read_b128 a[20:23], v178 offset:2048\n"
"v_mfma_f32_16x16x32_bf16 v[36:39], a[88:91], a[60:63], v[36:39]\n"
"buffer_load_dwordx4 v198, s[32:35], s5 offen offset:1024 lds\n"
"v_mfma_f32_16x16x32_bf16 v[72:75], a[96:99], a[32:35], v[72:75]\n"
"ds_read_b64_tr_b16 a[76:77], v184 offset:9216\n"
"v_mfma_f32_16x16x32_bf16 v[76:79], a[96:99], a[36:39], v[76:79]\n"
"ds_read_b128 a[24:27], v179 offset:0\n"
"v_mfma_f32_16x16x32_bf16 v[204:207], a[124:127], a[112:115], v[204:207]\n"
"ds_read_b64_tr_b16 a[78:79], v184 offset:10240\n"
"v_mfma_f32_16x16x32_bf16 v[80:83], a[100:103], a[32:35], v[80:83]\n"
"buffer_load_dwordx4 v199, s[32:35], s5 offen offset:2048 lds\n"
"v_mfma_f32_16x16x32_bf16 v[112:115], a[104:107], a[32:35], v[112:115]\n"
"ds_read_b128 a[28:31], v179 offset:2048\n"
"v_mfma_f32_16x16x32_bf16 v[116:119], a[104:107], a[36:39], v[116:119]\n"
"ds_read_b64_tr_b16 a[80:81], v185 offset:9216\n"
"v_mfma_f32_16x16x32_bf16 v[84:87], a[100:103], a[36:39], v[84:87]\n"
"ds_read_b64_tr_b16 a[82:83], v185 offset:10240\n"
"v_mfma_f32_16x16x32_bf16 v[88:91], a[100:103], a[40:43], v[88:91]\n"
"buffer_load_dwordx4 v200, s[32:35], s5 offen offset:3072 lds\n"
"v_mfma_f32_16x16x32_bf16 v[120:123], a[104:107], a[40:43], v[120:123]\n"
"ds_read_b64_tr_b16 a[84:85], v186 offset:9216\n"
"v_and_b32 v240, 1, v1\n"
"v_cmp_eq_u32 vcc, 1, v240\n"
"v_mov_b32 v241, s68\n"
"v_mov_b32 v243, s69\n"
"v_cndmask_b32 v240, v204, v206, vcc\n"
"v_cndmask_b32 v242, v205, v207, vcc\n"
"buffer_store_dwordx4 v[240:243], v250, s[36:39], s17 offen sc1\n"
"v_mfma_f32_16x16x32_bf16 v[124:127], a[104:107], a[44:47], v[124:127]\n"
"ds_read_b64_tr_b16 a[86:87], v186 offset:9472\n"
"v_mfma_f32_16x16x32_bf16 v[92:95], a[100:103], a[44:47], v[92:95]\n"
"v_mfma_f32_16x16x32_bf16 v[96:99], a[100:103], a[48:51], v[96:99]\n"
"v_mfma_f32_16x16x32_bf16 v[128:131], a[104:107], a[48:51], v[128:131]\n"
"v_mfma_f32_16x16x32_bf16 v[132:135], a[104:107], a[52:55], v[132:135]\n"
"s_add_u32 m0, s7, 45056\n"
"s_nop 0\n"
"buffer_load_dwordx4 v201, s[32:35], s5 offen offset:0 lds\n"
"s_add_u32 s5, s5, 589824\n"
"v_mfma_f32_16x16x32_bf16 v[100:103], a[100:103], a[52:55], v[100:103]\n"
"v_mfma_f32_16x16x32_bf16 v[104:107], a[100:103], a[56:59], v[104:107]\n"
"v_mfma_f32_16x16x32_bf16 v[136:139], a[104:107], a[56:59], v[136:139]\n"
"v_mfma_f32_16x16x32_bf16 v[140:143], a[104:107], a[60:63], v[140:143]\n"
"s_add_u32 m0, s6, 32768\n"
"s_nop 0\n"
"buffer_load_dwordx4 v2, s[28:31], s4 offen offset:0 lds\n"
"v_mfma_f32_16x16x32_bf16 v[108:111], a[100:103], a[60:63], v[108:111]\n"
"v_mfma_f32_16x16x32_bf16 v[144:147], a[108:111], a[32:35], v[144:147]\n"
"v_mfma_f32_16x16x32_bf16 v[148:151], a[108:111], a[36:39], v[148:151]\n"
"s_waitcnt lgkmcnt(0)\n"
"v_mfma_f32_16x16x32_bf16 v[8:11], a[64:67], a[0:3], v[8:11]\n"
"ds_read_b64_tr_b16 a[88:89], v184 offset:18432\n"
"v_mfma_f32_16x16x32_bf16 v[40:43], a[68:71], a[0:3], v[40:43]\n"
"ds_read_b128 a[32:35], v180 offset:0\n"
"v_mfma_f32_16x16x32_bf16 v[44:47], a[68:71], a[4:7], v[44:47]\n"
"ds_read_b64_tr_b16 a[90:91], v184 offset:19456\n"
"v_mfma_f32_16x16x32_bf16 v[12:15], a[64:67], a[4:7], v[12:15]\n"
"buffer_load_dwordx4 v3, s[28:31], s4 offen offset:1024 lds\n"
"v_mfma_f32_16x16x32_bf16 v[16:19], a[64:67], a[8:11], v[16:19]\n"
"ds_read_b128 a[36:39], v180 offset:2048\n"
"v_mfma_f32_16x16x32_bf16 v[48:51], a[68:71], a[8:11], v[48:51]\n"
"ds_read_b64_tr_b16 a[92:93], v185 offset:18432\n"
"v_mfma_f32_16x16x32_bf16 v[52:55], a[68:71], a[12:15], v[52:55]\n"
"ds_read_b128 a[40:43], v181 offset:0\n"
"v_mfma_f32_16x16x32_bf16 v[20:23], a[64:67], a[12:15], v[20:23]\n"
"v_mfma_f32_16x16x32_bf16 v[24:27], a[64:67], a[16:19], v[24:27]\n"
"ds_read_b64_tr_b16 a[94:95], v185 offset:19456\n"
"v_mfma_f32_16x16x32_bf16 v[56:59], a[68:71], a[16:19], v[56:59]\n"
"ds_read_b128 a[44:47], v181 offset:2048\n"
"v_mfma_f32_16x16x32_bf16 v[60:63], a[68:71], a[20:23], v[60:63]\n"
"ds_read_b64_tr_b16 a[96:97], v186 offset:18432\n"
"v_mfma_f32_16x16x32_bf16 v[28:31], a[64:67], a[20:23], v[28:31]\n"
"buffer_load_dwordx4 v4, s[28:31], s4 offen offset:2048 lds\n"
"v_mfma_f32_16x16x32_bf16 v[32:35], a[64:67], a[24:27], v[32:35]\n"
"ds_read_b128 a[48:51], v182 offset:0\n"
"v_mfma_f32_16x16x32_bf16 v[64:67], a[68:71], a[24:27], v[64:67]\n"
"ds_read_b64_tr_b16 a[98:99], v186 offset:18688\n"
"v_mfma_f32_16x16x32_bf16 v[68:71], a[68:71], a[28:31], v[68:71]\n"
"ds_read_b128 a[52:55], v182 offset:2048\n"
"v_mfma_f32_16x16x32_bf16 v[36:39], a[64:67], a[28:31], v[36:39]\n"
"buffer_load_dwordx4 v5, s[28:31], s4 offen offset:3072 lds\n"
"s_add_u32 s4, s4, 128\n"
"v_mfma_f32_16x16x32_bf16 v[156:159], a[68:71], a[112:115], 0\n"
"ds_read_b64_tr_b16 a[100:101], v184 offset:27648\n"
"v_mfma_f32_16x16x32_bf16 v[152:155], a[64:67], a[112:115], 0\n"
"ds_read_b128 a[56:59], v183 offset:0\n"
"v_mfma_f32_16x16x32_bf16 v[160:163], a[72:75], a[112:115], 0\n"
"ds_read_b64_tr_b16 a[102:103], v184 offset:28672\n"
"v_mfma_f32_16x16x32_bf16 v[72:75], a[72:75], a[0:3], v[72:75]\n"
"v_mfma_f32_16x16x32_bf16 v[76:79], a[72:75], a[4:7], v[76:79]\n"
"ds_read_b128 a[60:63], v183 offset:2048\n"
"v_mfma_f32_16x16x32_bf16 v[80:83], a[76:79], a[0:3], v[80:83]\n"
"ds_read_b64_tr_b16 a[104:105], v185 offset:27648\n"
"v_mfma_f32_16x16x32_bf16 v[112:115], a[80:83], a[0:3], v[112:115]\n"
"ds_read_b64_tr_b16 a[106:107], v185 offset:28672\n"
"v_mfma_f32_16x16x32_bf16 v[116:119], a[80:83], a[4:7], v[116:119]\n"
"s_add_u32 m0, s7, 73728\n"
"s_nop 0\n"
"buffer_load_dwordx4 v193, s[32:35], s5 offen offset:0 lds\n"
"v_mfma_f32_16x16x32_bf16 v[84:87], a[76:79], a[4:7], v[84:87]\n"
"ds_read_b64_tr_b16 a[108:109], v186 offset:27648\n"
"v_mfma_f32_16x16x32_bf16 v[88:91], a[76:79], a[8:11], v[88:91]\n"
"ds_read_b64_tr_b16 a[110:111], v186 offset:27904\n"
"v_mfma_f32_16x16x32_bf16 v[120:123], a[80:83], a[8:11], v[120:123]\n"
"v_mfma_f32_16x16x32_bf16 v[124:127], a[80:83], a[12:15], v[124:127]\n"
"v_mfma_f32_16x16x32_bf16 v[92:95], a[76:79], a[12:15], v[92:95]\n"
"v_mfma_f32_16x16x32_bf16 v[96:99], a[76:79], a[16:19], v[96:99]\n"
"buffer_load_dwordx4 v194, s[32:35], s5 offen offset:1024 lds\n"
"v_mfma_f32_16x16x32_bf16 v[128:131], a[80:83], a[16:19], v[128:131]\n"
"v_mfma_f32_16x16x32_bf16 v[132:135], a[80:83], a[20:23], v[132:135]\n"
"v_mfma_f32_16x16x32_bf16 v[100:103], a[76:79], a[20:23], v[100:103]\n"
"v_mfma_f32_16x16x32_bf16 v[104:107], a[76:79], a[24:27], v[104:107]\n"
"buffer_load_dwordx4 v195, s[32:35], s5 offen offset:2048 lds\n"
"v_mfma_f32_16x16x32_bf16 v[136:139], a[80:83], a[24:27], v[136:139]\n"
"v_mfma_f32_16x16x32_bf16 v[140:143], a[80:83], a[28:31], v[140:143]\n"
"v_mfma_f32_16x16x32_bf16 v[108:111], a[76:79], a[28:31], v[108:111]\n"
"v_mfma_f32_16x16x32_bf16 v[168:171], a[80:83], a[112:115], 0\n"
"v_mfma_f32_16x16x32_bf16 v[164:167], a[76:79], a[112:115], 0\n"
"v_mfma_f32_16x16x32_bf16 v[172:175], a[84:87], a[112:115], 0\n"
"v_mfma_f32_16x16x32_bf16 v[144:147], a[84:87], a[0:3], v[144:147]\n"
"v_mfma_f32_16x16x32_bf16 v[148:151], a[84:87], a[4:7], v[148:151]\n"
"s_waitcnt vmcnt(7) lgkmcnt(0)\n"
"s_barrier\n"
"v_mfma_f32_16x16x32_bf16 v[8:11], a[88:91], a[32:35], v[8:11]\n"
"ds_read_b64_tr_b16 a[64:65], v187 offset:0\n"
"buffer_load_dwordx4 v[208:211], v244, s[36:39], s12 offen offset:0 sc1\n"
"v_mfma_f32_16x16x32_bf16 v[40:43], a[92:95], a[32:35], v[40:43]\n"
"ds_read_b128 a[0:3], v176 offset:16384\n"
"v_mfma_f32_16x16x32_bf16 v[44:47], a[92:95], a[36:39], v[44:47]\n"
"ds_read_b64_tr_b16 a[66:67], v187 offset:1024\n"
"v_mfma_f32_16x16x32_bf16 v[12:15], a[88:91], a[36:39], v[12:15]\n"
"buffer_load_dwordx4 v196, s[32:35], s5 offen offset:3072 lds\n"
"buffer_load_dwordx4 v[212:215], v244, s[36:39], s12 offen offset:256 sc1\n"
"v_mfma_f32_16x16x32_bf16 v[16:19], a[88:91], a[40:43], v[16:19]\n"
"ds_read_b128 a[4:7], v176 offset:18432\n"
"v_mfma_f32_16x16x32_bf16 v[48:51], a[92:95], a[40:43], v[48:51]\n"
"ds_read_b64_tr_b16 a[68:69], v188 offset:0\n"
"v_mfma_f32_16x16x32_bf16 v[52:55], a[92:95], a[44:47], v[52:55]\n"
"ds_read_b128 a[8:11], v177 offset:16384\n"
"buffer_load_dwordx4 v[216:219], v245, s[36:39], s12 offen offset:0 sc1\n"
"v_mfma_f32_16x16x32_bf16 v[20:23], a[88:91], a[44:47], v[20:23]\n"
"s_add_u32 m0, s7, 77824\n"
"s_nop 0\n"
"buffer_load_dwordx4 v197, s[32:35], s5 offen offset:0 lds\n"
"v_mfma_f32_16x16x32_bf16 v[24:27], a[88:91], a[48:51], v[24:27]\n"
"ds_read_b64_tr_b16 a[70:71], v188 offset:1024\n"
"v_mfma_f32_16x16x32_bf16 v[56:59], a[92:95], a[48:51], v[56:59]\n"
"ds_read_b128 a[12:15], v177 offset:18432\n"
"buffer_load_dwordx4 v[220:223], v245, s[36:39], s12 offen offset:256 sc1\n"
"v_mfma_f32_16x16x32_bf16 v[60:63], a[92:95], a[52:55], v[60:63]\n"
"ds_read_b64_tr_b16 a[72:73], v189 offset:0\n"
"v_mfma_f32_16x16x32_bf16 v[28:31], a[88:91], a[52:55], v[28:31]\n"
"v_mfma_f32_16x16x32_bf16 v[32:35], a[88:91], a[56:59], v[32:35]\n"
"ds_read_b128 a[16:19], v178 offset:16384\n"
"buffer_load_dwordx4 v[224:227], v246, s[36:39], s12 offen offset:0 sc1\n"
"v_mfma_f32_16x16x32_bf16 v[64:67], a[92:95], a[56:59], v[64:67]\n"
"ds_read_b64_tr_b16 a[74:75], v189 offset:256\n"
"v_mfma_f32_16x16x32_bf16 v[68:71], a[92:95], a[60:63], v[68:71]\n"
"ds_read_b128 a[20:23], v178 offset:18432\n"
"v_mfma_f32_16x16x32_bf16 v[36:39], a[88:91], a[60:63], v[36:39]\n"
"buffer_load_dwordx4 v198, s[32:35], s5 offen offset:1024 lds\n"
"buffer_load_dwordx4 v[228:231], v246, s[36:39], s12 offen offset:256 sc1\n"
"v_mfma_f32_16x16x32_bf16 v[156:159], a[92:95], a[112:115], v[156:159]\n"
"ds_read_b64_tr_b16 a[76:77], v187 offset:9216\n"
"v_mfma_f32_16x16x32_bf16 v[152:155], a[88:91], a[112:115], v[152:155]\n"
"ds_read_b128 a[24:27], v179 offset:16384\n"
"v_mfma_f32_16x16x32_bf16 v[160:163], a[96:99], a[112:115], v[160:163]\n"
"ds_read_b64_tr_b16 a[78:79], v187 offset:10240\n"
"buffer_load_dwordx4 v[232:235], v247, s[36:39], s12 offen offset:0 sc1\n"
"v_mfma_f32_16x16x32_bf16 v[72:75], a[96:99], a[32:35], v[72:75]\n"
"buffer_load_dwordx4 v199, s[32:35], s5 offen offset:2048 lds\n"
"v_mfma_f32_16x16x32_bf16 v[76:79], a[96:99], a[36:39], v[76:79]\n"
"ds_read_b128 a[28:31], v179 offset:18432\n"
"v_mfma_f32_16x16x32_bf16 v[80:83], a[100:103], a[32:35], v[80:83]\n"
"ds_read_b64_tr_b16 a[80:81], v188 offset:9216\n"
"buffer_load_dwordx4 v[236:239], v247, s[36:39], s12 offen offset:256 sc1\n"
"v_mfma_f32_16x16x32_bf16 v[112:115], a[104:107], a[32:35], v[112:115]\n"
"ds_read_b64_tr_b16 a[82:83], v188 offset:10240\n"
"v_mfma_f32_16x16x32_bf16 v[116:119], a[104:107], a[36:39], v[116:119]\n"
"buffer_load_dwordx4 v200, s[32:35], s5 offen offset:3072 lds\n"
"v_mfma_f32_16x16x32_bf16 v[84:87], a[100:103], a[36:39], v[84:87]\n"
"ds_read_b64_tr_b16 a[84:85], v189 offset:9216\n"
"v_mfma_f32_16x16x32_bf16 v[88:91], a[100:103], a[40:43], v[88:91]\n"
"ds_read_b64_tr_b16 a[86:87], v189 offset:9472\n"
"v_mfma_f32_16x16x32_bf16 v[120:123], a[104:107], a[40:43], v[120:123]\n"
"v_mfma_f32_16x16x32_bf16 v[124:127], a[104:107], a[44:47], v[124:127]\n"
"v_mfma_f32_16x16x32_bf16 v[92:95], a[100:103], a[44:47], v[92:95]\n"
"v_mfma_f32_16x16x32_bf16 v[96:99], a[100:103], a[48:51], v[96:99]\n"
"s_add_u32 m0, s7, 81920\n"
"s_nop 0\n"
"buffer_load_dwordx4 v201, s[32:35], s5 offen offset:0 lds\n"
"s_add_u32 s5, s5, 589824\n"
"v_mfma_f32_16x16x32_bf16 v[128:131], a[104:107], a[48:51], v[128:131]\n"
"v_mfma_f32_16x16x32_bf16 v[132:135], a[104:107], a[52:55], v[132:135]\n"
"v_mfma_f32_16x16x32_bf16 v[100:103], a[100:103], a[52:55], v[100:103]\n"
"v_mfma_f32_16x16x32_bf16 v[104:107], a[100:103], a[56:59], v[104:107]\n"
"v_mfma_f32_16x16x32_bf16 v[136:139], a[104:107], a[56:59], v[136:139]\n"
"v_mfma_f32_16x16x32_bf16 v[140:143], a[104:107], a[60:63], v[140:143]\n"
"v_mfma_f32_16x16x32_bf16 v[108:111], a[100:103], a[60:63], v[108:111]\n"
"v_mfma_f32_16x16x32_bf16 v[168:171], a[104:107], a[112:115], v[168:171]\n"
"v_mfma_f32_16x16x32_bf16 v[164:167], a[100:103], a[112:115], v[164:167]\n"
"v_mfma_f32_16x16x32_bf16 v[172:175], a[108:111], a[112:115], v[172:175]\n"
"v_mfma_f32_16x16x32_bf16 v[144:147], a[108:111], a[32:35], v[144:147]\n"
"v_mfma_f32_16x16x32_bf16 v[148:151], a[108:111], a[36:39], v[148:151]\n"
"s_waitcnt lgkmcnt(0)\n"
"v_mfma_f32_16x16x32_bf16 v[8:11], a[64:67], a[0:3], v[8:11]\n"
"ds_read_b64_tr_b16 a[88:89], v187 offset:18432\n"
"buffer_load_dwordx4 v[204:207], v251, s[36:39], s17 offen sc1\n"
"v_mfma_f32_16x16x32_bf16 v[40:43], a[68:71], a[0:3], v[40:43]\n"
"ds_read_b128 a[32:35], v180 offset:16384\n"
"s_lshr_b32 s71, s64, 3\n"
"v_mfma_f32_16x16x32_bf16 v[44:47], a[68:71], a[4:7], v[44:47]\n"
"ds_read_b64_tr_b16 a[90:91], v187 offset:19456\n"
"s_mul_i32 s71, s71, 73728\n"
"v_mfma_f32_16x16x32_bf16 v[12:15], a[64:67], a[4:7], v[12:15]\n"
"s_and_b32 s76, s64, 7\n"
"s_mul_i32 s76, s76, 144\n"
"v_mfma_f32_16x16x32_bf16 v[16:19], a[64:67], a[8:11], v[16:19]\n"
"ds_read_b128 a[36:39], v180 offset:18432\n"
"s_add_u32 s71, s71, s76\n"
"v_mfma_f32_16x16x32_bf16 v[48:51], a[68:71], a[8:11], v[48:51]\n"
"ds_read_b64_tr_b16 a[92:93], v188 offset:18432\n"
"v_mfma_f32_16x16x32_bf16 v[52:55], a[68:71], a[12:15], v[52:55]\n"
"ds_read_b128 a[40:43], v181 offset:16384\n"
"v_mfma_f32_16x16x32_bf16 v[20:23], a[64:67], a[12:15], v[20:23]\n"
"s_cmp_lt_u32 s65, 2\n"
"s_cselect_b32 s76, 3456, 0\n"
"s_cselect_b32 s74, 49152, 0\n"
"v_mfma_f32_16x16x32_bf16 v[24:27], a[64:67], a[16:19], v[24:27]\n"
"ds_read_b64_tr_b16 a[94:95], v188 offset:19456\n"
"s_sub_u32 s71, s71, s76\n"
"v_mfma_f32_16x16x32_bf16 v[56:59], a[68:71], a[16:19], v[56:59]\n"
"ds_read_b128 a[44:47], v181 offset:18432\n"
"s_and_b32 s76, s65, 1\n"
"v_mfma_f32_16x16x32_bf16 v[60:63], a[68:71], a[20:23], v[60:63]\n"
"ds_read_b64_tr_b16 a[96:97], v189 offset:18432\n"
"s_mul_i32 s73, s76, 5120\n"
"v_mfma_f32_16x16x32_bf16 v[28:31], a[64:67], a[20:23], v[28:31]\n"
"s_add_u32 s74, s74, s73\n"
"s_mul_i32 s76, s76, 320\n"
"v_mfma_f32_16x16x32_bf16 v[32:35], a[64:67], a[24:27], v[32:35]\n"
"ds_read_b128 a[48:51], v182 offset:16384\n"
"s_movk_i32 s75, 1008\n"
"v_mfma_f32_16x16x32_bf16 v[64:67], a[68:71], a[24:27], v[64:67]\n"
"ds_read_b64_tr_b16 a[98:99], v189 offset:18688\n"
"v_add_u32 v240, s76, v1\n"
"v_mfma_f32_16x16x32_bf16 v[68:71], a[68:71], a[28:31], v[68:71]\n"
"ds_read_b128 a[52:55], v182 offset:18432\n"
"v_mul_u32_u24 v241, 7282, v240\n"
"v_mfma_f32_16x16x32_bf16 v[36:39], a[64:67], a[28:31], v[36:39]\n"
"v_lshl_add_u32 v242, v240, 4, s71\n"
"v_lshrrev_b32 v241, 16, v241\n"
"v_mfma_f32_16x16x32_bf16 v[156:159], a[68:71], a[112:115], v[156:159]\n"
"ds_read_b64_tr_b16 a[100:101], v187 offset:27648\n"
"v_mad_u32_u24 v242, v241, s75, v242\n"
"v_mfma_f32_16x16x32_bf16 v[152:155], a[64:67], a[112:115], v[152:155]\n"
"ds_read_b128 a[56:59], v183 offset:16384\n"
"v_mfma_f32_16x16x32_bf16 v[160:163], a[72:75], a[112:115], v[160:163]\n"
"ds_read_b64_tr_b16 a[102:103], v187 offset:28672\n"
"v_mfma_f32_16x16x32_bf16 v[72:75], a[72:75], a[0:3], v[72:75]\n"
"s_add_u32 m0, s74, 0\n"
"s_nop 0\n"
"buffer_load_dwordx4 v242, s[40:43], 0 offen lds\n"
"v_mfma_f32_16x16x32_bf16 v[76:79], a[72:75], a[4:7], v[76:79]\n"
"ds_read_b128 a[60:63], v183 offset:18432\n"
"v_add_u32 v240, 64, v240\n"
"v_mfma_f32_16x16x32_bf16 v[80:83], a[76:79], a[0:3], v[80:83]\n"
"ds_read_b64_tr_b16 a[104:105], v188 offset:27648\n"
"v_mul_u32_u24 v241, 7282, v240\n"
"v_mfma_f32_16x16x32_bf16 v[112:115], a[80:83], a[0:3], v[112:115]\n"
"ds_read_b64_tr_b16 a[106:107], v188 offset:28672\n"
"v_lshl_add_u32 v242, v240, 4, s71\n"
"v_mfma_f32_16x16x32_bf16 v[116:119], a[80:83], a[4:7], v[116:119]\n"
"v_lshrrev_b32 v241, 16, v241\n"
"v_mfma_f32_16x16x32_bf16 v[84:87], a[76:79], a[4:7], v[84:87]\n"
"ds_read_b64_tr_b16 a[108:109], v189 offset:27648\n"
"v_mad_u32_u24 v242, v241, s75, v242\n"
"v_mfma_f32_16x16x32_bf16 v[88:91], a[76:79], a[8:11], v[88:91]\n"
"ds_read_b64_tr_b16 a[110:111], v189 offset:27904\n"
"v_mfma_f32_16x16x32_bf16 v[120:123], a[80:83], a[8:11], v[120:123]\n"
"s_add_u32 m0, s74, 1024\n"
"s_nop 0\n"
"buffer_load_dwordx4 v242, s[40:43], 0 offen lds\n"
"v_mfma_f32_16x16x32_bf16 v[124:127], a[80:83], a[12:15], v[124:127]\n"
"v_add_u32 v240, 64, v240\n"
"v_mfma_f32_16x16x32_bf16 v[92:95], a[76:79], a[12:15], v[92:95]\n"
"v_mul_u32_u24 v241, 7282, v240\n"
"v_lshl_add_u32 v242, v240, 4, s71\n"
"v_mfma_f32_16x16x32_bf16 v[96:99], a[76:79], a[16:19], v[96:99]\n"
"v_lshrrev_b32 v241, 16, v241\n"
"v_mfma_f32_16x16x32_bf16 v[128:131], a[80:83], a[16:19], v[128:131]\n"
"v_mad_u32_u24 v242, v241, s75, v242\n"
"v_mfma_f32_16x16x32_bf16 v[132:135], a[80:83], a[20:23], v[132:135]\n"
"s_add_u32 m0, s74, 2048\n"
"s_nop 0\n"
"buffer_load_dwordx4 v242, s[40:43], 0 offen lds\n"
"v_mfma_f32_16x16x32_bf16 v[100:103], a[76:79], a[20:23], v[100:103]\n"
"v_add_u32 v240, 64, v240\n"
"v_mfma_f32_16x16x32_bf16 v[104:107], a[76:79], a[24:27], v[104:107]\n"
"v_mul_u32_u24 v241, 7282, v240\n"
"v_lshl_add_u32 v242, v240, 4, s71\n"
"v_mfma_f32_16x16x32_bf16 v[136:139], a[80:83], a[24:27], v[136:139]\n"
"v_lshrrev_b32 v241, 16, v241\n"
"v_mfma_f32_16x16x32_bf16 v[140:143], a[80:83], a[28:31], v[140:143]\n"
"v_mad_u32_u24 v242, v241, s75, v242\n"
"v_mfma_f32_16x16x32_bf16 v[108:111], a[76:79], a[28:31], v[108:111]\n"
"s_add_u32 m0, s74, 3072\n"
"s_nop 0\n"
"buffer_load_dwordx4 v242, s[40:43], 0 offen lds\n"
"v_mfma_f32_16x16x32_bf16 v[168:171], a[80:83], a[112:115], v[168:171]\n"
"v_add_u32 v240, 64, v240\n"
"v_mfma_f32_16x16x32_bf16 v[164:167], a[76:79], a[112:115], v[164:167]\n"
"v_mul_u32_u24 v241, 7282, v240\n"
"v_lshl_add_u32 v242, v240, 4, s71\n"
"v_mfma_f32_16x16x32_bf16 v[172:175], a[84:87], a[112:115], v[172:175]\n"
"v_lshrrev_b32 v241, 16, v241\n"
"v_mfma_f32_16x16x32_bf16 v[144:147], a[84:87], a[0:3], v[144:147]\n"
"v_mad_u32_u24 v242, v241, s75, v242\n"
"v_mfma_f32_16x16x32_bf16 v[148:151], a[84:87], a[4:7], v[148:151]\n"
"s_add_u32 m0, s74, 4096\n"
"s_nop 0\n"
"buffer_load_dwordx4 v242, s[40:43], 0 offen lds\n"
"s_waitcnt vmcnt(6) lgkmcnt(0)\n"
"s_barrier\n"
"v_mfma_f32_16x16x32_bf16 v[8:11], a[88:91], a[32:35], v[8:11]\n"
"ds_read_b64_tr_b16 a[64:65], v190 offset:0\n"
"v_lshrrev_b32 v4, 3, v1\n"
"v_mfma_f32_16x16x32_bf16 v[40:43], a[92:95], a[32:35], v[40:43]\n"
"ds_read_b128 a[0:3], v176 offset:32768\n"
"v_mul_u32_u24 v4, 1152, v4\n"
"v_mfma_f32_16x16x32_bf16 v[44:47], a[92:95], a[36:39], v[44:47]\n"
"ds_read_b64_tr_b16 a[66:67], v190 offset:1024\n"
"v_and_b32 v5, 7, v1\n"
"v_mfma_f32_16x16x32_bf16 v[12:15], a[88:91], a[36:39], v[12:15]\n"
"v_lshl_add_u32 v4, v5, 4, v4\n"
"v_mfma_f32_16x16x32_bf16 v[16:19], a[88:91], a[40:43], v[16:19]\n"
"ds_read_b128 a[4:7], v176 offset:34816\n"
"v_or_b32 v4, s13, v4\n"
"v_mfma_f32_16x16x32_bf16 v[48:51], a[92:95], a[40:43], v[48:51]\n"
"ds_read_b64_tr_b16 a[68:69], v191 offset:0\n"
"v_add_u32 v5, 8192, v4\n"
"v_mfma_f32_16x16x32_bf16 v[52:55], a[92:95], a[44:47], v[52:55]\n"
"ds_read_b128 a[8:11], v177 offset:32768\n"
"s_mul_i32 s76, s65, 5120\n"
"v_mfma_f32_16x16x32_bf16 v[20:23], a[88:91], a[44:47], v[20:23]\n"
"s_add_u32 m0, s76, 65536\n"
"s_nop 0\n"
"buffer_load_dwordx4 v4, s[56:59], s78 offen lds\n"
"v_mfma_f32_16x16x32_bf16 v[24:27], a[88:91], a[48:51], v[24:27]\n"
"ds_read_b64_tr_b16 a[70:71], v191 offset:1024\n"
"v_mfma_f32_16x16x32_bf16 v[56:59], a[92:95], a[48:51], v[56:59]\n"
"ds_read_b128 a[12:15], v177 offset:34816\n"
"v_mfma_f32_16x16x32_bf16 v[60:63], a[92:95], a[52:55], v[60:63]\n"
"ds_read_b64_tr_b16 a[72:73], v192 offset:0\n"
"buffer_load_dwordx4 v5, s[56:59], s78 offen offset:1024 lds\n"
"v_mfma_f32_16x16x32_bf16 v[28:31], a[88:91], a[52:55], v[28:31]\n"
"v_mfma_f32_16x16x32_bf16 v[32:35], a[88:91], a[56:59], v[32:35]\n"
"ds_read_b128 a[16:19], v178 offset:32768\n"
"v_mfma_f32_16x16x32_bf16 v[64:67], a[92:95], a[56:59], v[64:67]\n"
"ds_read_b64_tr_b16 a[74:75], v192 offset:256\n"
"v_mfma_f32_16x16x32_bf16 v[68:71], a[92:95], a[60:63], v[68:71]\n"
"ds_read_b128 a[20:23], v178 offset:34816\n"
"v_mfma_f32_16x16x32_bf16 v[36:39], a[88:91], a[60:63], v[36:39]\n"
"s_add_u32 m0, s76, 67584\n"
"s_nop 0\n"
"buffer_load_dwordx4 v4, s[56:59], s79 offen lds\n"
"v_mfma_f32_16x16x32_bf16 v[156:159], a[92:95], a[112:115], v[156:159]\n"
"ds_read_b64_tr_b16 a[76:77], v190 offset:9216\n"
"v_mfma_f32_16x16x32_bf16 v[152:155], a[88:91], a[112:115], v[152:155]\n"
"ds_read_b128 a[24:27], v179 offset:32768\n"
"v_mfma_f32_16x16x32_bf16 v[160:163], a[96:99], a[112:115], v[160:163]\n"
"ds_read_b64_tr_b16 a[78:79], v190 offset:10240\n"
"buffer_load_dwordx4 v5, s[56:59], s79 offen offset:1024 lds\n"
"v_mfma_f32_16x16x32_bf16 v[72:75], a[96:99], a[32:35], v[72:75]\n"
"v_lshrrev_b32 v4, 1, v1\n"
"v_mfma_f32_16x16x32_bf16 v[76:79], a[96:99], a[36:39], v[76:79]\n"
"ds_read_b128 a[28:31], v179 offset:34816\n"
"v_mul_u32_u24 v4, 1152, v4\n"
"v_mfma_f32_16x16x32_bf16 v[80:83], a[100:103], a[32:35], v[80:83]\n"
"ds_read_b64_tr_b16 a[80:81], v191 offset:9216\n"
"v_and_b32 v5, 1, v1\n"
"v_mfma_f32_16x16x32_bf16 v[112:115], a[104:107], a[32:35], v[112:115]\n"
"ds_read_b64_tr_b16 a[82:83], v191 offset:10240\n"
"v_lshl_add_u32 v4, v5, 4, v4\n"
"v_mfma_f32_16x16x32_bf16 v[116:119], a[104:107], a[36:39], v[116:119]\n"
"v_or_b32 v4, s13, v4\n"
"s_add_u32 s78, s78, 128\n"
"v_mfma_f32_16x16x32_bf16 v[84:87], a[100:103], a[36:39], v[84:87]\n"
"ds_read_b64_tr_b16 a[84:85], v192 offset:9216\n"
"v_mfma_f32_16x16x32_bf16 v[88:91], a[100:103], a[40:43], v[88:91]\n"
"ds_read_b64_tr_b16 a[86:87], v192 offset:9472\n"
"v_mfma_f32_16x16x32_bf16 v[120:123], a[104:107], a[40:43], v[120:123]\n"
"s_add_u32 m0, s76, 69632\n"
"s_nop 0\n"
"buffer_load_dwordx4 v4, s[56:59], s78 offen lds\n"
"v_mfma_f32_16x16x32_bf16 v[124:127], a[104:107], a[44:47], v[124:127]\n"
"v_mfma_f32_16x16x32_bf16 v[92:95], a[100:103], a[44:47], v[92:95]\n"
"v_mfma_f32_16x16x32_bf16 v[96:99], a[100:103], a[48:51], v[96:99]\n"
"v_mfma_f32_16x16x32_bf16 v[128:131], a[104:107], a[48:51], v[128:131]\n"
"v_mfma_f32_16x16x32_bf16 v[132:135], a[104:107], a[52:55], v[132:135]\n"
"v_mfma_f32_16x16x32_bf16 v[100:103], a[100:103], a[52:55], v[100:103]\n"
"v_mfma_f32_16x16x32_bf16 v[104:107], a[100:103], a[56:59], v[104:107]\n"
"v_mfma_f32_16x16x32_bf16 v[136:139], a[104:107], a[56:59], v[136:139]\n"
"v_mfma_f32_16x16x32_bf16 v[140:143], a[104:107], a[60:63], v[140:143]\n"
"v_mfma_f32_16x16x32_bf16 v[108:111], a[100:103], a[60:63], v[108:111]\n"
"v_mfma_f32_16x16x32_bf16 v[168:171], a[104:107], a[112:115], v[168:171]\n"
"v_mfma_f32_16x16x32_bf16 v[164:167], a[100:103], a[112:115], v[164:167]\n"
"v_mfma_f32_16x16x32_bf16 v[172:175], a[108:111], a[112:115], v[172:175]\n"
"v_mfma_f32_16x16x32_bf16 v[144:147], a[108:111], a[32:35], v[144:147]\n"
"v_mfma_f32_16x16x32_bf16 v[148:151], a[108:111], a[36:39], v[148:151]\n"
"s_waitcnt lgkmcnt(0)\n"
"v_mfma_f32_16x16x32_bf16 v[8:11], a[64:67], a[0:3], v[8:11]\n"
"ds_read_b64_tr_b16 a[88:89], v190 offset:18432\n"
"s_waitcnt vmcnt(13)\n"
"v_mfma_f32_16x16x32_bf16 v[40:43], a[68:71], a[0:3], v[40:43]\n"
"ds_read_b128 a[32:35], v180 offset:32768\n"
"s_and_b32 s71, s64, 1\n"
"v_mfma_f32_16x16x32_bf16 v[44:47], a[68:71], a[4:7], v[44:47]\n"
"ds_read_b64_tr_b16 a[90:91], v190 offset:19456\n"
"s_lshl_b32 s71, s71, 2\n"
"v_mfma_f32_16x16x32_bf16 v[12:15], a[64:67], a[4:7], v[12:15]\n"
"s_bfe_u32 s72, s64, 0x20001\n"
"s_add_u32 s71, s71, s72\n"
"v_mfma_f32_16x16x32_bf16 v[16:19], a[64:67], a[8:11], v[16:19]\n"
"ds_read_b128 a[36:39], v180 offset:34816\n"
"s_lshl_b32 s71, s71, 5\n"
"v_mfma_f32_16x16x32_bf16 v[48:51], a[68:71], a[8:11], v[48:51]\n"
"ds_read_b64_tr_b16 a[92:93], v191 offset:18432\n"
"s_lshr_b32 s72, s64, 3\n"
"v_mfma_f32_16x16x32_bf16 v[52:55], a[68:71], a[12:15], v[52:55]\n"
"ds_read_b128 a[40:43], v181 offset:32768\n"
"s_add_u32 s71, s71, s72\n"
"v_mfma_f32_16x16x32_bf16 v[20:23], a[64:67], a[12:15], v[20:23]\n"
"s_lshl_b32 s71, s71, 15\n"
"v_lshlrev_b32 v202, 7, v0\n"
"v_mfma_f32_16x16x32_bf16 v[24:27], a[64:67], a[16:19], v[24:27]\n"
"ds_read_b64_tr_b16 a[94:95], v191 offset:19456\n"
"buffer_load_dword a249, v202, s[48:51], s71 offen\n"
"v_mfma_f32_16x16x32_bf16 v[56:59], a[68:71], a[16:19], v[56:59]\n"
"ds_read_b128 a[44:47], v181 offset:34816\n"
"v_xor_b32 v6, s68, v209\n"
"v_mfma_f32_16x16x32_bf16 v[60:63], a[68:71], a[20:23], v[60:63]\n"
"ds_read_b64_tr_b16 a[96:97], v192 offset:18432\n"
"v_xor_b32 v7, s68, v213\n"
"v_mfma_f32_16x16x32_bf16 v[28:31], a[64:67], a[20:23], v[28:31]\n"
"v_or_b32 v6, v6, v7\n"
"v_xor_b32 v7, s68, v217\n"
"v_mfma_f32_16x16x32_bf16 v[32:35], a[64:67], a[24:27], v[32:35]\n"
"ds_read_b128 a[48:51], v182 offset:32768\n"
"v_or_b32 v6, v6, v7\n"
"v_mfma_f32_16x16x32_bf16 v[64:67], a[68:71], a[24:27], v[64:67]\n"
"ds_read_b64_tr_b16 a[98:99], v192 offset:18688\n"
"v_xor_b32 v7, s68, v221\n"
"v_mfma_f32_16x16x32_bf16 v[68:71], a[68:71], a[28:31], v[68:71]\n"
"ds_read_b128 a[52:55], v182 offset:34816\n"
"v_or_b32 v6, v6, v7\n"
"v_mfma_f32_16x16x32_bf16 v[36:39], a[64:67], a[28:31], v[36:39]\n"
"v_xor_b32 v7, s68, v225\n"
"v_mfma_f32_16x16x32_bf16 v[156:159], a[68:71], a[112:115], v[156:159]\n"
"ds_read_b64_tr_b16 a[100:101], v190 offset:27648\n"
"v_or_b32 v6, v6, v7\n"
"v_mfma_f32_16x16x32_bf16 v[152:155], a[64:67], a[112:115], v[152:155]\n"
"ds_read_b128 a[56:59], v183 offset:32768\n"
"v_xor_b32 v7, s68, v229\n"
"v_mfma_f32_16x16x32_bf16 v[160:163], a[72:75], a[112:115], v[160:163]\n"
"ds_read_b64_tr_b16 a[102:103], v190 offset:28672\n"
"v_or_b32 v6, v6, v7\n"
"v_mfma_f32_16x16x32_bf16 v[72:75], a[72:75], a[0:3], v[72:75]\n"
"v_xor_b32 v7, s68, v233\n"
"v_mfma_f32_16x16x32_bf16 v[76:79], a[72:75], a[4:7], v[76:79]\n"
"ds_read_b128 a[60:63], v183 offset:34816\n"
"v_or_b32 v6, v6, v7\n"
"v_mfma_f32_16x16x32_bf16 v[80:83], a[76:79], a[0:3], v[80:83]\n"
"ds_read_b64_tr_b16 a[104:105], v191 offset:27648\n"
"v_xor_b32 v7, s68, v237\n"
"v_mfma_f32_16x16x32_bf16 v[112:115], a[80:83], a[0:3], v[112:115]\n"
"ds_read_b64_tr_b16 a[106:107], v191 offset:28672\n"
"v_or_b32 v6, v6, v7\n"
"v_mfma_f32_16x16x32_bf16 v[116:119], a[80:83], a[4:7], v[116:119]\n"
"v_xor_b32 v7, s69, v211\n"
"v_mfma_f32_16x16x32_bf16 v[84:87], a[76:79], a[4:7], v[84:87]\n"
"ds_read_b64_tr_b16 a[108:109], v192 offset:27648\n"
"v_or_b32 v6, v6, v7\n"
"v_mfma_f32_16x16x32_bf16 v[88:91], a[76:79], a[8:11], v[88:91]\n"
"ds_read_b64_tr_b16 a[110:111], v192 offset:27904\n"
"v_xor_b32 v7, s69, v215\n"
"v_mfma_f32_16x16x32_bf16 v[120:123], a[80:83], a[8:11], v[120:123]\n"
"v_or_b32 v6, v6, v7\n"
"v_xor_b32 v7, s69, v219\n"
"v_mfma_f32_16x16x32_bf16 v[124:127], a[80:83], a[12:15], v[124:127]\n"
"v_or_b32 v6, v6, v7\n"
"v_xor_b32 v7, s69, v223\n"
"v_mfma_f32_16x16x32_bf16 v[92:95], a[76:79], a[12:15], v[92:95]\n"
"v_or_b32 v6, v6, v7\n"
"v_xor_b32 v7, s69, v227\n"
"v_mfma_f32_16x16x32_bf16 v[96:99], a[76:79], a[16:19], v[96:99]\n"
"v_or_b32 v6, v6, v7\n"
"v_xor_b32 v7, s69, v231\n"
"v_mfma_f32_16x16x32_bf16 v[128:131], a[80:83], a[16:19], v[128:131]\n"
"v_or_b32 v6, v6, v7\n"
"v_xor_b32 v7, s69, v235\n"
"v_mfma_f32_16x16x32_bf16 v[132:135], a[80:83], a[20:23], v[132:135]\n"
"v_or_b32 v6, v6, v7\n"
"v_xor_b32 v7, s69, v239\n"
"v_mfma_f32_16x16x32_bf16 v[100:103], a[76:79], a[20:23], v[100:103]\n"
"v_or_b32 v6, v6, v7\n"
"v_mfma_f32_16x16x32_bf16 v[104:107], a[76:79], a[24:27], v[104:107]\n"
"v_rcp_f32 v211, v210\n"
"v_mfma_f32_16x16x32_bf16 v[136:139], a[80:83], a[24:27], v[136:139]\n"
"v_rcp_f32 v215, v214\n"
"v_mfma_f32_16x16x32_bf16 v[140:143], a[80:83], a[28:31], v[140:143]\n"
"v_rcp_f32 v219, v218\n"
"v_mfma_f32_16x16x32_bf16 v[108:111], a[76:79], a[28:31], v[108:111]\n"
"v_rcp_f32 v223, v222\n"
"v_mfma_f32_16x16x32_bf16 v[168:171], a[80:83], a[112:115], v[168:171]\n"
"v_rcp_f32 v227, v226\n"
"v_mfma_f32_16x16x32_bf16 v[164:167], a[76:79], a[112:115], v[164:167]\n"
"v_rcp_f32 v231, v230\n"
"v_mfma_f32_16x16x32_bf16 v[172:175], a[84:87], a[112:115], v[172:175]\n"
"v_rcp_f32 v235, v234\n"
"v_mfma_f32_16x16x32_bf16 v[144:147], a[84:87], a[0:3], v[144:147]\n"
"v_rcp_f32 v239, v238\n"
"v_mfma_f32_16x16x32_bf16 v[148:151], a[84:87], a[4:7], v[148:151]\n"
"v_sub_f32 v208, 0, v208\n"
"v_sub_f32 v212, 0, v212\n"
"s_waitcnt lgkmcnt(0)\n"
"v_mfma_f32_16x16x32_bf16 v[8:11], a[88:91], a[32:35], v[8:11]\n"
"v_sub_f32 v216, 0, v216\n"
"v_sub_f32 v220, 0, v220\n"
"v_mfma_f32_16x16x32_bf16 v[40:43], a[92:95], a[32:35], v[40:43]\n"
"v_sub_f32 v224, 0, v224\n"
"v_sub_f32 v228, 0, v228\n"
"v_mfma_f32_16x16x32_bf16 v[44:47], a[92:95], a[36:39], v[44:47]\n"
"v_sub_f32 v232, 0, v232\n"
"v_sub_f32 v236, 0, v236\n"
"v_mfma_f32_16x16x32_bf16 v[12:15], a[88:91], a[36:39], v[12:15]\n"
"v_mul_f32 v209, 0xc0135761, v210\n"
"v_mul_f32 v213, 0xc0135761, v214\n"
"v_mfma_f32_16x16x32_bf16 v[16:19], a[88:91], a[40:43], v[16:19]\n"
"v_mul_f32 v217, 0xc0135761, v218\n"
"v_mul_f32 v221, 0xc0135761, v222\n"
"v_mfma_f32_16x16x32_bf16 v[48:51], a[92:95], a[40:43], v[48:51]\n"
"v_mul_f32 v225, 0xc0135761, v226\n"
"v_mul_f32 v229, 0xc0135761, v230\n"
"v_mfma_f32_16x16x32_bf16 v[52:55], a[92:95], a[44:47], v[52:55]\n"
"v_mul_f32 v233, 0xc0135761, v234\n"
"v_mul_f32 v237, 0xc0135761, v238\n"
"v_mfma_f32_16x16x32_bf16 v[20:23], a[88:91], a[44:47], v[20:23]\n"
"v_mul_f32 v210, v210, v210\n"
"v_mul_f32 v214, v214, v214\n"
"v_mfma_f32_16x16x32_bf16 v[24:27], a[88:91], a[48:51], v[24:27]\n"
"v_mul_f32 v218, v218, v218\n"
"v_mul_f32 v222, v222, v222\n"
"v_mfma_f32_16x16x32_bf16 v[56:59], a[92:95], a[48:51], v[56:59]\n"
"v_mul_f32 v226, v226, v226\n"
"v_mul_f32 v230, v230, v230\n"
"v_mfma_f32_16x16x32_bf16 v[60:63], a[92:95], a[52:55], v[60:63]\n"
"v_mul_f32 v234, v234, v234\n"
"v_mul_f32 v238, v238, v238\n"
"v_mfma_f32_16x16x32_bf16 v[28:31], a[88:91], a[52:55], v[28:31]\n"
"v_mul_f32 v210, v210, v209\n"
"v_mul_f32 v214, v214, v213\n"
"v_mfma_f32_16x16x32_bf16 v[32:35], a[88:91], a[56:59], v[32:35]\n"
"v_mul_f32 v218, v218, v217\n"
"v_mul_f32 v222, v222, v221\n"
"v_mfma_f32_16x16x32_bf16 v[64:67], a[92:95], a[56:59], v[64:67]\n"
"v_mul_f32 v226, v226, v225\n"
"v_mul_f32 v230, v230, v229\n"
"v_mfma_f32_16x16x32_bf16 v[68:71], a[92:95], a[60:63], v[68:71]\n"
"v_mul_f32 v234, v234, v233\n"
"v_mul_f32 v238, v238, v237\n"
"v_mfma_f32_16x16x32_bf16 v[36:39], a[88:91], a[60:63], v[36:39]\n"
"v_mul_f32 v210, 0x3d372713, v210\n"
"v_mul_f32 v214, 0x3d372713, v214\n"
"v_mfma_f32_16x16x32_bf16 v[156:159], a[92:95], a[112:115], v[156:159]\n"
"v_mul_f32 v218, 0x3d372713, v218\n"
"v_mul_f32 v222, 0x3d372713, v222\n"
"v_mfma_f32_16x16x32_bf16 v[152:155], a[88:91], a[112:115], v[152:155]\n"
"v_mul_f32 v226, 0x3d372713, v226\n"
"v_mul_f32 v230, 0x3d372713, v230\n"
"v_mfma_f32_16x16x32_bf16 v[160:163], a[96:99], a[112:115], v[160:163]\n"
"v_mul_f32 v234, 0x3d372713, v234\n"
"v_mul_f32 v238, 0x3d372713, v238\n"
"v_mfma_f32_16x16x32_bf16 v[72:75], a[96:99], a[32:35], v[72:75]\n"
"v_mfma_f32_16x16x32_bf16 v[76:79], a[96:99], a[36:39], v[76:79]\n"
"v_mfma_f32_16x16x32_bf16 v[80:83], a[100:103], a[32:35], v[80:83]\n"
"v_mfma_f32_16x16x32_bf16 v[112:115], a[104:107], a[32:35], v[112:115]\n"
"v_mfma_f32_16x16x32_bf16 v[116:119], a[104:107], a[36:39], v[116:119]\n"
"v_mfma_f32_16x16x32_bf16 v[84:87], a[100:103], a[36:39], v[84:87]\n"
"v_mfma_f32_16x16x32_bf16 v[88:91], a[100:103], a[40:43], v[88:91]\n"
"v_mfma_f32_16x16x32_bf16 v[120:123], a[104:107], a[40:43], v[120:123]\n"
"v_mfma_f32_16x16x32_bf16 v[124:127], a[104:107], a[44:47], v[124:127]\n"
"v_mfma_f32_16x16x32_bf16 v[92:95], a[100:103], a[44:47], v[92:95]\n"
"v_mfma_f32_16x16x32_bf16 v[96:99], a[100:103], a[48:51], v[96:99]\n"
"v_mfma_f32_16x16x32_bf16 v[128:131], a[104:107], a[48:51], v[128:131]\n"
"v_mfma_f32_16x16x32_bf16 v[132:135], a[104:107], a[52:55], v[132:135]\n"
"v_mfma_f32_16x16x32_bf16 v[100:103], a[100:103], a[52:55], v[100:103]\n"
"v_mfma_f32_16x16x32_bf16 v[104:107], a[100:103], a[56:59], v[104:107]\n"
"v_mfma_f32_16x16x32_bf16 v[136:139], a[104:107], a[56:59], v[136:139]\n"
"v_mfma_f32_16x16x32_bf16 v[140:143], a[104:107], a[60:63], v[140:143]\n"
"v_mfma_f32_16x16x32_bf16 v[108:111], a[100:103], a[60:63], v[108:111]\n"
"v_mfma_f32_16x16x32_bf16 v[168:171], a[104:107], a[112:115], v[168:171]\n"
"v_mfma_f32_16x16x32_bf16 v[164:167], a[100:103], a[112:115], v[164:167]\n"
"v_mfma_f32_16x16x32_bf16 v[172:175], a[108:111], a[112:115], v[172:175]\n"
"v_mfma_f32_16x16x32_bf16 v[144:147], a[108:111], a[32:35], v[144:147]\n"
"v_mfma_f32_16x16x32_bf16 v[148:151], a[108:111], a[36:39], v[148:151]\n"
"v_cmp_ne_u32 vcc, 0, v6\n"
"s_cbranch_vccz xgok_%=\n"
"xgre_%=:\n"
"s_sub_u32 s70, s70, 1\n"
"s_cbranch_scc1 xgto_%=\n"
"s_sleep 1\n"
"buffer_load_dwordx4 v[208:211], v244, s[36:39], s12 offen offset:0 sc1\n"
"buffer_load_dwordx4 v[212:215], v244, s[36:39], s12 offen offset:256 sc1\n"
"buffer_load_dwordx4 v[216:219], v245, s[36:39], s12 offen offset:0 sc1\n"
"buffer_load_dwordx4 v[220:223], v245, s[36:39], s12 offen offset:256 sc1\n"
"buffer_load_dwordx4 v[224:227], v246, s[36:39], s12 offen offset:0 sc1\n"
"buffer_load_dwordx4 v[228:231], v246, s[36:39], s12 offen offset:256 sc1\n"
"buffer_load_dwordx4 v[232:235], v247, s[36:39], s12 offen offset:0 sc1\n"
"buffer_load_dwordx4 v[236:239], v247, s[36:39], s12 offen offset:256 sc1\n"
"s_waitcnt vmcnt(0)\n"
"v_xor_b32 v6, s68, v209\n"
"v_xor_b32 v7, s68, v213\n"
"v_or_b32 v6, v6, v7\n"
"v_xor_b32 v7, s68, v217\n"
"v_or_b32 v6, v6, v7\n"
"v_xor_b32 v7, s68, v221\n"
"v_or_b32 v6, v6, v7\n"
"v_xor_b32 v7, s68, v225\n"
"v_or_b32 v6, v6, v7\n"
"v_xor_b32 v7, s68, v229\n"
"v_or_b32 v6, v6, v7\n"
"v_xor_b32 v7, s68, v233\n"
"v_or_b32 v6, v6, v7\n"
"v_xor_b32 v7, s68, v237\n"
"v_or_b32 v6, v6, v7\n"
"v_xor_b32 v7, s69, v211\n"
"v_or_b32 v6, v6, v7\n"
"v_xor_b32 v7, s69, v215\n"
"v_or_b32 v6, v6, v7\n"
"v_xor_b32 v7, s69, v219\n"
"v_or_b32 v6, v6, v7\n"
"v_xor_b32 v7, s69, v223\n"
"v_or_b32 v6, v6, v7\n"
"v_xor_b32 v7, s69, v227\n"
"v_or_b32 v6, v6, v7\n"
"v_xor_b32 v7, s69, v231\n"
"v_or_b32 v6, v6, v7\n"
"v_xor_b32 v7, s69, v235\n"
"v_or_b32 v6, v6, v7\n"
"v_xor_b32 v7, s69, v239\n"
"v_or_b32 v6, v6, v7\n"
"v_rcp_f32 v211, v210\n"
"v_rcp_f32 v215, v214\n"
"v_rcp_f32 v219, v218\n"
"v_rcp_f32 v223, v222\n"
"v_rcp_f32 v227, v226\n"
"v_rcp_f32 v231, v230\n"
"v_rcp_f32 v235, v234\n"
"v_rcp_f32 v239, v238\n"
"v_sub_f32 v208, 0, v208\n"
"v_sub_f32 v212, 0, v212\n"
"v_sub_f32 v216, 0, v216\n"
"v_sub_f32 v220, 0, v220\n"
"v_sub_f32 v224, 0, v224\n"
"v_sub_f32 v228, 0, v228\n"
"v_sub_f32 v232, 0, v232\n"
"v_sub_f32 v236, 0, v236\n"
"v_mul_f32 v209, 0xc0135761, v210\n"
"v_mul_f32 v213, 0xc0135761, v214\n"
"v_mul_f32 v217, 0xc0135761, v218\n"
"v_mul_f32 v221, 0xc0135761, v222\n"
"v_mul_f32 v225, 0xc0135761, v226\n"
"v_mul_f32 v229, 0xc0135761, v230\n"
"v_mul_f32 v233, 0xc0135761, v234\n"
"v_mul_f32 v237, 0xc0135761, v238\n"
"v_mul_f32 v210, v210, v210\n"
"v_mul_f32 v214, v214, v214\n"
"v_mul_f32 v218, v218, v218\n"
"v_mul_f32 v222, v222, v222\n"
"v_mul_f32 v226, v226, v226\n"
"v_mul_f32 v230, v230, v230\n"
"v_mul_f32 v234, v234, v234\n"
"v_mul_f32 v238, v238, v238\n"
"v_mul_f32 v210, v210, v209\n"
"v_mul_f32 v214, v214, v213\n"
"v_mul_f32 v218, v218, v217\n"
"v_mul_f32 v222, v222, v221\n"
"v_mul_f32 v226, v226, v225\n"
"v_mul_f32 v230, v230, v229\n"
"v_mul_f32 v234, v234, v233\n"
"v_mul_f32 v238, v238, v237\n"
"v_mul_f32 v210, 0x3d372713, v210\n"
"v_mul_f32 v214, 0x3d372713, v214\n"
"v_mul_f32 v218, 0x3d372713, v218\n"
"v_mul_f32 v222, 0x3d372713, v222\n"
"v_mul_f32 v226, 0x3d372713, v226\n"
"v_mul_f32 v230, 0x3d372713, v230\n"
"v_mul_f32 v234, 0x3d372713, v234\n"
"v_mul_f32 v238, 0x3d372713, v238\n"
"v_cmp_ne_u32 vcc, 0, v6\n"
"s_cbranch_vccnz xgre_%=\n"
"s_branch xgok_%=\n"
"xgto_%=:\n"
"s_mov_b32 s70, 0\n"
"xgok_%=:\n"
"s_waitcnt vmcnt(11)\n"
"poll1c_%=:\n"
"v_xor_b32 v241, s68, v205\n"
"v_xor_b32 v242, s69, v207\n"
"v_or_b32 v240, v241, v242\n"
"v_cmp_ne_u32 vcc, 0, v240\n"
"s_cbranch_vccz poll1d_%=\n"
"s_sub_u32 s70, s70, 1\n"
"s_cbranch_scc1 poll1t_%=\n"
"s_sleep 2\n"
"buffer_load_dwordx4 v[204:207], v251, s[36:39], s17 offen sc1\n"
"s_waitcnt vmcnt(0)\n"
"s_branch poll1c_%=\n"
"poll1t_%=:\n"
"s_mov_b32 s70, 0\n"
"poll1d_%=:\n"
"s_nop 4\n"
"v_add_f32_dpp v152, v204, v152 row_newbcast:0 row_mask:0xf bank_mask:0xf\n"
"v_add_f32_dpp v156, v204, v156 row_newbcast:2 row_mask:0xf bank_mask:0xf\n"
"v_add_f32_dpp v160, v204, v160 row_newbcast:4 row_mask:0xf bank_mask:0xf\n"
"v_add_f32_dpp v164, v204, v164 row_newbcast:6 row_mask:0xf bank_mask:0xf\n"
"v_add_f32_dpp v168, v204, v168 row_newbcast:8 row_mask:0xf bank_mask:0xf\n"
"v_add_f32_dpp v172, v204, v172 row_newbcast:10 row_mask:0xf bank_mask:0xf\n"
"v_add_f32_dpp v153, v206, v153 row_newbcast:0 row_mask:0xf bank_mask:0xf\n"
"v_add_f32_dpp v157, v206, v157 row_newbcast:2 row_mask:0xf bank_mask:0xf\n"
"v_add_f32_dpp v161, v206, v161 row_newbcast:4 row_mask:0xf bank_mask:0xf\n"
"v_add_f32_dpp v165, v206, v165 row_newbcast:6 row_mask:0xf bank_mask:0xf\n"
"v_add_f32_dpp v169, v206, v169 row_newbcast:8 row_mask:0xf bank_mask:0xf\n"
"v_add_f32_dpp v173, v206, v173 row_newbcast:10 row_mask:0xf bank_mask:0xf\n"
"v_add_f32_dpp v154, v204, v154 row_newbcast:1 row_mask:0xf bank_mask:0xf\n"
"v_add_f32_dpp v158, v204, v158 row_newbcast:3 row_mask:0xf bank_mask:0xf\n"
"v_add_f32_dpp v162, v204, v162 row_newbcast:5 row_mask:0xf bank_mask:0xf\n"
"v_add_f32_dpp v166, v204, v166 row_newbcast:7 row_mask:0xf bank_mask:0xf\n"
"v_add_f32_dpp v170, v204, v170 row_newbcast:9 row_mask:0xf bank_mask:0xf\n"
"v_add_f32_dpp v174, v204, v174 row_newbcast:11 row_mask:0xf bank_mask:0xf\n"
"v_add_f32_dpp v155, v206, v155 row_newbcast:1 row_mask:0xf bank_mask:0xf\n"
"v_add_f32_dpp v159, v206, v159 row_newbcast:3 row_mask:0xf bank_mask:0xf\n"
"v_add_f32_dpp v163, v206, v163 row_newbcast:5 row_mask:0xf bank_mask:0xf\n"
"v_add_f32_dpp v167, v206, v167 row_newbcast:7 row_mask:0xf bank_mask:0xf\n"
"v_add_f32_dpp v171, v206, v171 row_newbcast:9 row_mask:0xf bank_mask:0xf\n"
"v_add_f32_dpp v175, v206, v175 row_newbcast:11 row_mask:0xf bank_mask:0xf\n"
"v_pk_fma_f32 v[8:9], v[152:153], v[208:209], v[8:9] op_sel_hi:[1,0,1]\n"
"v_pk_fma_f32 v[10:11], v[154:155], v[208:209], v[10:11] op_sel_hi:[1,0,1]\n"
"v_pk_fma_f32 v[40:41], v[156:157], v[208:209], v[40:41] op_sel_hi:[1,0,1]\n"
"v_pk_fma_f32 v[42:43], v[158:159], v[208:209], v[42:43] op_sel_hi:[1,0,1]\n"
"v_pk_fma_f32 v[72:73], v[160:161], v[208:209], v[72:73] op_sel_hi:[1,0,1]\n"
"v_pk_fma_f32 v[74:75], v[162:163], v[208:209], v[74:75] op_sel_hi:[1,0,1]\n"
"v_pk_fma_f32 v[12:13], v[152:153], v[212:213], v[12:13] op_sel_hi:[1,0,1]\n"
"v_pk_fma_f32 v[14:15], v[154:155], v[212:213], v[14:15] op_sel_hi:[1,0,1]\n"
"v_pk_fma_f32 v[44:45], v[156:157], v[212:213], v[44:45] op_sel_hi:[1,0,1]\n"
"v_pk_fma_f32 v[46:47], v[158:159], v[212:213], v[46:47] op_sel_hi:[1,0,1]\n"
"v_pk_fma_f32 v[76:77], v[160:161], v[212:213], v[76:77] op_sel_hi:[1,0,1]\n"
"v_pk_fma_f32 v[78:79], v[162:163], v[212:213], v[78:79] op_sel_hi:[1,0,1]\n"
"v_pk_fma_f32 v[16:17], v[152:153], v[216:217], v[16:17] op_sel_hi:[1,0,1]\n"
"v_pk_fma_f32 v[18:19], v[154:155], v[216:217], v[18:19] op_sel_hi:[1,0,1]\n"
"v_pk_fma_f32 v[48:49], v[156:157], v[216:217], v[48:49] op_sel_hi:[1,0,1]\n"
"v_pk_fma_f32 v[50:51], v[158:159], v[216:217], v[50:51] op_sel_hi:[1,0,1]\n"
"v_pk_fma_f32 v[20:21], v[152:153], v[220:221], v[20:21] op_sel_hi:[1,0,1]\n"
"v_pk_fma_f32 v[22:23], v[154:155], v[220:221], v[22:23] op_sel_hi:[1,0,1]\n"
"v_pk_fma_f32 v[52:53], v[156:157], v[220:221], v[52:53] op_sel_hi:[1,0,1]\n"
"v_pk_fma_f32 v[54:55], v[158:159], v[220:221], v[54:55] op_sel_hi:[1,0,1]\n"
"v_pk_fma_f32 v[24:25], v[152:153], v[224:225], v[24:25] op_sel_hi:[1,0,1]\n"
"v_pk_fma_f32 v[26:27], v[154:155], v[224:225], v[26:27] op_sel_hi:[1,0,1]\n"
"v_pk_fma_f32 v[56:57], v[156:157], v[224:225], v[56:57] op_sel_hi:[1,0,1]\n"
"v_pk_fma_f32 v[58:59], v[158:159], v[224:225], v[58:59] op_sel_hi:[1,0,1]\n"
"v_pk_fma_f32 v[28:29], v[152:153], v[228:229], v[28:29] op_sel_hi:[1,0,1]\n"
"v_pk_fma_f32 v[30:31], v[154:155], v[228:229], v[30:31] op_sel_hi:[1,0,1]\n"
"v_pk_fma_f32 v[60:61], v[156:157], v[228:229], v[60:61] op_sel_hi:[1,0,1]\n"
"v_pk_fma_f32 v[62:63], v[158:159], v[228:229], v[62:63] op_sel_hi:[1,0,1]\n"
"v_pk_fma_f32 v[32:33], v[152:153], v[232:233], v[32:33] op_sel_hi:[1,0,1]\n"
"v_pk_fma_f32 v[34:35], v[154:155], v[232:233], v[34:35] op_sel_hi:[1,0,1]\n"
"v_pk_fma_f32 v[64:65], v[156:157], v[232:233], v[64:65] op_sel_hi:[1,0,1]\n"
"v_pk_fma_f32 v[66:67], v[158:159], v[232:233], v[66:67] op_sel_hi:[1,0,1]\n"
"v_pk_fma_f32 v[36:37], v[152:153], v[236:237], v[36:37] op_sel_hi:[1,0,1]\n"
"v_pk_fma_f32 v[38:39], v[154:155], v[236:237], v[38:39] op_sel_hi:[1,0,1]\n"
"v_pk_fma_f32 v[68:69], v[156:157], v[236:237], v[68:69] op_sel_hi:[1,0,1]\n"
"v_pk_fma_f32 v[70:71], v[158:159], v[236:237], v[70:71] op_sel_hi:[1,0,1]\n"
"v_pk_fma_f32 v[80:81], v[164:165], v[208:209], v[80:81] op_sel_hi:[1,0,1]\n"
"v_pk_fma_f32 v[82:83], v[166:167], v[208:209], v[82:83] op_sel_hi:[1,0,1]\n"
"v_pk_fma_f32 v[112:113], v[168:169], v[208:209], v[112:113] op_sel_hi:[1,0,1]\n"
"v_pk_fma_f32 v[114:115], v[170:171], v[208:209], v[114:115] op_sel_hi:[1,0,1]\n"
"v_pk_fma_f32 v[144:145], v[172:173], v[208:209], v[144:145] op_sel_hi:[1,0,1]\n"
"v_pk_fma_f32 v[146:147], v[174:175], v[208:209], v[146:147] op_sel_hi:[1,0,1]\n"
"v_pk_fma_f32 v[84:85], v[164:165], v[212:213], v[84:85] op_sel_hi:[1,0,1]\n"
"v_pk_fma_f32 v[86:87], v[166:167], v[212:213], v[86:87] op_sel_hi:[1,0,1]\n"
"v_pk_fma_f32 v[116:117], v[168:169], v[212:213], v[116:117] op_sel_hi:[1,0,1]\n"
"v_pk_fma_f32 v[118:119], v[170:171], v[212:213], v[118:119] op_sel_hi:[1,0,1]\n"
"v_pk_fma_f32 v[148:149], v[172:173], v[212:213], v[148:149] op_sel_hi:[1,0,1]\n"
"v_pk_fma_f32 v[150:151], v[174:175], v[212:213], v[150:151] op_sel_hi:[1,0,1]\n"
"v_pk_fma_f32 v[88:89], v[164:165], v[216:217], v[88:89] op_sel_hi:[1,0,1]\n"
"v_pk_fma_f32 v[90:91], v[166:167], v[216:217], v[90:91] op_sel_hi:[1,0,1]\n"
"v_pk_fma_f32 v[120:121], v[168:169], v[216:217], v[120:121] op_sel_hi:[1,0,1]\n"
"v_pk_fma_f32 v[122:123], v[170:171], v[216:217], v[122:123] op_sel_hi:[1,0,1]\n"
"v_pk_fma_f32 v[92:93], v[164:165], v[220:221], v[92:93] op_sel_hi:[1,0,1]\n"
"v_pk_fma_f32 v[94:95], v[166:167], v[220:221], v[94:95] op_sel_hi:[1,0,1]\n"
"v_pk_fma_f32 v[124:125], v[168:169], v[220:221], v[124:125] op_sel_hi:[1,0,1]\n"
"v_pk_fma_f32 v[126:127], v[170:171], v[220:221], v[126:127] op_sel_hi:[1,0,1]\n"
"v_pk_fma_f32 v[96:97], v[164:165], v[224:225], v[96:97] op_sel_hi:[1,0,1]\n"
"v_pk_fma_f32 v[98:99], v[166:167], v[224:225], v[98:99] op_sel_hi:[1,0,1]\n"
"v_pk_fma_f32 v[128:129], v[168:169], v[224:225], v[128:129] op_sel_hi:[1,0,1]\n"
"v_pk_fma_f32 v[130:131], v[170:171], v[224:225], v[130:131] op_sel_hi:[1,0,1]\n"
"v_pk_fma_f32 v[100:101], v[164:165], v[228:229], v[100:101] op_sel_hi:[1,0,1]\n"
"v_pk_fma_f32 v[102:103], v[166:167], v[228:229], v[102:103] op_sel_hi:[1,0,1]\n"
"v_pk_fma_f32 v[132:133], v[168:169], v[228:229], v[132:133] op_sel_hi:[1,0,1]\n"
"v_pk_fma_f32 v[134:135], v[170:171], v[228:229], v[134:135] op_sel_hi:[1,0,1]\n"
"v_pk_fma_f32 v[104:105], v[164:165], v[232:233], v[104:105] op_sel_hi:[1,0,1]\n"
"v_pk_fma_f32 v[106:107], v[166:167], v[232:233], v[106:107] op_sel_hi:[1,0,1]\n"
"v_pk_fma_f32 v[136:137], v[168:169], v[232:233], v[136:137] op_sel_hi:[1,0,1]\n"
"v_pk_fma_f32 v[138:139], v[170:171], v[232:233], v[138:139] op_sel_hi:[1,0,1]\n"
"v_pk_fma_f32 v[108:109], v[164:165], v[236:237], v[108:109] op_sel_hi:[1,0,1]\n"
"v_pk_fma_f32 v[110:111], v[166:167], v[236:237], v[110:111] op_sel_hi:[1,0,1]\n"
"v_pk_fma_f32 v[140:141], v[168:169], v[236:237], v[140:141] op_sel_hi:[1,0,1]\n"
"v_pk_fma_f32 v[142:143], v[170:171], v[236:237], v[142:143] op_sel_hi:[1,0,1]\n"
"v_pk_mul_f32 v[240:241], v[8:9], v[8:9]\n"
"v_pk_mul_f32 v[242:243], v[10:11], v[10:11]\n"
"v_pk_mul_f32 v[244:245], v[40:41], v[40:41]\n"
"v_pk_mul_f32 v[246:247], v[42:43], v[42:43]\n"
"v_pk_mul_f32 v[248:249], v[72:73], v[72:73]\n"
"v_pk_mul_f32 v[250:251], v[74:75], v[74:75]\n"
"v_pk_fma_f32 v[240:241], v[240:241], v[210:211], v[208:209] op_sel:[0,0,1] op_sel_hi:[1,0,1]\n"
"v_pk_fma_f32 v[242:243], v[242:243], v[210:211], v[208:209] op_sel:[0,0,1] op_sel_hi:[1,0,1]\n"
"v_pk_fma_f32 v[244:245], v[244:245], v[210:211], v[208:209] op_sel:[0,0,1] op_sel_hi:[1,0,1]\n"
"v_pk_fma_f32 v[246:247], v[246:247], v[210:211], v[208:209] op_sel:[0,0,1] op_sel_hi:[1,0,1]\n"
"v_pk_fma_f32 v[248:249], v[248:249], v[210:211], v[208:209] op_sel:[0,0,1] op_sel_hi:[1,0,1]\n"
"v_pk_fma_f32 v[250:251], v[250:251], v[210:211], v[208:209] op_sel:[0,0,1] op_sel_hi:[1,0,1]\n"
"v_pk_mul_f32 v[240:241], v[8:9], v[240:241]\n"
"v_pk_mul_f32 v[242:243], v[10:11], v[242:243]\n"
"v_pk_mul_f32 v[244:245], v[40:41], v[244:245]\n"
"v_pk_mul_f32 v[246:247], v[42:43], v[246:247]\n"
"v_pk_mul_f32 v[248:249], v[72:73], v[248:249]\n"
"v_pk_mul_f32 v[250:251], v[74:75], v[250:251]\n"
"v_exp_f32 v240, v240\n"
"v_exp_f32 v241, v241\n"
"s_getpc_b64 s[76:77]\n"
"ct1_%=:\n"
"s_and_b32 s76, s76, 0xffffff80\n"
"s_lshr_b32 s71, s64, 3\n"
"s_lshl_b32 s71, s71, 10\n"
"s_lshl_b32 s72, s65, 8\n"
"s_add_u32 s71, s71, s72\n"
"s_add_u32 s71, s71, 6144\n"
"v_lshrrev_b32 v2, 5, v1\n"
"v_lshl_add_u32 v2, v2, 7, s71\n"
"s_mov_b32 s72, kend_%=-ct1_%=-4\n"
"v_min_u32 v2, s72, v2\n"
"global_load_dword a253, v2, s[76:77]\n"
"v_exp_f32 v242, v242\n"
"v_exp_f32 v243, v243\n"
"v_exp_f32 v244, v244\n"
"v_exp_f32 v245, v245\n"
"v_exp_f32 v246, v246\n"
"v_exp_f32 v247, v247\n"
"v_exp_f32 v248, v248\n"
"v_exp_f32 v249, v249\n"
"v_exp_f32 v250, v250\n"
"v_exp_f32 v251, v251\n"
"v_pk_fma_f32 v[240:241], v[240:241], v[210:211], v[210:211] op_sel:[0,1,1] op_sel_hi:[1,1,1]\n"
"v_pk_fma_f32 v[242:243], v[242:243], v[210:211], v[210:211] op_sel:[0,1,1] op_sel_hi:[1,1,1]\n"
"v_pk_fma_f32 v[244:245], v[244:245], v[210:211], v[210:211] op_sel:[0,1,1] op_sel_hi:[1,1,1]\n"
"v_pk_fma_f32 v[246:247], v[246:247], v[210:211], v[210:211] op_sel:[0,1,1] op_sel_hi:[1,1,1]\n"
"v_pk_fma_f32 v[248:249], v[248:249], v[210:211], v[210:211] op_sel:[0,1,1] op_sel_hi:[1,1,1]\n"
"v_pk_fma_f32 v[250:251], v[250:251], v[210:211], v[210:211] op_sel:[0,1,1] op_sel_hi:[1,1,1]\n"
"v_rcp_f32 v240, v240\n"
"v_rcp_f32 v241, v241\n"
"v_rcp_f32 v242, v242\n"
"v_rcp_f32 v243, v243\n"
"v_rcp_f32 v244, v244\n"
"v_rcp_f32 v245, v245\n"
"v_rcp_f32 v246, v246\n"
"v_rcp_f32 v247, v247\n"
"v_rcp_f32 v248, v248\n"
"v_rcp_f32 v249, v249\n"
"v_rcp_f32 v250, v250\n"
"v_rcp_f32 v251, v251\n"
"v_pk_mul_f32 v[8:9], v[8:9], v[240:241]\n"
"v_pk_mul_f32 v[10:11], v[10:11], v[242:243]\n"
"v_pk_mul_f32 v[40:41], v[40:41], v[244:245]\n"
"v_pk_mul_f32 v[42:43], v[42:43], v[246:247]\n"
"v_pk_mul_f32 v[72:73], v[72:73], v[248:249]\n"
"v_pk_mul_f32 v[74:75], v[74:75], v[250:251]\n"
"v_pk_add_f32 v[176:177], v[8:9], v[10:11]\n"
"v_pk_mul_f32 v[178:179], v[8:9], v[8:9]\n"
"v_pk_fma_f32 v[178:179], v[10:11], v[10:11], v[178:179]\n"
"v_pk_add_f32 v[176:177], v[176:177], v[40:41]\n"
"v_pk_fma_f32 v[178:179], v[40:41], v[40:41], v[178:179]\n"
"v_pk_add_f32 v[176:177], v[176:177], v[42:43]\n"
"v_pk_fma_f32 v[178:179], v[42:43], v[42:43], v[178:179]\n"
"v_pk_add_f32 v[176:177], v[176:177], v[72:73]\n"
"v_pk_fma_f32 v[178:179], v[72:73], v[72:73], v[178:179]\n"
"v_pk_add_f32 v[176:177], v[176:177], v[74:75]\n"
"v_pk_fma_f32 v[178:179], v[74:75], v[74:75], v[178:179]\n"
"v_pk_mul_f32 v[240:241], v[12:13], v[12:13]\n"
"v_pk_mul_f32 v[242:243], v[14:15], v[14:15]\n"
"v_pk_mul_f32 v[244:245], v[44:45], v[44:45]\n"
"v_pk_mul_f32 v[246:247], v[46:47], v[46:47]\n"
"v_pk_mul_f32 v[248:249], v[76:77], v[76:77]\n"
"v_pk_mul_f32 v[250:251], v[78:79], v[78:79]\n"
"v_pk_fma_f32 v[240:241], v[240:241], v[214:215], v[212:213] op_sel:[0,0,1] op_sel_hi:[1,0,1]\n"
"v_pk_fma_f32 v[242:243], v[242:243], v[214:215], v[212:213] op_sel:[0,0,1] op_sel_hi:[1,0,1]\n"
"v_pk_fma_f32 v[244:245], v[244:245], v[214:215], v[212:213] op_sel:[0,0,1] op_sel_hi:[1,0,1]\n"
"v_pk_fma_f32 v[246:247], v[246:247], v[214:215], v[212:213] op_sel:[0,0,1] op_sel_hi:[1,0,1]\n"
"v_pk_fma_f32 v[248:249], v[248:249], v[214:215], v[212:213] op_sel:[0,0,1] op_sel_hi:[1,0,1]\n"
"v_pk_fma_f32 v[250:251], v[250:251], v[214:215], v[212:213] op_sel:[0,0,1] op_sel_hi:[1,0,1]\n"
"v_pk_mul_f32 v[240:241], v[12:13], v[240:241]\n"
"v_pk_mul_f32 v[242:243], v[14:15], v[242:243]\n"
"v_pk_mul_f32 v[244:245], v[44:45], v[244:245]\n"
"v_pk_mul_f32 v[246:247], v[46:47], v[246:247]\n"
"v_pk_mul_f32 v[248:249], v[76:77], v[248:249]\n"
"v_pk_mul_f32 v[250:251], v[78:79], v[250:251]\n"
"v_exp_f32 v240, v240\n"
"v_exp_f32 v241, v241\n"
"v_exp_f32 v242, v242\n"
"v_exp_f32 v243, v243\n"
"v_exp_f32 v244, v244\n"
"v_exp_f32 v245, v245\n"
"v_exp_f32 v246, v246\n"
"v_exp_f32 v247, v247\n"
"v_exp_f32 v248, v248\n"
"v_exp_f32 v249, v249\n"
"v_exp_f32 v250, v250\n"
"v_exp_f32 v251, v251\n"
"v_pk_fma_f32 v[240:241], v[240:241], v[214:215], v[214:215] op_sel:[0,1,1] op_sel_hi:[1,1,1]\n"
"v_pk_fma_f32 v[242:243], v[242:243], v[214:215], v[214:215] op_sel:[0,1,1] op_sel_hi:[1,1,1]\n"
"v_pk_fma_f32 v[244:245], v[244:245], v[214:215], v[214:215] op_sel:[0,1,1] op_sel_hi:[1,1,1]\n"
"v_pk_fma_f32 v[246:247], v[246:247], v[214:215], v[214:215] op_sel:[0,1,1] op_sel_hi:[1,1,1]\n"
"v_pk_fma_f32 v[248:249], v[248:249], v[214:215], v[214:215] op_sel:[0,1,1] op_sel_hi:[1,1,1]\n"
"v_pk_fma_f32 v[250:251], v[250:251], v[214:215], v[214:215] op_sel:[0,1,1] op_sel_hi:[1,1,1]\n"
"v_rcp_f32 v240, v240\n"
"v_rcp_f32 v241, v241\n"
"v_rcp_f32 v242, v242\n"
"v_rcp_f32 v243, v243\n"
"v_rcp_f32 v244, v244\n"
"v_rcp_f32 v245, v245\n"
"v_rcp_f32 v246, v246\n"
"v_rcp_f32 v247, v247\n"
"v_rcp_f32 v248, v248\n"
"v_rcp_f32 v249, v249\n"
"v_rcp_f32 v250, v250\n"
"v_rcp_f32 v251, v251\n"
"v_pk_mul_f32 v[12:13], v[12:13], v[240:241]\n"
"v_pk_mul_f32 v[14:15], v[14:15], v[242:243]\n"
"v_pk_mul_f32 v[44:45], v[44:45], v[244:245]\n"
"v_pk_mul_f32 v[46:47], v[46:47], v[246:247]\n"
"v_pk_mul_f32 v[76:77], v[76:77], v[248:249]\n"
"v_pk_mul_f32 v[78:79], v[78:79], v[250:251]\n"
"v_pk_add_f32 v[180:181], v[12:13], v[14:15]\n"
"v_lshrrev_b32 v6, 2, v1\n"
"v_and_b32 v7, 3, v1\n"
"s_mul_i32 s76, s65, 5120\n"
"s_add_u32 s76, s76, 65536\n"
"v_lshl_add_u32 v4, v7, 4, s76\n"
"v_lshl_add_u32 v4, v6, 7, v4\n"
"v_lshl_add_u32 v5, v7, 3, s76\n"
"v_lshl_add_u32 v5, v6, 5, v5\n"
"s_waitcnt vmcnt(2)\n"
"ds_read_b64_tr_b16 a[40:41], v4 offset:0\n"
"ds_read_b64_tr_b16 a[42:43], v4 offset:2048\n"
"ds_read_b64_tr_b16 a[44:45], v4 offset:8\n"
"ds_read_b64_tr_b16 a[46:47], v4 offset:2056\n"
"ds_read_b64_tr_b16 a[48:49], v4 offset:64\n"
"ds_read_b64_tr_b16 a[50:51], v4 offset:2112\n"
"ds_read_b64_tr_b16 a[52:53], v4 offset:72\n"
"ds_read_b64_tr_b16 a[54:55], v4 offset:2120\n"
"ds_read_b64_tr_b16 a[56:57], v5 offset:4096\n"
"ds_read_b64_tr_b16 a[58:59], v5 offset:4608\n"
"v_lshlrev_b32 v6, 4, v1\n"
"v_or_b32 v6, s13, v6\n"
"v_bfe_u32 v7, v1, 3, 1\n"
"v_lshl_or_b32 v7, v7, 30, v6\n"
"v_pk_mul_f32 v[182:183], v[12:13], v[12:13]\n"
"v_pk_fma_f32 v[182:183], v[14:15], v[14:15], v[182:183]\n"
"v_pk_add_f32 v[180:181], v[180:181], v[44:45]\n"
"v_pk_fma_f32 v[182:183], v[44:45], v[44:45], v[182:183]\n"
"v_pk_add_f32 v[180:181], v[180:181], v[46:47]\n"
"v_pk_fma_f32 v[182:183], v[46:47], v[46:47], v[182:183]\n"
"v_pk_add_f32 v[180:181], v[180:181], v[76:77]\n"
"v_pk_fma_f32 v[182:183], v[76:77], v[76:77], v[182:183]\n"
"v_pk_add_f32 v[180:181], v[180:181], v[78:79]\n"
"v_pk_fma_f32 v[182:183], v[78:79], v[78:79], v[182:183]\n"
"v_pk_mul_f32 v[240:241], v[16:17], v[16:17]\n"
"v_pk_mul_f32 v[242:243], v[18:19], v[18:19]\n"
"v_pk_mul_f32 v[244:245], v[48:49], v[48:49]\n"
"v_pk_mul_f32 v[246:247], v[50:51], v[50:51]\n"
"v_pk_fma_f32 v[240:241], v[240:241], v[218:219], v[216:217] op_sel:[0,0,1] op_sel_hi:[1,0,1]\n"
"v_pk_fma_f32 v[242:243], v[242:243], v[218:219], v[216:217] op_sel:[0,0,1] op_sel_hi:[1,0,1]\n"
"v_pk_fma_f32 v[244:245], v[244:245], v[218:219], v[216:217] op_sel:[0,0,1] op_sel_hi:[1,0,1]\n"
"v_pk_fma_f32 v[246:247], v[246:247], v[218:219], v[216:217] op_sel:[0,0,1] op_sel_hi:[1,0,1]\n"
"v_pk_mul_f32 v[240:241], v[16:17], v[240:241]\n"
"v_pk_mul_f32 v[242:243], v[18:19], v[242:243]\n"
"v_pk_mul_f32 v[244:245], v[48:49], v[244:245]\n"
"v_pk_mul_f32 v[246:247], v[50:51], v[246:247]\n"
"v_exp_f32 v240, v240\n"
"v_exp_f32 v241, v241\n"
"v_exp_f32 v242, v242\n"
"v_exp_f32 v243, v243\n"
"v_exp_f32 v244, v244\n"
"v_exp_f32 v245, v245\n"
"v_exp_f32 v246, v246\n"
"v_exp_f32 v247, v247\n"
"s_waitcnt lgkmcnt(0)\n"
"buffer_store_dwordx4 a[40:43], v6, s[36:39], s19 offen offset:0 sc1\n"
"v_pk_fma_f32 v[240:241], v[240:241], v[218:219], v[218:219] op_sel:[0,1,1] op_sel_hi:[1,1,1]\n"
"v_pk_fma_f32 v[242:243], v[242:243], v[218:219], v[218:219] op_sel:[0,1,1] op_sel_hi:[1,1,1]\n"
"v_pk_fma_f32 v[244:245], v[244:245], v[218:219], v[218:219] op_sel:[0,1,1] op_sel_hi:[1,1,1]\n"
"v_pk_fma_f32 v[246:247], v[246:247], v[218:219], v[218:219] op_sel:[0,1,1] op_sel_hi:[1,1,1]\n"
"v_rcp_f32 v240, v240\n"
"v_rcp_f32 v241, v241\n"
"v_rcp_f32 v242, v242\n"
"v_rcp_f32 v243, v243\n"
"v_rcp_f32 v244, v244\n"
"v_rcp_f32 v245, v245\n"
"v_rcp_f32 v246, v246\n"
"v_rcp_f32 v247, v247\n"
"v_pk_mul_f32 v[16:17], v[16:17], v[240:241]\n"
"v_pk_mul_f32 v[18:19], v[18:19], v[242:243]\n"
"buffer_store_dwordx4 a[44:47], v6, s[36:39], s19 offen offset:1024 sc1\n"
"v_pk_mul_f32 v[48:49], v[48:49], v[244:245]\n"
"v_pk_mul_f32 v[50:51], v[50:51], v[246:247]\n"
"v_pk_add_f32 v[184:185], v[16:17], v[18:19]\n"
"v_pk_mul_f32 v[186:187], v[16:17], v[16:17]\n"
"v_pk_fma_f32 v[186:187], v[18:19], v[18:19], v[186:187]\n"
"v_pk_add_f32 v[184:185], v[184:185], v[48:49]\n"
"v_pk_fma_f32 v[186:187], v[48:49], v[48:49], v[186:187]\n"
"v_pk_add_f32 v[184:185], v[184:185], v[50:51]\n"
"v_pk_fma_f32 v[186:187], v[50:51], v[50:51], v[186:187]\n"
"v_pk_mul_f32 v[240:241], v[20:21], v[20:21]\n"
"v_pk_mul_f32 v[242:243], v[22:23], v[22:23]\n"
"v_pk_mul_f32 v[244:245], v[52:53], v[52:53]\n"
"v_pk_mul_f32 v[246:247], v[54:55], v[54:55]\n"
"v_pk_fma_f32 v[240:241], v[240:241], v[222:223], v[220:221] op_sel:[0,0,1] op_sel_hi:[1,0,1]\n"
"buffer_store_dwordx4 a[48:51], v6, s[36:39], s19 offen offset:2048 sc1\n"
"v_pk_fma_f32 v[242:243], v[242:243], v[222:223], v[220:221] op_sel:[0,0,1] op_sel_hi:[1,0,1]\n"
"v_pk_fma_f32 v[244:245], v[244:245], v[222:223], v[220:221] op_sel:[0,0,1] op_sel_hi:[1,0,1]\n"
"v_pk_fma_f32 v[246:247], v[246:247], v[222:223], v[220:221] op_sel:[0,0,1] op_sel_hi:[1,0,1]\n"
"v_pk_mul_f32 v[240:241], v[20:21], v[240:241]\n"
"v_pk_mul_f32 v[242:243], v[22:23], v[242:243]\n"
"v_pk_mul_f32 v[244:245], v[52:53], v[244:245]\n"
"v_pk_mul_f32 v[246:247], v[54:55], v[246:247]\n"
"v_exp_f32 v240, v240\n"
"v_exp_f32 v241, v241\n"
"v_exp_f32 v242, v242\n"
"v_exp_f32 v243, v243\n"
"v_exp_f32 v244, v244\n"
"v_exp_f32 v245, v245\n"
"v_exp_f32 v246, v246\n"
"buffer_store_dwordx4 a[52:55], v6, s[36:39], s19 offen offset:3072 sc1\n"
"v_exp_f32 v247, v247\n"
"v_pk_fma_f32 v[240:241], v[240:241], v[222:223], v[222:223] op_sel:[0,1,1] op_sel_hi:[1,1,1]\n"
"v_pk_fma_f32 v[242:243], v[242:243], v[222:223], v[222:223] op_sel:[0,1,1] op_sel_hi:[1,1,1]\n"
"v_pk_fma_f32 v[244:245], v[244:245], v[222:223], v[222:223] op_sel:[0,1,1] op_sel_hi:[1,1,1]\n"
"v_pk_fma_f32 v[246:247], v[246:247], v[222:223], v[222:223] op_sel:[0,1,1] op_sel_hi:[1,1,1]\n"
"v_rcp_f32 v240, v240\n"
"v_rcp_f32 v241, v241\n"
"v_rcp_f32 v242, v242\n"
"v_rcp_f32 v243, v243\n"
"v_rcp_f32 v244, v244\n"
"v_rcp_f32 v245, v245\n"
"v_rcp_f32 v246, v246\n"
"v_rcp_f32 v247, v247\n"
"v_pk_mul_f32 v[20:21], v[20:21], v[240:241]\n"
"buffer_store_dwordx4 a[56:59], v7, s[36:39], s9 offen offset:0 sc1\n"
"v_pk_mul_f32 v[22:23], v[22:23], v[242:243]\n"
"v_pk_mul_f32 v[52:53], v[52:53], v[244:245]\n"
"v_pk_mul_f32 v[54:55], v[54:55], v[246:247]\n"
"v_pk_add_f32 v[188:189], v[20:21], v[22:23]\n"
"v_pk_mul_f32 v[190:191], v[20:21], v[20:21]\n"
"v_pk_fma_f32 v[190:191], v[22:23], v[22:23], v[190:191]\n"
"v_pk_add_f32 v[188:189], v[188:189], v[52:53]\n"
"v_pk_fma_f32 v[190:191], v[52:53], v[52:53], v[190:191]\n"
"v_pk_add_f32 v[188:189], v[188:189], v[54:55]\n"
"v_pk_fma_f32 v[190:191], v[54:55], v[54:55], v[190:191]\n"
"v_pk_mul_f32 v[240:241], v[24:25], v[24:25]\n"
"v_pk_mul_f32 v[242:243], v[26:27], v[26:27]\n"
"v_pk_mul_f32 v[244:245], v[56:57], v[56:57]\n"
"v_pk_mul_f32 v[246:247], v[58:59], v[58:59]\n"
"v_pk_fma_f32 v[240:241], v[240:241], v[226:227], v[224:225] op_sel:[0,0,1] op_sel_hi:[1,0,1]\n"
"v_pk_fma_f32 v[242:243], v[242:243], v[226:227], v[224:225] op_sel:[0,0,1] op_sel_hi:[1,0,1]\n"
"v_pk_fma_f32 v[244:245], v[244:245], v[226:227], v[224:225] op_sel:[0,0,1] op_sel_hi:[1,0,1]\n"
"v_pk_fma_f32 v[246:247], v[246:247], v[226:227], v[224:225] op_sel:[0,0,1] op_sel_hi:[1,0,1]\n"
"v_pk_mul_f32 v[240:241], v[24:25], v[240:241]\n"
"v_pk_mul_f32 v[242:243], v[26:27], v[242:243]\n"
"v_pk_mul_f32 v[244:245], v[56:57], v[244:245]\n"
"v_pk_mul_f32 v[246:247], v[58:59], v[246:247]\n"
"v_exp_f32 v240, v240\n"
"v_exp_f32 v241, v241\n"
"v_exp_f32 v242, v242\n"
"v_exp_f32 v243, v243\n"
"v_exp_f32 v244, v244\n"
"v_exp_f32 v245, v245\n"
"v_exp_f32 v246, v246\n"
"v_exp_f32 v247, v247\n"
"v_pk_fma_f32 v[240:241], v[240:241], v[226:227], v[226:227] op_sel:[0,1,1] op_sel_hi:[1,1,1]\n"
"v_pk_fma_f32 v[242:243], v[242:243], v[226:227], v[226:227] op_sel:[0,1,1] op_sel_hi:[1,1,1]\n"
"v_pk_fma_f32 v[244:245], v[244:245], v[226:227], v[226:227] op_sel:[0,1,1] op_sel_hi:[1,1,1]\n"
"v_pk_fma_f32 v[246:247], v[246:247], v[226:227], v[226:227] op_sel:[0,1,1] op_sel_hi:[1,1,1]\n"
"v_rcp_f32 v240, v240\n"
"v_rcp_f32 v241, v241\n"
"v_rcp_f32 v242, v242\n"
"v_rcp_f32 v243, v243\n"
"v_rcp_f32 v244, v244\n"
"v_rcp_f32 v245, v245\n"
"v_rcp_f32 v246, v246\n"
"v_rcp_f32 v247, v247\n"
"v_pk_mul_f32 v[24:25], v[24:25], v[240:241]\n"
"v_pk_mul_f32 v[26:27], v[26:27], v[242:243]\n"
"v_lshrrev_b32 v2, 4, v1\n"
"v_xor_b32 v2, v2, v1\n"
"v_and_b32 v2, 7, v2\n"
"v_lshlrev_b32 v2, 4, v2\n"
"v_lshrrev_b32 v3, 3, v1\n"
"s_lshl_b32 s71, s66, 7\n"
"s_lshl_b32 s72, s65, 5\n"
"s_add_u32 s71, s71, s72\n"
"s_mul_i32 s71, s71, 4096\n"
"s_movk_i32 s72, 4096\n"
"v_mad_u32_u24 v2, v3, s72, v2\n"
"v_add_u32 v208, s71, v2\n"
"v_xor_b32 v2, 64, v208\n"
"v_add_u32 v212, 31744, v2\n"
"v_add_u32 v216, 63488, v208\n"
"v_add_u32 v220, 95232, v2\n"
"s_mov_b32 s24, 0\n"
"s_lshl_b32 s6, s65, 12\n"
"s_lshl_b32 s7, s65, 10\n"
"v_pk_mul_f32 v[56:57], v[56:57], v[244:245]\n"
"v_pk_mul_f32 v[58:59], v[58:59], v[246:247]\n"
"v_pk_add_f32 v[192:193], v[24:25], v[26:27]\n"
"v_pk_mul_f32 v[194:195], v[24:25], v[24:25]\n"
"v_pk_fma_f32 v[194:195], v[26:27], v[26:27], v[194:195]\n"
"v_pk_add_f32 v[192:193], v[192:193], v[56:57]\n"
"v_pk_fma_f32 v[194:195], v[56:57], v[56:57], v[194:195]\n"
"v_pk_add_f32 v[192:193], v[192:193], v[58:59]\n"
"v_pk_fma_f32 v[194:195], v[58:59], v[58:59], v[194:195]\n"
"v_pk_mul_f32 v[240:241], v[28:29], v[28:29]\n"
"v_pk_mul_f32 v[242:243], v[30:31], v[30:31]\n"
"v_pk_mul_f32 v[244:245], v[60:61], v[60:61]\n"
"v_pk_mul_f32 v[246:247], v[62:63], v[62:63]\n"
"v_pk_fma_f32 v[240:241], v[240:241], v[230:231], v[228:229] op_sel:[0,0,1] op_sel_hi:[1,0,1]\n"
"v_pk_fma_f32 v[242:243], v[242:243], v[230:231], v[228:229] op_sel:[0,0,1] op_sel_hi:[1,0,1]\n"
"v_pk_fma_f32 v[244:245], v[244:245], v[230:231], v[228:229] op_sel:[0,0,1] op_sel_hi:[1,0,1]\n"
"v_pk_fma_f32 v[246:247], v[246:247], v[230:231], v[228:229] op_sel:[0,0,1] op_sel_hi:[1,0,1]\n"
"s_add_u32 m0, s6, 86016\n"
"s_nop 0\n"
"buffer_load_dwordx4 v208, s[48:51], s24 offen offset:0 lds\n"
"v_pk_mul_f32 v[240:241], v[28:29], v[240:241]\n"
"v_pk_mul_f32 v[242:243], v[30:31], v[242:243]\n"
"v_pk_mul_f32 v[244:245], v[60:61], v[244:245]\n"
"v_pk_mul_f32 v[246:247], v[62:63], v[246:247]\n"
"v_exp_f32 v240, v240\n"
"v_exp_f32 v241, v241\n"
"v_exp_f32 v242, v242\n"
"v_exp_f32 v243, v243\n"
"v_exp_f32 v244, v244\n"
"v_exp_f32 v245, v245\n"
"v_exp_f32 v246, v246\n"
"v_exp_f32 v247, v247\n"
"v_pk_fma_f32 v[240:241], v[240:241], v[230:231], v[230:231] op_sel:[0,1,1] op_sel_hi:[1,1,1]\n"
"v_pk_fma_f32 v[242:243], v[242:243], v[230:231], v[230:231] op_sel:[0,1,1] op_sel_hi:[1,1,1]\n"
"v_pk_fma_f32 v[244:245], v[244:245], v[230:231], v[230:231] op_sel:[0,1,1] op_sel_hi:[1,1,1]\n"
"v_pk_fma_f32 v[246:247], v[246:247], v[230:231], v[230:231] op_sel:[0,1,1] op_sel_hi:[1,1,1]\n"
"v_rcp_f32 v240, v240\n"
"buffer_load_dwordx4 v212, s[48:51], s24 offen offset:1024 lds\n"
"v_rcp_f32 v241, v241\n"
"v_rcp_f32 v242, v242\n"
"v_rcp_f32 v243, v243\n"
"v_rcp_f32 v244, v244\n"
"v_rcp_f32 v245, v245\n"
"v_rcp_f32 v246, v246\n"
"v_rcp_f32 v247, v247\n"
"v_pk_mul_f32 v[28:29], v[28:29], v[240:241]\n"
"v_pk_mul_f32 v[30:31], v[30:31], v[242:243]\n"
"v_pk_mul_f32 v[60:61], v[60:61], v[244:245]\n"
"v_pk_mul_f32 v[62:63], v[62:63], v[246:247]\n"
"v_pk_add_f32 v[196:197], v[28:29], v[30:31]\n"
"v_pk_mul_f32 v[198:199], v[28:29], v[28:29]\n"
"v_pk_fma_f32 v[198:199], v[30:31], v[30:31], v[198:199]\n"
"v_pk_add_f32 v[196:197], v[196:197], v[60:61]\n"
"v_pk_fma_f32 v[198:199], v[60:61], v[60:61], v[198:199]\n"
"v_pk_add_f32 v[196:197], v[196:197], v[62:63]\n"
"buffer_load_dwordx4 v216, s[48:51], s24 offen offset:2048 lds\n"
"v_pk_fma_f32 v[198:199], v[62:63], v[62:63], v[198:199]\n"
"v_pk_mul_f32 v[240:241], v[32:33], v[32:33]\n"
"v_pk_mul_f32 v[242:243], v[34:35], v[34:35]\n"
"v_pk_mul_f32 v[244:245], v[64:65], v[64:65]\n"
"v_pk_mul_f32 v[246:247], v[66:67], v[66:67]\n"
"v_pk_fma_f32 v[240:241], v[240:241], v[234:235], v[232:233] op_sel:[0,0,1] op_sel_hi:[1,0,1]\n"
"v_pk_fma_f32 v[242:243], v[242:243], v[234:235], v[232:233] op_sel:[0,0,1] op_sel_hi:[1,0,1]\n"
"v_pk_fma_f32 v[244:245], v[244:245], v[234:235], v[232:233] op_sel:[0,0,1] op_sel_hi:[1,0,1]\n"
"v_pk_fma_f32 v[246:247], v[246:247], v[234:235], v[232:233] op_sel:[0,0,1] op_sel_hi:[1,0,1]\n"
"v_pk_mul_f32 v[240:241], v[32:33], v[240:241]\n"
"v_pk_mul_f32 v[242:243], v[34:35], v[242:243]\n"
"v_pk_mul_f32 v[244:245], v[64:65], v[244:245]\n"
"v_pk_mul_f32 v[246:247], v[66:67], v[246:247]\n"
"v_exp_f32 v240, v240\n"
"v_exp_f32 v241, v241\n"
"v_exp_f32 v242, v242\n"
"v_exp_f32 v243, v243\n"
"buffer_load_dwordx4 v220, s[48:51], s24 offen offset:3072 lds\n"
"s_add_u32 s24, s24, 128\n"
"v_exp_f32 v244, v244\n"
"v_exp_f32 v245, v245\n"
"v_exp_f32 v246, v246\n"
"v_exp_f32 v247, v247\n"
"v_pk_fma_f32 v[240:241], v[240:241], v[234:235], v[234:235] op_sel:[0,1,1] op_sel_hi:[1,1,1]\n"
"v_pk_fma_f32 v[242:243], v[242:243], v[234:235], v[234:235] op_sel:[0,1,1] op_sel_hi:[1,1,1]\n"
"v_pk_fma_f32 v[244:245], v[244:245], v[234:235], v[234:235] op_sel:[0,1,1] op_sel_hi:[1,1,1]\n"
"v_pk_fma_f32 v[246:247], v[246:247], v[234:235], v[234:235] op_sel:[0,1,1] op_sel_hi:[1,1,1]\n"
"v_rcp_f32 v240, v240\n"
"v_rcp_f32 v241, v241\n"
"v_rcp_f32 v242, v242\n"
"v_rcp_f32 v243, v243\n"
"v_rcp_f32 v244, v244\n"
"v_rcp_f32 v245, v245\n"
"v_rcp_f32 v246, v246\n"
"v_rcp_f32 v247, v247\n"
"v_pk_mul_f32 v[32:33], v[32:33], v[240:241]\n"
"s_add_u32 m0, s6, 102400\n"
"s_nop 0\n"
"buffer_load_dwordx4 v208, s[48:51], s24 offen offset:0 lds\n"
"v_pk_mul_f32 v[34:35], v[34:35], v[242:243]\n"
"v_pk_mul_f32 v[64:65], v[64:65], v[244:245]\n"
"v_pk_mul_f32 v[66:67], v[66:67], v[246:247]\n"
"v_pk_add_f32 v[200:201], v[32:33], v[34:35]\n"
"v_pk_mul_f32 v[202:203], v[32:33], v[32:33]\n"
"v_pk_fma_f32 v[202:203], v[34:35], v[34:35], v[202:203]\n"
"v_pk_add_f32 v[200:201], v[200:201], v[64:65]\n"
"v_pk_fma_f32 v[202:203], v[64:65], v[64:65], v[202:203]\n"
"v_pk_add_f32 v[200:201], v[200:201], v[66:67]\n"
"v_pk_fma_f32 v[202:203], v[66:67], v[66:67], v[202:203]\n"
"v_pk_mul_f32 v[240:241], v[36:37], v[36:37]\n"
"v_pk_mul_f32 v[242:243], v[38:39], v[38:39]\n"
"v_pk_mul_f32 v[244:245], v[68:69], v[68:69]\n"
"v_pk_mul_f32 v[246:247], v[70:71], v[70:71]\n"
"v_pk_fma_f32 v[240:241], v[240:241], v[238:239], v[236:237] op_sel:[0,0,1] op_sel_hi:[1,0,1]\n"
"v_pk_fma_f32 v[242:243], v[242:243], v[238:239], v[236:237] op_sel:[0,0,1] op_sel_hi:[1,0,1]\n"
"v_pk_fma_f32 v[244:245], v[244:245], v[238:239], v[236:237] op_sel:[0,0,1] op_sel_hi:[1,0,1]\n"
"buffer_load_dwordx4 v212, s[48:51], s24 offen offset:1024 lds\n"
"v_pk_fma_f32 v[246:247], v[246:247], v[238:239], v[236:237] op_sel:[0,0,1] op_sel_hi:[1,0,1]\n"
"v_pk_mul_f32 v[240:241], v[36:37], v[240:241]\n"
"v_pk_mul_f32 v[242:243], v[38:39], v[242:243]\n"
"v_pk_mul_f32 v[244:245], v[68:69], v[244:245]\n"
"v_pk_mul_f32 v[246:247], v[70:71], v[246:247]\n"
"v_exp_f32 v240, v240\n"
"v_exp_f32 v241, v241\n"
"v_exp_f32 v242, v242\n"
"v_exp_f32 v243, v243\n"
"v_exp_f32 v244, v244\n"
"v_exp_f32 v245, v245\n"
"v_exp_f32 v246, v246\n"
"v_exp_f32 v247, v247\n"
"v_pk_fma_f32 v[240:241], v[240:241], v[238:239], v[238:239] op_sel:[0,1,1] op_sel_hi:[1,1,1]\n"
"v_pk_fma_f32 v[242:243], v[242:243], v[238:239], v[238:239] op_sel:[0,1,1] op_sel_hi:[1,1,1]\n"
"v_pk_fma_f32 v[244:245], v[244:245], v[238:239], v[238:239] op_sel:[0,1,1] op_sel_hi:[1,1,1]\n"
"v_pk_fma_f32 v[246:247], v[246:247], v[238:239], v[238:239] op_sel:[0,1,1] op_sel_hi:[1,1,1]\n"
"buffer_load_dwordx4 v216, s[48:51], s24 offen offset:2048 lds\n"
"v_rcp_f32 v240, v240\n"
"v_rcp_f32 v241, v241\n"
"v_rcp_f32 v242, v242\n"
"v_rcp_f32 v243, v243\n"
"v_rcp_f32 v244, v244\n"
"v_rcp_f32 v245, v245\n"
"v_rcp_f32 v246, v246\n"
"v_rcp_f32 v247, v247\n"
"v_pk_mul_f32 v[36:37], v[36:37], v[240:241]\n"
"v_pk_mul_f32 v[38:39], v[38:39], v[242:243]\n"
"v_pk_mul_f32 v[68:69], v[68:69], v[244:245]\n"
"v_pk_mul_f32 v[70:71], v[70:71], v[246:247]\n"
"v_pk_add_f32 v[204:205], v[36:37], v[38:39]\n"
"v_pk_mul_f32 v[206:207], v[36:37], v[36:37]\n"
"v_pk_fma_f32 v[206:207], v[38:39], v[38:39], v[206:207]\n"
"v_pk_add_f32 v[204:205], v[204:205], v[68:69]\n"
"v_pk_fma_f32 v[206:207], v[68:69], v[68:69], v[206:207]\n"
"buffer_load_dwordx4 v220, s[48:51], s24 offen offset:3072 lds\n"
"s_add_u32 s24, s24, 128\n"
"v_pk_add_f32 v[204:205], v[204:205], v[70:71]\n"
"v_pk_fma_f32 v[206:207], v[70:71], v[70:71], v[206:207]\n"
"v_add_f32 v176, v176, v177\n"
"v_add_f32 v177, v178, v179\n"
"v_add_f32 v180, v180, v181\n"
"v_add_f32 v181, v182, v183\n"
"v_add_f32 v184, v184, v185\n"
"v_add_f32 v185, v186, v187\n"
"v_add_f32 v188, v188, v189\n"
"v_add_f32 v189, v190, v191\n"
"v_add_f32 v192, v192, v193\n"
"v_add_f32 v193, v194, v195\n"
"v_add_f32 v196, v196, v197\n"
"v_add_f32 v197, v198, v199\n"
"v_add_f32 v200, v200, v201\n"
"v_add_f32 v201, v202, v203\n"
"v_add_f32 v204, v204, v205\n"
"v_add_f32 v205, v206, v207\n"
"v_and_b32 v4, 15, v1\n"
"v_lshrrev_b32 v5, 4, v1\n"
"v_add_u32 v5, s65, v5\n"
"v_and_b32 v5, 3, v5\n"
"v_lshlrev_b32 v5, 8, v5\n"
"v_lshl_add_u32 v5, v4, 3, v5\n"
"s_lshl_b32 s71, s65, 10\n"
"s_add_u32 s71, s71, 26624\n"
"v_add_u32 v5, s71, v5\n"
"v_permlane32_swap_b32 v176, v192\n"
"v_permlane32_swap_b32 v177, v193\n"
"v_permlane32_swap_b32 v180, v196\n"
"v_permlane32_swap_b32 v181, v197\n"
"v_permlane32_swap_b32 v184, v200\n"
"v_permlane32_swap_b32 v185, v201\n"
"v_permlane32_swap_b32 v188, v204\n"
"v_permlane32_swap_b32 v189, v205\n"
"v_add_f32 v176, v176, v192\n"
"v_add_f32 v177, v177, v193\n"
"v_add_f32 v180, v180, v196\n"
"v_add_f32 v181, v181, v197\n"
"v_add_f32 v184, v184, v200\n"
"v_add_f32 v185, v185, v201\n"
"v_add_f32 v188, v188, v204\n"
"v_add_f32 v189, v189, v205\n"
"v_permlane16_swap_b32 v176, v184\n"
"v_permlane16_swap_b32 v177, v185\n"
"v_permlane16_swap_b32 v180, v188\n"
"v_permlane16_swap_b32 v181, v189\n"
"v_add_f32 v176, v176, v184\n"
"v_add_f32 v177, v177, v185\n"
"v_add_f32 v180, v180, v188\n"
"v_add_f32 v181, v181, v189\n"
"ds_write_b64 v5, v[176:177] offset:0\n"
"ds_write_b64 v5, v[180:181] offset:128\n"
"v_and_b32 v4, 31, v1\n"
"s_lshl_b32 s71, s65, 5\n"
"v_add_u32 v4, s71, v4\n"
"v_lshlrev_b32 v5, 3, v4\n"
"v_add_u32 v5, 26624, v5\n"
"v_lshlrev_b32 v4, 4, v4\n"
"v_lshrrev_b32 v6, 5, v1\n"
"v_lshl_or_b32 v4, v6, 30, v4\n"
"s_lshl_b32 s71, s66, 4\n"
"s_add_u32 s71, s71, s67\n"
"s_lshl_b32 s71, s71, 11\n"
"s_add_u32 s71, s71, 262144\n"
"s_waitcnt vmcnt(20) lgkmcnt(0)\n"
"s_barrier\n"
"ds_read_b64 v[176:177], v5 offset:0\n"
"ds_read_b64 v[178:179], v5 offset:1024\n"
"ds_read_b64 v[180:181], v5 offset:2048\n"
"ds_read_b64 v[182:183], v5 offset:3072\n"
"v_mov_b32 v185, s68\n"
"v_mov_b32 v187, s69\n"
"s_waitcnt lgkmcnt(0)\n"
"v_pk_add_f32 v[176:177], v[176:177], v[178:179]\n"
"v_pk_add_f32 v[180:181], v[180:181], v[182:183]\n"
"v_add_f32 v184, v176, v180\n"
"v_add_f32 v186, v177, v181\n"
"buffer_store_dwordx4 v[184:187], v4, s[36:39], s71 offen sc1\n"
"s_waitcnt vmcnt(9)\n"
"v_mov_b32 v4, s68\n"
"v_mov_b32 v5, s69\n"
"v_min_u32 v6, 1, v1\n"
"v_lshlrev_b32 v6, 30, v6\n"
"v_or_b32 v6, s13, v6\n"
"buffer_store_dwordx2 v[4:5], v6, s[36:39], s22 offen sc1\n"
"ds_read_b32 v176, v252 offset:49152\n"
"ds_read_b32 v177, v252 offset:49296\n"
"ds_read_b32 v178, v252 offset:49440\n"
"ds_read_b32 v179, v252 offset:49584\n"
"ds_read_b32 v180, v252 offset:49728\n"
"ds_read_b32 v181, v252 offset:49872\n"
"ds_read_b32 v182, v252 offset:50016\n"
"ds_read_b32 v183, v252 offset:50160\n"
"ds_read_b32 v184, v252 offset:50304\n"
"ds_read_b32 v185, v252 offset:50448\n"
"ds_read_b32 v186, v252 offset:50592\n"
"ds_read_b32 v187, v252 offset:50736\n"
"ds_read_b32 v188, v252 offset:50880\n"
"ds_read_b32 v189, v252 offset:0\n"
"ds_read_b32 v190, v252 offset:144\n"
"v_accvgpr_read_b32 v200, a116\n"
"v_accvgpr_read_b32 v201, a117\n"
"v_accvgpr_read_b32 v202, a118\n"
"v_accvgpr_read_b32 v203, a119\n"
"s_and_b32 s71, s64, 7\n"
"s_mul_i32 s71, s71, 294912\n"
"s_lshr_b32 s72, s64, 3\n"
"s_lshl_b32 s72, s72, 7\n"
"s_add_u32 s71, s71, s72\n"
"s_add_u32 m0, s6, 122880\n"
"s_nop 0\n"
"buffer_load_dwordx4 v208, s[48:51], s24 offen offset:0 lds\n"
"s_add_u32 s71, s71, 1048576\n"
"s_add_u32 s72, s71, 131072\n"
"s_add_u32 s73, s71, 262144\n"
"s_min_u32 s74, s65, 1\n"
"s_lshl_b32 s74, s74, 30\n"
"v_or_b32 v3, s74, v255\n"
"v_lshlrev_b32 v240, 16, v200\n"
"v_and_b32 v242, 0xffff0000, v200\n"
"v_lshlrev_b32 v244, 16, v201\n"
"v_and_b32 v246, 0xffff0000, v201\n"
"v_lshlrev_b32 v241, 16, v202\n"
"v_and_b32 v243, 0xffff0000, v202\n"
"v_lshlrev_b32 v245, 16, v203\n"
"v_and_b32 v247, 0xffff0000, v203\n"
"s_waitcnt lgkmcnt(6)\n"
"ds_read_b32 v191, v252 offset:288\n"
"ds_read_b32 v192, v252 offset:432\n"
"ds_read_b32 v193, v252 offset:576\n"
"ds_read_b32 v194, v252 offset:720\n"
"ds_read_b32 v195, v252 offset:864\n"
"buffer_load_dwordx4 v212, s[48:51], s24 offen offset:1024 lds\n"
"ds_read_b32 v196, v252 offset:1008\n"
"ds_read_b32 v197, v252 offset:1152\n"
"ds_read_b32 v198, v252 offset:1296\n"
"v_lshlrev_b32 v200, 16, v176\n"
"v_and_b32 v201, 0xffff0000, v176\n"
"v_lshlrev_b32 v202, 16, v177\n"
"v_and_b32 v203, 0xffff0000, v177\n"
"v_lshlrev_b32 v204, 16, v178\n"
"v_and_b32 v205, 0xffff0000, v178\n"
"v_lshlrev_b32 v206, 16, v179\n"
"v_and_b32 v207, 0xffff0000, v179\n"
"v_pk_mul_f32 v[4:5], v[200:201], v[246:247]\n"
"v_pk_fma_f32 v[4:5], v[202:203], v[244:245], v[4:5]\n"
"v_pk_fma_f32 v[4:5], v[204:205], v[242:243], v[4:5]\n"
"v_pk_fma_f32 v[4:5], v[206:207], v[240:241], v[4:5]\n"
"s_waitcnt lgkmcnt(0)\n"
"v_lshlrev_b32 v248, 16, v189\n"
"v_and_b32 v249, 0xffff0000, v189\n"
"v_pk_mul_f32 v[4:5], v[4:5], v[248:249]\n"
"v_lshlrev_b32 v200, 16, v180\n"
"buffer_load_dwordx4 v216, s[48:51], s24 offen offset:2048 lds\n"
"v_and_b32 v201, 0xffff0000, v180\n"
"v_pk_mul_f32 v[6:7], v[202:203], v[246:247]\n"
"v_pk_fma_f32 v[6:7], v[204:205], v[244:245], v[6:7]\n"
"v_pk_fma_f32 v[6:7], v[206:207], v[242:243], v[6:7]\n"
"v_pk_fma_f32 v[6:7], v[200:201], v[240:241], v[6:7]\n"
"v_lshlrev_b32 v248, 16, v190\n"
"v_and_b32 v249, 0xffff0000, v190\n"
"v_pk_mul_f32 v[6:7], v[6:7], v[248:249]\n"
"v_cvt_pk_bf16_f32 v189, v6, v4\n"
"ds_write_b32 v253, v189 offset:16\n"
"v_cvt_pk_bf16_f32 v190, v7, v5\n"
"ds_write_b32 v253, v190 offset:148\n"
"v_lshlrev_b32 v202, 16, v181\n"
"v_and_b32 v203, 0xffff0000, v181\n"
"v_pk_mul_f32 v[4:5], v[204:205], v[246:247]\n"
"v_pk_fma_f32 v[4:5], v[206:207], v[244:245], v[4:5]\n"
"v_pk_fma_f32 v[4:5], v[200:201], v[242:243], v[4:5]\n"
"v_pk_fma_f32 v[4:5], v[202:203], v[240:241], v[4:5]\n"
"v_lshlrev_b32 v248, 16, v191\n"
"v_and_b32 v249, 0xffff0000, v191\n"
"buffer_load_dwordx4 v220, s[48:51], s24 offen offset:3072 lds\n"
"s_add_u32 s24, s24, 128\n"
"v_pk_mul_f32 v[4:5], v[4:5], v[248:249]\n"
"v_lshlrev_b32 v204, 16, v182\n"
"v_and_b32 v205, 0xffff0000, v182\n"
"v_pk_mul_f32 v[6:7], v[206:207], v[246:247]\n"
"v_pk_fma_f32 v[6:7], v[200:201], v[244:245], v[6:7]\n"
"v_pk_fma_f32 v[6:7], v[202:203], v[242:243], v[6:7]\n"
"v_pk_fma_f32 v[6:7], v[204:205], v[240:241], v[6:7]\n"
"v_lshlrev_b32 v248, 16, v192\n"
"v_and_b32 v249, 0xffff0000, v192\n"
"v_pk_mul_f32 v[6:7], v[6:7], v[248:249]\n"
"v_cvt_pk_bf16_f32 v191, v6, v4\n"
"ds_write_b32 v253, v191 offset:12\n"
"v_cvt_pk_bf16_f32 v192, v7, v5\n"
"ds_write_b32 v253, v192 offset:144\n"
"v_lshlrev_b32 v206, 16, v183\n"
"v_and_b32 v207, 0xffff0000, v183\n"
"v_pk_mul_f32 v[4:5], v[200:201], v[246:247]\n"
"v_pk_fma_f32 v[4:5], v[202:203], v[244:245], v[4:5]\n"
"v_pk_fma_f32 v[4:5], v[204:205], v[242:243], v[4:5]\n"
"v_pk_fma_f32 v[4:5], v[206:207], v[240:241], v[4:5]\n"
"s_add_u32 m0, s6, 139264\n"
"s_nop 0\n"
"buffer_load_dwordx4 v208, s[48:51], s24 offen offset:0 lds\n"
"v_lshlrev_b32 v248, 16, v193\n"
"v_and_b32 v249, 0xffff0000, v193\n"
"v_pk_mul_f32 v[4:5], v[4:5], v[248:249]\n"
"v_lshlrev_b32 v200, 16, v184\n"
"v_and_b32 v201, 0xffff0000, v184\n"
"v_pk_mul_f32 v[6:7], v[202:203], v[246:247]\n"
"v_pk_fma_f32 v[6:7], v[204:205], v[244:245], v[6:7]\n"
"v_pk_fma_f32 v[6:7], v[206:207], v[242:243], v[6:7]\n"
"v_pk_fma_f32 v[6:7], v[200:201], v[240:241], v[6:7]\n"
"v_lshlrev_b32 v248, 16, v194\n"
"v_and_b32 v249, 0xffff0000, v194\n"
"v_pk_mul_f32 v[6:7], v[6:7], v[248:249]\n"
"v_cvt_pk_bf16_f32 v193, v6, v4\n"
"ds_write_b32 v253, v193 offset:8\n"
"v_cvt_pk_bf16_f32 v194, v7, v5\n"
"ds_write_b32 v253, v194 offset:140\n"
"v_lshlrev_b32 v202, 16, v185\n"
"v_and_b32 v203, 0xffff0000, v185\n"
"v_pk_mul_f32 v[4:5], v[204:205], v[246:247]\n"
"v_pk_fma_f32 v[4:5], v[206:207], v[244:245], v[4:5]\n"
"buffer_load_dwordx4 v212, s[48:51], s24 offen offset:1024 lds\n"
"v_pk_fma_f32 v[4:5], v[200:201], v[242:243], v[4:5]\n"
"v_pk_fma_f32 v[4:5], v[202:203], v[240:241], v[4:5]\n"
"v_lshlrev_b32 v248, 16, v195\n"
"v_and_b32 v249, 0xffff0000, v195\n"
"v_pk_mul_f32 v[4:5], v[4:5], v[248:249]\n"
"v_lshlrev_b32 v204, 16, v186\n"
"v_and_b32 v205, 0xffff0000, v186\n"
"v_pk_mul_f32 v[6:7], v[206:207], v[246:247]\n"
"v_pk_fma_f32 v[6:7], v[200:201], v[244:245], v[6:7]\n"
"v_pk_fma_f32 v[6:7], v[202:203], v[242:243], v[6:7]\n"
"v_pk_fma_f32 v[6:7], v[204:205], v[240:241], v[6:7]\n"
"v_lshlrev_b32 v248, 16, v196\n"
"v_and_b32 v249, 0xffff0000, v196\n"
"v_pk_mul_f32 v[6:7], v[6:7], v[248:249]\n"
"v_cvt_pk_bf16_f32 v195, v6, v4\n"
"ds_write_b32 v253, v195 offset:4\n"
"v_cvt_pk_bf16_f32 v196, v7, v5\n"
"ds_write_b32 v253, v196 offset:136\n"
"v_lshlrev_b32 v206, 16, v187\n"
"v_and_b32 v207, 0xffff0000, v187\n"
"buffer_load_dwordx4 v216, s[48:51], s24 offen offset:2048 lds\n"
"v_pk_mul_f32 v[4:5], v[200:201], v[246:247]\n"
"v_pk_fma_f32 v[4:5], v[202:203], v[244:245], v[4:5]\n"
"v_pk_fma_f32 v[4:5], v[204:205], v[242:243], v[4:5]\n"
"v_pk_fma_f32 v[4:5], v[206:207], v[240:241], v[4:5]\n"
"v_lshlrev_b32 v248, 16, v197\n"
"v_and_b32 v249, 0xffff0000, v197\n"
"v_pk_mul_f32 v[4:5], v[4:5], v[248:249]\n"
"v_lshlrev_b32 v200, 16, v188\n"
"v_and_b32 v201, 0xffff0000, v188\n"
"v_pk_mul_f32 v[6:7], v[202:203], v[246:247]\n"
"v_pk_fma_f32 v[6:7], v[204:205], v[244:245], v[6:7]\n"
"v_pk_fma_f32 v[6:7], v[206:207], v[242:243], v[6:7]\n"
"v_pk_fma_f32 v[6:7], v[200:201], v[240:241], v[6:7]\n"
"v_lshlrev_b32 v248, 16, v198\n"
"v_and_b32 v249, 0xffff0000, v198\n"
"v_pk_mul_f32 v[6:7], v[6:7], v[248:249]\n"
"v_cvt_pk_bf16_f32 v197, v6, v4\n"
"ds_write_b32 v253, v197 offset:0\n"
"v_cvt_pk_bf16_f32 v198, v7, v5\n"
"ds_write_b32 v253, v198 offset:132\n"
"buffer_load_dwordx4 v220, s[48:51], s24 offen offset:3072 lds\n"
"s_add_u32 s24, s24, 128\n"
"s_waitcnt lgkmcnt(0)\n"
"s_barrier\n"
"ds_read_b32 v176, v254 offset:0\n"
"ds_read_b32 v177, v254 offset:4\n"
"ds_read_b32 v178, v254 offset:8\n"
"ds_read_b32 v179, v254 offset:12\n"
"ds_read_b32 v180, v254 offset:4224\n"
"ds_read_b32 v181, v254 offset:4228\n"
"ds_read_b32 v182, v254 offset:4232\n"
"ds_read_b32 v183, v254 offset:4236\n"
"ds_read_b32 v184, v254 offset:8448\n"
"ds_read_b32 v185, v254 offset:8452\n"
"ds_read_b32 v186, v254 offset:8456\n"
"ds_read_b32 v187, v254 offset:8460\n"
"s_waitcnt lgkmcnt(8)\n"
"buffer_store_dwordx4 v[176:179], v255, s[36:39], s71 offen sc1\n"
"s_waitcnt lgkmcnt(4)\n"
"buffer_store_dwordx4 v[180:183], v255, s[36:39], s72 offen sc1\n"
"s_waitcnt lgkmcnt(0)\n"
"buffer_store_dwordx4 v[184:187], v3, s[36:39], s73 offen sc1\n"
"s_lshr_b32 s99, s64, 6\n"
"s_and_b32 s73, s64, 7\n"
"s_lshl_b32 s73, s73, 2\n"
"s_add_u32 s99, s99, s73\n"
"s_bfe_u32 s23, s64, 0x30003\n"
"s_mul_i32 s75, s23, 36\n"
"s_mul_i32 s76, s65, 9\n"
"s_add_u32 s75, s75, s76\n"
"s_mul_i32 s75, s75, 9216\n"
"v_lshl_add_u32 v236, v1, 4, s75\n"
"s_mov_b32 s4, 7340032\n"
"v_min_u32 v228, 35, v1\n"
"v_lshlrev_b32 v228, 4, v228\n"
"s_lshl_b32 s75, s23, 10\n"
"s_add_u32 s16, s75, 40960\n"
"v_and_b32 v180, 127, v0\n"
"v_lshrrev_b32 v181, 7, v0\n"
"v_lshlrev_b32 v176, 14, v181\n"
"v_lshl_add_u32 v176, v180, 4, v176\n"
"v_add_u32 v177, 4096, v176\n"
"v_add_u32 v178, 8192, v176\n"
"v_add_u32 v179, 12288, v176\n"
"s_lshl_b32 s27, s66, 15\n"
"s_add_u32 s27, s27, 262144\n"
"v_lshrrev_b32 v3, 4, v1\n"
"v_min_u32 v3, 1, v3\n"
"v_lshlrev_b32 v3, 8, v3\n"
"v_add_u32 v3, s64, v3\n"
"v_cmp_lt_u32 vcc, 31, v1\n"
"v_mov_b32 v2, s98\n"
"v_bfe_u32 v4, v1, 1, 3\n"
"v_cndmask_b32 v3, v3, v2, vcc\n"
"v_mul_u32_u24 v2, 911, v3\n"
"v_lshrrev_b32 v2, 16, v2\n"
"v_mul_u32_u24 v5, 72, v2\n"
"v_sub_u32 v3, v3, v5\n"
"v_lshrrev_b32 v3, 3, v3\n"
"v_and_b32 v5, 3, v3\n"
"v_cmp_lt_u32 vcc, 7, v3\n"
"v_lshlrev_b32 v4, 7, v4\n"
"v_lshl_add_u32 v4, v2, 14, v4\n"
"v_cndmask_b32_e64 v5, v5, 0, vcc\n"
"v_lshl_add_u32 v224, v5, 12, v4\n"
"s_lshl_b32 s8, s65, 10\n"
"s_add_u32 s8, s8, 3407872\n"
"v_lshlrev_b32 v171, 4, v0\n"
"v_min_u32 v169, 149, v0\n"
"v_lshlrev_b32 v169, 4, v169\n"
"v_add_u32 v169, 71040, v169\n"
"v_lshrrev_b32 v184, 3, v0\n"
"v_and_b32 v185, 7, v0\n"
"v_mul_u32_u24 v168, 160, v184\n"
"v_lshl_add_u32 v168, v185, 4, v168\n"
"v_add_u32 v168, 73440, v168\n"
"v_mul_u32_u24 v167, 272, v184\n"
"v_lshl_add_u32 v167, v185, 5, v167\n"
"v_add_u32 v167, 36864, v167\n"
"s_mul_i32 s71, s65, 4288\n"
"s_add_u32 s71, s71, 36864\n"
"v_lshl_add_u32 v170, v1, 1, s71\n"
"v_and_b32 v182, 15, v1\n"
"v_lshrrev_b32 v183, 4, v1\n"
"v_lshlrev_b32 v183, 4, v183\n"
"v_and_b32 v184, 3, v1\n"
"v_mul_u32_u24 v164, 4288, v184\n"
"v_add_u32 v164, v164, v183\n"
"v_bfe_u32 v184, v1, 2, 2\n"
"v_lshl_add_u32 v164, v184, 3, v164\n"
"s_lshl_b32 s71, s65, 8\n"
"s_add_u32 s71, s71, 36864\n"
"v_add_u32 v164, s71, v164\n"
"s_and_b32 s72, s64, 3\n"
"s_lshl_b32 s71, s72, 5\n"
"s_add_u32 s71, s71, 17088\n"
"v_add_u32 v172, s71, v164\n"
"v_mul_u32_u24 v165, 160, v182\n"
"v_add_u32 v165, v165, v183\n"
"s_mul_i32 s71, s65, 320\n"
"s_sub_u32 s71, 69440, s71\n"
"v_add_u32 v165, s71, v165\n"
"v_mul_u32_u24 v166, 272, v182\n"
"v_add_u32 v166, v166, v183\n"
"s_mul_i32 s71, s65, 8704\n"
"s_add_u32 s71, s71, 36864\n"
"v_add_u32 v166, s71, v166\n"
"v_mul_u32_u24 v173, 80, v182\n"
"v_add_u32 v173, v173, v183\n"
"s_mul_i32 s71, s65, 2560\n"
"s_add_u32 s71, s71, 71680\n"
"v_add_u32 v173, s71, v173\n"
"v_lshrrev_b32 v184, 1, v1\n"
"v_and_b32 v185, 1, v1\n"
"v_mul_u32_u24 v174, 80, v184\n"
"v_lshl_add_u32 v174, v185, 5, v174\n"
"v_add_u32 v174, 71680, v174\n"
"v_lshlrev_b32 v175, 7, v184\n"
"v_lshl_add_u32 v175, v185, 4, v175\n"
"s_lshl_b32 s72, s72, 5\n"
"s_min_u32 s71, s65, 1\n"
"s_lshl_b32 s71, s71, 30\n"
"s_add_u32 s71, s71, s72\n"
"v_add_u32 v175, s71, v175\n"
"v_lshlrev_b32 v2, 4, v180\n"
"v_lshl_add_u32 v2, v181, 3, v2\n"
"v_add_u32 v252, 159744, v2\n"
"v_and_b32 v3, 15, v1\n"
"v_lshlrev_b32 v3, 4, v3\n"
"s_and_b32 s72, s64, 7\n"
"s_lshl_b32 s72, s72, 2\n"
"s_add_u32 s72, s72, s65\n"
"s_lshl_b32 s72, s72, 5\n"
"s_lshr_b32 s71, s64, 3\n"
"s_add_u32 s71, s71, s72\n"
"s_lshl_b32 s71, s71, 7\n"
"s_add_u32 s71, s71, 3407872\n"
"s_waitcnt vmcnt(0)\n"
"v_mov_b32 v4, s68\n"
"v_mov_b32 v5, s69\n"
"v_min_u32 v6, 1, v1\n"
"v_lshlrev_b32 v6, 30, v6\n"
"buffer_store_dwordx2 v[4:5], v6, s[36:39], s71 offen sc1\n"
"v_add_u32 v3, 159744, v3\n"
"s_lshl_b32 s71, s44, 9\n"
"s_lshl_b32 s76, s96, 12\n"
"v_lshlrev_b32 v4, 4, v0\n"
"buffer_load_dwordx4 a[64:67], v4, s[52:55], s76 offen\n"
"v_add_u32 v240, s71, v3\n"
"s_lshl_b32 s71, s45, 9\n"
"v_add_u32 v241, s71, v3\n"
"s_lshl_b32 s71, s46, 9\n"
"v_add_u32 v242, s71, v3\n"
"buffer_load_dwordx2 a[68:69], v4, s[52:55], s76 offen offset:16\n"
"s_lshl_b32 s71, s47, 9\n"
"v_add_u32 v243, s71, v3\n"
"v_and_b32 v2, 3, v1\n"
"v_bfe_u32 v3, v1, 2, 1\n"
"v_bfe_u32 v183, v1, 3, 1\n"
"v_add_u32 v5, s65, v1\n"
"v_subrev_u32 v5, 63, v5\n"
"v_lshlrev_b32 v4, 1, v5\n"
"v_cmp_gt_i32 vcc, 0, v5\n"
"v_mov_b32 v5, 2146435072\n"
"s_nop 0\n"
"v_cndmask_b32 v4, v4, v5, vcc\n"
"buffer_load_ushort v195, v4, s[52:55], s76 offen\n"
"v_lshl_add_u32 v182, v3, 1, v183\n"
"v_lshl_add_u32 v2, v182, 2, v2\n"
"v_lshrrev_b32 v3, 4, v1\n"
"v_xor_b32 v3, v3, v182\n"
"v_lshlrev_b32 v2, 5, v2\n"
"s_lshl_b32 s77, s96, 1\n"
"v_mov_b32 v4, s77\n"
"global_load_ushort v196, v4, s[14:15]\n"
"v_lshl_add_u32 v2, v3, 3, v2\n"
"s_lshl_b32 s71, s65, 11\n"
"v_add_u32 v244, s71, v2\n"
"v_lshrrev_b32 v2, 5, v1\n"
"v_lshlrev_b32 v2, 4, v2\n"
"s_lshl_b32 s76, s98, 12\n"
"v_lshlrev_b32 v4, 4, v0\n"
"buffer_load_dwordx4 a[72:75], v4, s[52:55], s76 offen\n"
"v_bfe_u32 v3, v1, 4, 1\n"
"v_lshl_add_u32 v2, v3, 2, v2\n"
"v_bfe_u32 v183, v1, 2, 2\n"
"v_add_u32 v2, v2, v183\n"
"v_lshlrev_b32 v2, 5, v2\n"
"buffer_load_dwordx2 a[76:77], v4, s[52:55], s76 offen offset:16\n"
"v_and_b32 v183, 3, v1\n"
"v_xor_b32 v183, v183, v3\n"
"v_lshl_add_u32 v182, v183, 3, v2\n"
"v_add_u32 v245, s71, v182\n"
"v_xor_b32 v183, 2, v183\n"
"v_add_u32 v5, s65, v1\n"
"v_subrev_u32 v5, 63, v5\n"
"v_lshlrev_b32 v4, 1, v5\n"
"v_cmp_gt_i32 vcc, 0, v5\n"
"v_mov_b32 v5, 2146435072\n"
"s_nop 0\n"
"v_cndmask_b32 v4, v4, v5, vcc\n"
"buffer_load_ushort v197, v4, s[52:55], s76 offen\n"
"v_lshl_add_u32 v182, v183, 3, v2\n"
"v_add_u32 v182, 256, v182\n"
"v_add_u32 v246, s71, v182\n"
"v_lshlrev_b32 v247, 4, v1\n"
"s_lshl_b32 s77, s98, 1\n"
"v_mov_b32 v4, s77\n"
"global_load_ushort v198, v4, s[14:15]\n"
"v_and_b32 v6, 15, v1\n"
"v_lshlrev_b32 v6, 1, v6\n"
"s_lshl_b32 s76, s66, 2\n"
"s_add_u32 s76, s76, s44\n"
"s_lshl_b32 s76, s76, 6\n"
"v_add_u32 v7, s76, v6\n"
"global_load_ushort a88, v7, s[20:21] offset:0\n"
"global_load_ushort a89, v7, s[20:21] offset:32\n"
"s_lshl_b32 s76, s66, 2\n"
"s_add_u32 s76, s76, s45\n"
"s_lshl_b32 s76, s76, 6\n"
"v_add_u32 v7, s76, v6\n"
"global_load_ushort a90, v7, s[20:21] offset:0\n"
"global_load_ushort a91, v7, s[20:21] offset:32\n"
"s_lshl_b32 s76, s66, 2\n"
"s_add_u32 s76, s76, s46\n"
"s_lshl_b32 s76, s76, 6\n"
"v_add_u32 v7, s76, v6\n"
"global_load_ushort a92, v7, s[20:21] offset:0\n"
"global_load_ushort a93, v7, s[20:21] offset:32\n"
"s_lshl_b32 s76, s66, 2\n"
"s_add_u32 s76, s76, s47\n"
"s_lshl_b32 s76, s76, 6\n"
"v_add_u32 v7, s76, v6\n"
"global_load_ushort a94, v7, s[20:21] offset:0\n"
"global_load_ushort a95, v7, s[20:21] offset:32\n"
"buffer_load_dwordx4 v[152:155], v176, s[36:39], s27 offen offset:0 sc1\n"
"v_pk_mul_f32 v[2:3], v[88:89], v[88:89]\n"
"v_pk_mul_f32 v[4:5], v[90:91], v[90:91]\n"
"v_pk_mul_f32 v[6:7], v[120:121], v[120:121]\n"
"v_pk_mul_f32 v[192:193], v[122:123], v[122:123]\n"
"v_pk_fma_f32 v[2:3], v[2:3], v[218:219], v[216:217] op_sel:[0,0,1] op_sel_hi:[1,0,1]\n"
"v_pk_fma_f32 v[4:5], v[4:5], v[218:219], v[216:217] op_sel:[0,0,1] op_sel_hi:[1,0,1]\n"
"buffer_load_dwordx4 v[156:159], v176, s[36:39], s27 offen offset:2048 sc1\n"
"v_pk_fma_f32 v[6:7], v[6:7], v[218:219], v[216:217] op_sel:[0,0,1] op_sel_hi:[1,0,1]\n"
"v_pk_fma_f32 v[192:193], v[192:193], v[218:219], v[216:217] op_sel:[0,0,1] op_sel_hi:[1,0,1]\n"
"v_pk_mul_f32 v[2:3], v[88:89], v[2:3]\n"
"v_pk_mul_f32 v[4:5], v[90:91], v[4:5]\n"
"v_pk_mul_f32 v[6:7], v[120:121], v[6:7]\n"
"v_pk_mul_f32 v[192:193], v[122:123], v[192:193]\n"
"buffer_load_dwordx4 v[160:163], v177, s[36:39], s27 offen offset:0 sc1\n"
"v_exp_f32 v2, v2\n"
"v_exp_f32 v3, v3\n"
"v_exp_f32 v4, v4\n"
"v_exp_f32 v5, v5\n"
"v_exp_f32 v6, v6\n"
"v_exp_f32 v7, v7\n"
"buffer_load_dwordx4 v[184:187], v177, s[36:39], s27 offen offset:2048 sc1\n"
"v_exp_f32 v192, v192\n"
"v_exp_f32 v193, v193\n"
"v_pk_fma_f32 v[2:3], v[2:3], v[218:219], v[218:219] op_sel:[0,1,1] op_sel_hi:[1,1,1]\n"
"v_pk_fma_f32 v[4:5], v[4:5], v[218:219], v[218:219] op_sel:[0,1,1] op_sel_hi:[1,1,1]\n"
"v_pk_fma_f32 v[6:7], v[6:7], v[218:219], v[218:219] op_sel:[0,1,1] op_sel_hi:[1,1,1]\n"
"v_pk_fma_f32 v[192:193], v[192:193], v[218:219], v[218:219] op_sel:[0,1,1] op_sel_hi:[1,1,1]\n"
"buffer_load_dwordx4 v[188:191], v178, s[36:39], s27 offen offset:0 sc1\n"
"v_rcp_f32 v2, v2\n"
"v_rcp_f32 v3, v3\n"
"v_rcp_f32 v4, v4\n"
"v_rcp_f32 v5, v5\n"
"v_rcp_f32 v6, v6\n"
"v_rcp_f32 v7, v7\n"
"buffer_load_dwordx4 v[200:203], v178, s[36:39], s27 offen offset:2048 sc1\n"
"v_rcp_f32 v192, v192\n"
"v_rcp_f32 v193, v193\n"
"v_pk_mul_f32 v[88:89], v[88:89], v[2:3]\n"
"v_pk_mul_f32 v[90:91], v[90:91], v[4:5]\n"
"v_pk_mul_f32 v[120:121], v[120:121], v[6:7]\n"
"v_pk_mul_f32 v[122:123], v[122:123], v[192:193]\n"
"buffer_load_dwordx4 v[204:207], v179, s[36:39], s27 offen offset:0 sc1\n"
"v_pk_mul_f32 v[2:3], v[92:93], v[92:93]\n"
"v_pk_mul_f32 v[4:5], v[94:95], v[94:95]\n"
"v_pk_mul_f32 v[6:7], v[124:125], v[124:125]\n"
"v_pk_mul_f32 v[192:193], v[126:127], v[126:127]\n"
"v_pk_fma_f32 v[2:3], v[2:3], v[222:223], v[220:221] op_sel:[0,0,1] op_sel_hi:[1,0,1]\n"
"v_pk_fma_f32 v[4:5], v[4:5], v[222:223], v[220:221] op_sel:[0,0,1] op_sel_hi:[1,0,1]\n"
"buffer_load_dwordx4 v[248:251], v179, s[36:39], s27 offen offset:2048 sc1\n"
"v_pk_fma_f32 v[6:7], v[6:7], v[222:223], v[220:221] op_sel:[0,0,1] op_sel_hi:[1,0,1]\n"
"v_pk_fma_f32 v[192:193], v[192:193], v[222:223], v[220:221] op_sel:[0,0,1] op_sel_hi:[1,0,1]\n"
"v_pk_mul_f32 v[2:3], v[92:93], v[2:3]\n"
"v_pk_mul_f32 v[4:5], v[94:95], v[4:5]\n"
"v_pk_mul_f32 v[6:7], v[124:125], v[6:7]\n"
"v_pk_mul_f32 v[192:193], v[126:127], v[192:193]\n"
"v_exp_f32 v2, v2\n"
"v_exp_f32 v3, v3\n"
"v_exp_f32 v4, v4\n"
"v_exp_f32 v5, v5\n"
"v_exp_f32 v6, v6\n"
"v_exp_f32 v7, v7\n"
"v_exp_f32 v192, v192\n"
"v_exp_f32 v193, v193\n"
"v_pk_fma_f32 v[2:3], v[2:3], v[222:223], v[222:223] op_sel:[0,1,1] op_sel_hi:[1,1,1]\n"
"v_pk_fma_f32 v[4:5], v[4:5], v[222:223], v[222:223] op_sel:[0,1,1] op_sel_hi:[1,1,1]\n"
"v_pk_fma_f32 v[6:7], v[6:7], v[222:223], v[222:223] op_sel:[0,1,1] op_sel_hi:[1,1,1]\n"
"v_pk_fma_f32 v[192:193], v[192:193], v[222:223], v[222:223] op_sel:[0,1,1] op_sel_hi:[1,1,1]\n"
"v_rcp_f32 v2, v2\n"
"v_rcp_f32 v3, v3\n"
"v_rcp_f32 v4, v4\n"
"v_rcp_f32 v5, v5\n"
"v_rcp_f32 v6, v6\n"
"v_rcp_f32 v7, v7\n"
"v_rcp_f32 v192, v192\n"
"v_rcp_f32 v193, v193\n"
"v_pk_mul_f32 v[92:93], v[92:93], v[2:3]\n"
"v_pk_mul_f32 v[94:95], v[94:95], v[4:5]\n"
"v_pk_mul_f32 v[124:125], v[124:125], v[6:7]\n"
"v_pk_mul_f32 v[126:127], v[126:127], v[192:193]\n"
"v_pk_mul_f32 v[2:3], v[96:97], v[96:97]\n"
"v_pk_mul_f32 v[4:5], v[98:99], v[98:99]\n"
"v_pk_mul_f32 v[6:7], v[128:129], v[128:129]\n"
"v_pk_mul_f32 v[192:193], v[130:131], v[130:131]\n"
"v_pk_fma_f32 v[2:3], v[2:3], v[226:227], v[224:225] op_sel:[0,0,1] op_sel_hi:[1,0,1]\n"
"v_pk_fma_f32 v[4:5], v[4:5], v[226:227], v[224:225] op_sel:[0,0,1] op_sel_hi:[1,0,1]\n"
"v_pk_fma_f32 v[6:7], v[6:7], v[226:227], v[224:225] op_sel:[0,0,1] op_sel_hi:[1,0,1]\n"
"v_pk_fma_f32 v[192:193], v[192:193], v[226:227], v[224:225] op_sel:[0,0,1] op_sel_hi:[1,0,1]\n"
"v_pk_mul_f32 v[2:3], v[96:97], v[2:3]\n"
"v_pk_mul_f32 v[4:5], v[98:99], v[4:5]\n"
"v_pk_mul_f32 v[6:7], v[128:129], v[6:7]\n"
"v_pk_mul_f32 v[192:193], v[130:131], v[192:193]\n"
"v_exp_f32 v2, v2\n"
"v_exp_f32 v3, v3\n"
"v_exp_f32 v4, v4\n"
"v_exp_f32 v5, v5\n"
"v_exp_f32 v6, v6\n"
"v_exp_f32 v7, v7\n"
"v_exp_f32 v192, v192\n"
"v_exp_f32 v193, v193\n"
"v_pk_fma_f32 v[2:3], v[2:3], v[226:227], v[226:227] op_sel:[0,1,1] op_sel_hi:[1,1,1]\n"
"v_pk_fma_f32 v[4:5], v[4:5], v[226:227], v[226:227] op_sel:[0,1,1] op_sel_hi:[1,1,1]\n"
"v_pk_fma_f32 v[6:7], v[6:7], v[226:227], v[226:227] op_sel:[0,1,1] op_sel_hi:[1,1,1]\n"
"v_pk_fma_f32 v[192:193], v[192:193], v[226:227], v[226:227] op_sel:[0,1,1] op_sel_hi:[1,1,1]\n"
"v_rcp_f32 v2, v2\n"
"v_rcp_f32 v3, v3\n"
"v_rcp_f32 v4, v4\n"
"v_rcp_f32 v5, v5\n"
"v_rcp_f32 v6, v6\n"
"v_rcp_f32 v7, v7\n"
"v_rcp_f32 v192, v192\n"
"v_rcp_f32 v193, v193\n"
"v_pk_mul_f32 v[96:97], v[96:97], v[2:3]\n"
"v_pk_mul_f32 v[98:99], v[98:99], v[4:5]\n"
"v_pk_mul_f32 v[128:129], v[128:129], v[6:7]\n"
"v_pk_mul_f32 v[130:131], v[130:131], v[192:193]\n"
"v_pk_mul_f32 v[2:3], v[100:101], v[100:101]\n"
"v_pk_mul_f32 v[4:5], v[102:103], v[102:103]\n"
"v_pk_mul_f32 v[6:7], v[132:133], v[132:133]\n"
"v_pk_mul_f32 v[192:193], v[134:135], v[134:135]\n"
"v_pk_fma_f32 v[2:3], v[2:3], v[230:231], v[228:229] op_sel:[0,0,1] op_sel_hi:[1,0,1]\n"
"v_pk_fma_f32 v[4:5], v[4:5], v[230:231], v[228:229] op_sel:[0,0,1] op_sel_hi:[1,0,1]\n"
"v_pk_fma_f32 v[6:7], v[6:7], v[230:231], v[228:229] op_sel:[0,0,1] op_sel_hi:[1,0,1]\n"
"v_pk_fma_f32 v[192:193], v[192:193], v[230:231], v[228:229] op_sel:[0,0,1] op_sel_hi:[1,0,1]\n"
"v_pk_mul_f32 v[2:3], v[100:101], v[2:3]\n"
"v_pk_mul_f32 v[4:5], v[102:103], v[4:5]\n"
"v_pk_mul_f32 v[6:7], v[132:133], v[6:7]\n"
"v_pk_mul_f32 v[192:193], v[134:135], v[192:193]\n"
"v_exp_f32 v2, v2\n"
"v_exp_f32 v3, v3\n"
"v_exp_f32 v4, v4\n"
"v_exp_f32 v5, v5\n"
"v_exp_f32 v6, v6\n"
"v_exp_f32 v7, v7\n"
"v_exp_f32 v192, v192\n"
"v_exp_f32 v193, v193\n"
"v_pk_fma_f32 v[2:3], v[2:3], v[230:231], v[230:231] op_sel:[0,1,1] op_sel_hi:[1,1,1]\n"
"v_pk_fma_f32 v[4:5], v[4:5], v[230:231], v[230:231] op_sel:[0,1,1] op_sel_hi:[1,1,1]\n"
"v_pk_fma_f32 v[6:7], v[6:7], v[230:231], v[230:231] op_sel:[0,1,1] op_sel_hi:[1,1,1]\n"
"v_pk_fma_f32 v[192:193], v[192:193], v[230:231], v[230:231] op_sel:[0,1,1] op_sel_hi:[1,1,1]\n"
"v_rcp_f32 v2, v2\n"
"v_rcp_f32 v3, v3\n"
"v_rcp_f32 v4, v4\n"
"v_rcp_f32 v5, v5\n"
"v_rcp_f32 v6, v6\n"
"v_rcp_f32 v7, v7\n"
"v_rcp_f32 v192, v192\n"
"v_rcp_f32 v193, v193\n"
"v_pk_mul_f32 v[100:101], v[100:101], v[2:3]\n"
"v_pk_mul_f32 v[102:103], v[102:103], v[4:5]\n"
"v_pk_mul_f32 v[132:133], v[132:133], v[6:7]\n"
"v_pk_mul_f32 v[134:135], v[134:135], v[192:193]\n"
"v_pk_mul_f32 v[2:3], v[104:105], v[104:105]\n"
"v_pk_mul_f32 v[4:5], v[106:107], v[106:107]\n"
"v_pk_mul_f32 v[6:7], v[136:137], v[136:137]\n"
"v_pk_mul_f32 v[192:193], v[138:139], v[138:139]\n"
"v_pk_fma_f32 v[2:3], v[2:3], v[234:235], v[232:233] op_sel:[0,0,1] op_sel_hi:[1,0,1]\n"
"v_pk_fma_f32 v[4:5], v[4:5], v[234:235], v[232:233] op_sel:[0,0,1] op_sel_hi:[1,0,1]\n"
"v_pk_fma_f32 v[6:7], v[6:7], v[234:235], v[232:233] op_sel:[0,0,1] op_sel_hi:[1,0,1]\n"
"v_pk_fma_f32 v[192:193], v[192:193], v[234:235], v[232:233] op_sel:[0,0,1] op_sel_hi:[1,0,1]\n"
"v_pk_mul_f32 v[2:3], v[104:105], v[2:3]\n"
"v_pk_mul_f32 v[4:5], v[106:107], v[4:5]\n"
"v_pk_mul_f32 v[6:7], v[136:137], v[6:7]\n"
"v_pk_mul_f32 v[192:193], v[138:139], v[192:193]\n"
"v_exp_f32 v2, v2\n"
"v_exp_f32 v3, v3\n"
"v_exp_f32 v4, v4\n"
"v_exp_f32 v5, v5\n"
"v_exp_f32 v6, v6\n"
"v_exp_f32 v7, v7\n"
"v_exp_f32 v192, v192\n"
"v_exp_f32 v193, v193\n"
"v_pk_fma_f32 v[2:3], v[2:3], v[234:235], v[234:235] op_sel:[0,1,1] op_sel_hi:[1,1,1]\n"
"v_pk_fma_f32 v[4:5], v[4:5], v[234:235], v[234:235] op_sel:[0,1,1] op_sel_hi:[1,1,1]\n"
"v_pk_fma_f32 v[6:7], v[6:7], v[234:235], v[234:235] op_sel:[0,1,1] op_sel_hi:[1,1,1]\n"
"v_pk_fma_f32 v[192:193], v[192:193], v[234:235], v[234:235] op_sel:[0,1,1] op_sel_hi:[1,1,1]\n"
"v_rcp_f32 v2, v2\n"
"v_rcp_f32 v3, v3\n"
"v_rcp_f32 v4, v4\n"
"v_rcp_f32 v5, v5\n"
"v_rcp_f32 v6, v6\n"
"v_rcp_f32 v7, v7\n"
"v_rcp_f32 v192, v192\n"
"v_rcp_f32 v193, v193\n"
"v_pk_mul_f32 v[104:105], v[104:105], v[2:3]\n"
"v_pk_mul_f32 v[106:107], v[106:107], v[4:5]\n"
"v_pk_mul_f32 v[136:137], v[136:137], v[6:7]\n"
"v_pk_mul_f32 v[138:139], v[138:139], v[192:193]\n"
"v_pk_mul_f32 v[2:3], v[108:109], v[108:109]\n"
"v_pk_mul_f32 v[4:5], v[110:111], v[110:111]\n"
"v_pk_mul_f32 v[6:7], v[140:141], v[140:141]\n"
"v_pk_mul_f32 v[192:193], v[142:143], v[142:143]\n"
"v_pk_fma_f32 v[2:3], v[2:3], v[238:239], v[236:237] op_sel:[0,0,1] op_sel_hi:[1,0,1]\n"
"v_pk_fma_f32 v[4:5], v[4:5], v[238:239], v[236:237] op_sel:[0,0,1] op_sel_hi:[1,0,1]\n"
"v_pk_fma_f32 v[6:7], v[6:7], v[238:239], v[236:237] op_sel:[0,0,1] op_sel_hi:[1,0,1]\n"
"v_pk_fma_f32 v[192:193], v[192:193], v[238:239], v[236:237] op_sel:[0,0,1] op_sel_hi:[1,0,1]\n"
"v_pk_mul_f32 v[2:3], v[108:109], v[2:3]\n"
"v_pk_mul_f32 v[4:5], v[110:111], v[4:5]\n"
"v_pk_mul_f32 v[6:7], v[140:141], v[6:7]\n"
"v_pk_mul_f32 v[192:193], v[142:143], v[192:193]\n"
"v_exp_f32 v2, v2\n"
"v_exp_f32 v3, v3\n"
"v_exp_f32 v4, v4\n"
"v_exp_f32 v5, v5\n"
"v_exp_f32 v6, v6\n"
"v_exp_f32 v7, v7\n"
"v_exp_f32 v192, v192\n"
"v_exp_f32 v193, v193\n"
"v_pk_fma_f32 v[2:3], v[2:3], v[238:239], v[238:239] op_sel:[0,1,1] op_sel_hi:[1,1,1]\n"
"v_pk_fma_f32 v[4:5], v[4:5], v[238:239], v[238:239] op_sel:[0,1,1] op_sel_hi:[1,1,1]\n"
"v_pk_fma_f32 v[6:7], v[6:7], v[238:239], v[238:239] op_sel:[0,1,1] op_sel_hi:[1,1,1]\n"
"v_pk_fma_f32 v[192:193], v[192:193], v[238:239], v[238:239] op_sel:[0,1,1] op_sel_hi:[1,1,1]\n"
"v_rcp_f32 v2, v2\n"
"v_rcp_f32 v3, v3\n"
"v_rcp_f32 v4, v4\n"
"v_rcp_f32 v5, v5\n"
"v_rcp_f32 v6, v6\n"
"v_rcp_f32 v7, v7\n"
"v_rcp_f32 v192, v192\n"
"v_rcp_f32 v193, v193\n"
"v_pk_mul_f32 v[108:109], v[108:109], v[2:3]\n"
"v_pk_mul_f32 v[110:111], v[110:111], v[4:5]\n"
"v_pk_mul_f32 v[140:141], v[140:141], v[6:7]\n"
"v_pk_mul_f32 v[142:143], v[142:143], v[192:193]\n"
"s_mul_i32 s75, s67, 9\n"
"s_lshl_b32 s73, s65, 1\n"
"s_add_u32 s73, s73, s75\n"
"s_lshl_b32 s73, s73, 16\n"
"s_lshl_b32 s74, s66, 12\n"
"s_add_u32 s73, s73, s74\n"
"s_add_u32 s73, s73, 10485760\n"
"s_add_u32 s75, s75, 8\n"
"s_lshl_b32 s75, s75, 16\n"
"s_add_u32 s75, s75, s74\n"
"s_lshl_b32 s76, s65, 10\n"
"s_add_u32 s75, s75, s76\n"
"s_add_u32 s75, s75, 10485760\n"
"s_waitcnt vmcnt(0)\n"
"poll2c_%=:\n"
"v_xor_b32 v193, s68, v153\n"
"v_xor_b32 v194, s69, v155\n"
"v_or_b32 v192, v193, v194\n"
"v_xor_b32 v193, s68, v157\n"
"v_xor_b32 v194, s69, v159\n"
"v_or3_b32 v192, v192, v193, v194\n"
"v_xor_b32 v193, s68, v161\n"
"v_xor_b32 v194, s69, v163\n"
"v_or3_b32 v192, v192, v193, v194\n"
"v_xor_b32 v193, s68, v185\n"
"v_xor_b32 v194, s69, v187\n"
"v_or3_b32 v192, v192, v193, v194\n"
"v_xor_b32 v193, s68, v189\n"
"v_xor_b32 v194, s69, v191\n"
"v_or3_b32 v192, v192, v193, v194\n"
"v_xor_b32 v193, s68, v201\n"
"v_xor_b32 v194, s69, v203\n"
"v_or3_b32 v192, v192, v193, v194\n"
"v_xor_b32 v193, s68, v205\n"
"v_xor_b32 v194, s69, v207\n"
"v_or3_b32 v192, v192, v193, v194\n"
"v_xor_b32 v193, s68, v249\n"
"v_xor_b32 v194, s69, v251\n"
"v_or3_b32 v192, v192, v193, v194\n"
"v_cmp_ne_u32 vcc, 0, v192\n"
"s_cbranch_vccz poll2d_%=\n"
"s_sub_u32 s70, s70, 1\n"
"s_cbranch_scc1 poll2t_%=\n"
"s_sleep 2\n"
"buffer_load_dwordx4 v[152:155], v176, s[36:39], s27 offen offset:0 sc1\n"
"buffer_load_dwordx4 v[156:159], v176, s[36:39], s27 offen offset:2048 sc1\n"
"buffer_load_dwordx4 v[160:163], v177, s[36:39], s27 offen offset:0 sc1\n"
"buffer_load_dwordx4 v[184:187], v177, s[36:39], s27 offen offset:2048 sc1\n"
"buffer_load_dwordx4 v[188:191], v178, s[36:39], s27 offen offset:0 sc1\n"
"buffer_load_dwordx4 v[200:203], v178, s[36:39], s27 offen offset:2048 sc1\n"
"buffer_load_dwordx4 v[204:207], v179, s[36:39], s27 offen offset:0 sc1\n"
"buffer_load_dwordx4 v[248:251], v179, s[36:39], s27 offen offset:2048 sc1\n"
"s_waitcnt vmcnt(0)\n"
"s_branch poll2c_%=\n"
"poll2t_%=:\n"
"s_mov_b32 s70, 0\n"
"poll2d_%=:\n"
"v_add_f32 v152, v152, v156\n"
"v_add_f32 v154, v154, v158\n"
"v_add_f32 v152, v152, v160\n"
"v_add_f32 v154, v154, v162\n"
"v_add_f32 v152, v152, v184\n"
"v_add_f32 v154, v154, v186\n"
"v_add_f32 v152, v152, v188\n"
"v_add_f32 v154, v154, v190\n"
"v_add_f32 v152, v152, v200\n"
"v_add_f32 v154, v154, v202\n"
"v_add_f32 v152, v152, v204\n"
"v_add_f32 v154, v154, v206\n"
"v_add_f32 v152, v152, v248\n"
"v_add_f32 v154, v154, v250\n"
"v_mov_b32 v153, v154\n"
"ds_write_b64 v252, v[152:153]\n"
"s_waitcnt lgkmcnt(0)\n"
"s_barrier\n"
"buffer_load_dwordx2 v[254:255], v224, s[36:39], s8 offen sc1\n"
"ds_read_b128 v[152:155], v240 offset:0\n"
"ds_read_b128 v[156:159], v240 offset:256\n"
"ds_read_b128 v[160:163], v241 offset:0\n"
"ds_read_b128 v[184:187], v241 offset:256\n"
"ds_read_b128 v[188:191], v242 offset:0\n"
"ds_read_b128 v[200:203], v242 offset:256\n"
"ds_read_b128 v[204:207], v243 offset:0\n"
"ds_read_b128 v[248:251], v243 offset:256\n"
"s_waitcnt lgkmcnt(6)\n"
"v_pk_add_f32 v[152:153], v[152:153], v[154:155]\n"
"v_mov_b32 v154, 0x3727c5ac\n"
"v_mul_f32 v152, 0x39e38e39, v152\n"
"v_fmac_f32 v154, 0x39e38e39, v153\n"
"v_fma_f32 v154, -v152, v152, v154\n"
"v_max_f32 v154, 0x3727c5ac, v154\n"
"v_rsq_f32 v154, v154\n"
"v_pk_add_f32 v[156:157], v[156:157], v[158:159]\n"
"v_mov_b32 v158, 0x3727c5ac\n"
"v_mul_f32 v156, 0x39e38e39, v156\n"
"v_fmac_f32 v158, 0x39e38e39, v157\n"
"v_fma_f32 v158, -v156, v156, v158\n"
"v_max_f32 v158, 0x3727c5ac, v158\n"
"v_rsq_f32 v158, v158\n"
"s_waitcnt lgkmcnt(4)\n"
"v_pk_add_f32 v[160:161], v[160:161], v[162:163]\n"
"v_mov_b32 v162, 0x3727c5ac\n"
"v_mul_f32 v160, 0x39e38e39, v160\n"
"v_fmac_f32 v162, 0x39e38e39, v161\n"
"v_fma_f32 v162, -v160, v160, v162\n"
"v_max_f32 v162, 0x3727c5ac, v162\n"
"v_rsq_f32 v162, v162\n"
"v_pk_add_f32 v[184:185], v[184:185], v[186:187]\n"
"v_mov_b32 v186, 0x3727c5ac\n"
"v_mul_f32 v184, 0x39e38e39, v184\n"
"v_fmac_f32 v186, 0x39e38e39, v185\n"
"v_fma_f32 v186, -v184, v184, v186\n"
"v_max_f32 v186, 0x3727c5ac, v186\n"
"v_rsq_f32 v186, v186\n"
"s_waitcnt lgkmcnt(2)\n"
"v_pk_add_f32 v[188:189], v[188:189], v[190:191]\n"
"v_mov_b32 v190, 0x3727c5ac\n"
"v_mul_f32 v188, 0x39e38e39, v188\n"
"v_fmac_f32 v190, 0x39e38e39, v189\n"
"v_fma_f32 v190, -v188, v188, v190\n"
"v_max_f32 v190, 0x3727c5ac, v190\n"
"v_rsq_f32 v190, v190\n"
"v_pk_add_f32 v[200:201], v[200:201], v[202:203]\n"
"v_mov_b32 v202, 0x3727c5ac\n"
"v_mul_f32 v200, 0x39e38e39, v200\n"
"v_fmac_f32 v202, 0x39e38e39, v201\n"
"v_fma_f32 v202, -v200, v200, v202\n"
"v_max_f32 v202, 0x3727c5ac, v202\n"
"v_rsq_f32 v202, v202\n"
"s_waitcnt lgkmcnt(0)\n"
"v_pk_add_f32 v[204:205], v[204:205], v[206:207]\n"
"v_mov_b32 v206, 0x3727c5ac\n"
"v_mul_f32 v204, 0x39e38e39, v204\n"
"v_fmac_f32 v206, 0x39e38e39, v205\n"
"v_fma_f32 v206, -v204, v204, v206\n"
"v_max_f32 v206, 0x3727c5ac, v206\n"
"v_rsq_f32 v206, v206\n"
"v_pk_add_f32 v[248:249], v[248:249], v[250:251]\n"
"v_mov_b32 v250, 0x3727c5ac\n"
"v_mul_f32 v248, 0x39e38e39, v248\n"
"v_fmac_f32 v250, 0x39e38e39, v249\n"
"v_fma_f32 v250, -v248, v248, v250\n"
"v_max_f32 v250, 0x3727c5ac, v250\n"
"v_rsq_f32 v250, v250\n"
"v_mul_f32_e64 v155, -v152, v154\n"
"v_mul_f32_e64 v159, -v156, v158\n"
"v_mul_f32_e64 v163, -v160, v162\n"
"v_mul_f32_e64 v187, -v184, v186\n"
"v_mul_f32_e64 v191, -v188, v190\n"
"v_mul_f32_e64 v203, -v200, v202\n"
"v_mul_f32_e64 v207, -v204, v206\n"
"v_mul_f32_e64 v251, -v248, v250\n"
"s_waitcnt vmcnt(0)\n"
"poll3c_%=:\n"
"v_xor_b32 v193, s68, v254\n"
"v_xor_b32 v194, s69, v255\n"
"v_or_b32 v192, v193, v194\n"
"v_cmp_ne_u32 vcc, 0, v192\n"
"s_cbranch_vccz poll3d_%=\n"
"s_sub_u32 s70, s70, 1\n"
"s_cbranch_scc1 poll3t_%=\n"
"s_sleep 2\n"
"buffer_load_dwordx2 v[254:255], v224, s[36:39], s8 offen sc1\n"
"s_waitcnt vmcnt(0)\n"
"s_branch poll3c_%=\n"
"poll3t_%=:\n"
"s_mov_b32 s70, 0\n"
"poll3d_%=:\n"
"s_lshl_b32 s71, s96, 12\n"
"s_add_u32 s71, s71, 1048576\n"
"buffer_load_dwordx4 a[80:83], v171, s[36:39], s71 offen sc1\n"
"s_lshl_b32 s71, s98, 12\n"
"s_add_u32 s71, s71, 1048576\n"
"buffer_load_dwordx4 a[84:87], v171, s[36:39], s71 offen sc1\n"
"v_pk_fma_f32 v[8:9], v[8:9], v[154:155], v[154:155] op_sel:[0,0,1] op_sel_hi:[1,0,1]\n"
"v_pk_fma_f32 v[10:11], v[10:11], v[154:155], v[154:155] op_sel:[0,0,1] op_sel_hi:[1,0,1]\n"
"v_cvt_pk_bf16_f32 v8, v8, v9\n"
"v_cvt_pk_bf16_f32 v9, v10, v11\n"
"ds_write_b64 v244, v[8:9] offset:0\n"
"v_pk_fma_f32 v[12:13], v[12:13], v[158:159], v[158:159] op_sel:[0,0,1] op_sel_hi:[1,0,1]\n"
"v_pk_fma_f32 v[14:15], v[14:15], v[158:159], v[158:159] op_sel:[0,0,1] op_sel_hi:[1,0,1]\n"
"v_cvt_pk_bf16_f32 v12, v12, v13\n"
"v_cvt_pk_bf16_f32 v13, v14, v15\n"
"ds_write_b64 v244, v[12:13] offset:512\n"
"ds_read_b64_tr_b16 a[0:1], v245 offset:0\n"
"ds_read_b64_tr_b16 a[2:3], v246 offset:0\n"
"v_pk_fma_f32 v[16:17], v[16:17], v[162:163], v[162:163] op_sel:[0,0,1] op_sel_hi:[1,0,1]\n"
"v_pk_fma_f32 v[18:19], v[18:19], v[162:163], v[162:163] op_sel:[0,0,1] op_sel_hi:[1,0,1]\n"
"v_cvt_pk_bf16_f32 v16, v16, v17\n"
"v_cvt_pk_bf16_f32 v17, v18, v19\n"
"ds_write_b64 v244, v[16:17] offset:1024\n"
"v_pk_fma_f32 v[20:21], v[20:21], v[186:187], v[186:187] op_sel:[0,0,1] op_sel_hi:[1,0,1]\n"
"v_pk_fma_f32 v[22:23], v[22:23], v[186:187], v[186:187] op_sel:[0,0,1] op_sel_hi:[1,0,1]\n"
"v_cvt_pk_bf16_f32 v20, v20, v21\n"
"v_cvt_pk_bf16_f32 v21, v22, v23\n"
"ds_write_b64 v244, v[20:21] offset:1536\n"
"ds_read_b64_tr_b16 a[4:5], v245 offset:1024\n"
"ds_read_b64_tr_b16 a[6:7], v246 offset:1024\n"
"v_pk_fma_f32 v[24:25], v[24:25], v[190:191], v[190:191] op_sel:[0,0,1] op_sel_hi:[1,0,1]\n"
"v_pk_fma_f32 v[26:27], v[26:27], v[190:191], v[190:191] op_sel:[0,0,1] op_sel_hi:[1,0,1]\n"
"v_cvt_pk_bf16_f32 v24, v24, v25\n"
"v_cvt_pk_bf16_f32 v25, v26, v27\n"
"ds_write_b64 v244, v[24:25] offset:0\n"
"v_pk_fma_f32 v[28:29], v[28:29], v[202:203], v[202:203] op_sel:[0,0,1] op_sel_hi:[1,0,1]\n"
"v_pk_fma_f32 v[30:31], v[30:31], v[202:203], v[202:203] op_sel:[0,0,1] op_sel_hi:[1,0,1]\n"
"v_cvt_pk_bf16_f32 v28, v28, v29\n"
"v_cvt_pk_bf16_f32 v29, v30, v31\n"
"ds_write_b64 v244, v[28:29] offset:512\n"
"ds_read_b64_tr_b16 a[8:9], v245 offset:0\n"
"ds_read_b64_tr_b16 a[10:11], v246 offset:0\n"
"s_waitcnt lgkmcnt(8)\n"
"s_lshl_b32 s71, s44, 10\n"
"s_add_u32 s71, s71, s73\n"
"buffer_store_dwordx4 a[0:3], v247, s[36:39], s71 offen sc1\n"
"v_pk_fma_f32 v[32:33], v[32:33], v[206:207], v[206:207] op_sel:[0,0,1] op_sel_hi:[1,0,1]\n"
"v_pk_fma_f32 v[34:35], v[34:35], v[206:207], v[206:207] op_sel:[0,0,1] op_sel_hi:[1,0,1]\n"
"v_cvt_pk_bf16_f32 v32, v32, v33\n"
"v_cvt_pk_bf16_f32 v33, v34, v35\n"
"ds_write_b64 v244, v[32:33] offset:1024\n"
"v_pk_fma_f32 v[36:37], v[36:37], v[250:251], v[250:251] op_sel:[0,0,1] op_sel_hi:[1,0,1]\n"
"v_pk_fma_f32 v[38:39], v[38:39], v[250:251], v[250:251] op_sel:[0,0,1] op_sel_hi:[1,0,1]\n"
"v_cvt_pk_bf16_f32 v36, v36, v37\n"
"v_cvt_pk_bf16_f32 v37, v38, v39\n"
"ds_write_b64 v244, v[36:37] offset:1536\n"
"ds_read_b64_tr_b16 a[12:13], v245 offset:1024\n"
"ds_read_b64_tr_b16 a[14:15], v246 offset:1024\n"
"s_waitcnt lgkmcnt(8)\n"
"s_lshl_b32 s71, s45, 10\n"
"s_add_u32 s71, s71, s73\n"
"buffer_store_dwordx4 a[4:7], v247, s[36:39], s71 offen sc1\n"
"v_pk_fma_f32 v[40:41], v[40:41], v[154:155], v[154:155] op_sel:[0,0,1] op_sel_hi:[1,0,1]\n"
"v_pk_fma_f32 v[42:43], v[42:43], v[154:155], v[154:155] op_sel:[0,0,1] op_sel_hi:[1,0,1]\n"
"v_cvt_pk_bf16_f32 v40, v40, v41\n"
"v_cvt_pk_bf16_f32 v41, v42, v43\n"
"ds_write_b64 v244, v[40:41] offset:0\n"
"v_pk_fma_f32 v[44:45], v[44:45], v[158:159], v[158:159] op_sel:[0,0,1] op_sel_hi:[1,0,1]\n"
"v_pk_fma_f32 v[46:47], v[46:47], v[158:159], v[158:159] op_sel:[0,0,1] op_sel_hi:[1,0,1]\n"
"v_cvt_pk_bf16_f32 v44, v44, v45\n"
"v_cvt_pk_bf16_f32 v45, v46, v47\n"
"ds_write_b64 v244, v[44:45] offset:512\n"
"ds_read_b64_tr_b16 a[16:17], v245 offset:0\n"
"ds_read_b64_tr_b16 a[18:19], v246 offset:0\n"
"s_waitcnt lgkmcnt(8)\n"
"s_lshl_b32 s71, s46, 10\n"
"s_add_u32 s71, s71, s73\n"
"buffer_store_dwordx4 a[8:11], v247, s[36:39], s71 offen sc1\n"
"v_pk_fma_f32 v[48:49], v[48:49], v[162:163], v[162:163] op_sel:[0,0,1] op_sel_hi:[1,0,1]\n"
"v_pk_fma_f32 v[50:51], v[50:51], v[162:163], v[162:163] op_sel:[0,0,1] op_sel_hi:[1,0,1]\n"
"v_cvt_pk_bf16_f32 v48, v48, v49\n"
"v_cvt_pk_bf16_f32 v49, v50, v51\n"
"ds_write_b64 v244, v[48:49] offset:1024\n"
"v_pk_fma_f32 v[52:53], v[52:53], v[186:187], v[186:187] op_sel:[0,0,1] op_sel_hi:[1,0,1]\n"
"v_pk_fma_f32 v[54:55], v[54:55], v[186:187], v[186:187] op_sel:[0,0,1] op_sel_hi:[1,0,1]\n"
"v_cvt_pk_bf16_f32 v52, v52, v53\n"
"v_cvt_pk_bf16_f32 v53, v54, v55\n"
"ds_write_b64 v244, v[52:53] offset:1536\n"
"ds_read_b64_tr_b16 a[20:21], v245 offset:1024\n"
"ds_read_b64_tr_b16 a[22:23], v246 offset:1024\n"
"s_waitcnt lgkmcnt(8)\n"
"s_lshl_b32 s71, s47, 10\n"
"s_add_u32 s71, s71, s73\n"
"buffer_store_dwordx4 a[12:15], v247, s[36:39], s71 offen sc1\n"
"v_pk_fma_f32 v[56:57], v[56:57], v[190:191], v[190:191] op_sel:[0,0,1] op_sel_hi:[1,0,1]\n"
"v_pk_fma_f32 v[58:59], v[58:59], v[190:191], v[190:191] op_sel:[0,0,1] op_sel_hi:[1,0,1]\n"
"v_cvt_pk_bf16_f32 v56, v56, v57\n"
"v_cvt_pk_bf16_f32 v57, v58, v59\n"
"ds_write_b64 v244, v[56:57] offset:0\n"
"v_pk_fma_f32 v[60:61], v[60:61], v[202:203], v[202:203] op_sel:[0,0,1] op_sel_hi:[1,0,1]\n"
"v_pk_fma_f32 v[62:63], v[62:63], v[202:203], v[202:203] op_sel:[0,0,1] op_sel_hi:[1,0,1]\n"
"v_cvt_pk_bf16_f32 v60, v60, v61\n"
"v_cvt_pk_bf16_f32 v61, v62, v63\n"
"ds_write_b64 v244, v[60:61] offset:512\n"
"ds_read_b64_tr_b16 a[24:25], v245 offset:0\n"
"ds_read_b64_tr_b16 a[26:27], v246 offset:0\n"
"s_waitcnt lgkmcnt(8)\n"
"s_lshl_b32 s71, s44, 10\n"
"s_add_u32 s71, s71, s73\n"
"s_add_u32 s71, s71, 0x10000\n"
"buffer_store_dwordx4 a[16:19], v247, s[36:39], s71 offen sc1\n"
"v_pk_fma_f32 v[64:65], v[64:65], v[206:207], v[206:207] op_sel:[0,0,1] op_sel_hi:[1,0,1]\n"
"v_pk_fma_f32 v[66:67], v[66:67], v[206:207], v[206:207] op_sel:[0,0,1] op_sel_hi:[1,0,1]\n"
"v_cvt_pk_bf16_f32 v64, v64, v65\n"
"v_cvt_pk_bf16_f32 v65, v66, v67\n"
"ds_write_b64 v244, v[64:65] offset:1024\n"
"v_pk_fma_f32 v[68:69], v[68:69], v[250:251], v[250:251] op_sel:[0,0,1] op_sel_hi:[1,0,1]\n"
"v_pk_fma_f32 v[70:71], v[70:71], v[250:251], v[250:251] op_sel:[0,0,1] op_sel_hi:[1,0,1]\n"
"v_cvt_pk_bf16_f32 v68, v68, v69\n"
"v_cvt_pk_bf16_f32 v69, v70, v71\n"
"ds_write_b64 v244, v[68:69] offset:1536\n"
"ds_read_b64_tr_b16 a[28:29], v245 offset:1024\n"
"ds_read_b64_tr_b16 a[30:31], v246 offset:1024\n"
"s_waitcnt lgkmcnt(8)\n"
"s_lshl_b32 s71, s45, 10\n"
"s_add_u32 s71, s71, s73\n"
"s_add_u32 s71, s71, 0x10000\n"
"buffer_store_dwordx4 a[20:23], v247, s[36:39], s71 offen sc1\n"
"v_pk_fma_f32 v[72:73], v[72:73], v[154:155], v[154:155] op_sel:[0,0,1] op_sel_hi:[1,0,1]\n"
"v_pk_fma_f32 v[74:75], v[74:75], v[154:155], v[154:155] op_sel:[0,0,1] op_sel_hi:[1,0,1]\n"
"v_cvt_pk_bf16_f32 v72, v72, v73\n"
"v_cvt_pk_bf16_f32 v73, v74, v75\n"
"ds_write_b64 v244, v[72:73] offset:0\n"
"v_pk_fma_f32 v[76:77], v[76:77], v[158:159], v[158:159] op_sel:[0,0,1] op_sel_hi:[1,0,1]\n"
"v_pk_fma_f32 v[78:79], v[78:79], v[158:159], v[158:159] op_sel:[0,0,1] op_sel_hi:[1,0,1]\n"
"v_cvt_pk_bf16_f32 v76, v76, v77\n"
"v_cvt_pk_bf16_f32 v77, v78, v79\n"
"ds_write_b64 v244, v[76:77] offset:512\n"
"ds_read_b64_tr_b16 a[32:33], v245 offset:0\n"
"ds_read_b64_tr_b16 a[34:35], v246 offset:0\n"
"s_waitcnt lgkmcnt(8)\n"
"s_lshl_b32 s71, s46, 10\n"
"s_add_u32 s71, s71, s73\n"
"s_add_u32 s71, s71, 0x10000\n"
"buffer_store_dwordx4 a[24:27], v247, s[36:39], s71 offen sc1\n"
"s_waitcnt lgkmcnt(4)\n"
"s_lshl_b32 s71, s47, 10\n"
"s_add_u32 s71, s71, s73\n"
"s_add_u32 s71, s71, 0x10000\n"
"buffer_store_dwordx4 a[28:31], v247, s[36:39], s71 offen sc1\n"
"s_waitcnt lgkmcnt(0)\n"
"buffer_store_dwordx4 a[32:35], v247, s[36:39], s75 offen sc1\n"
"v_accvgpr_read_b32 v152, a88\n"
"v_accvgpr_read_b32 v156, a89\n"
"v_accvgpr_read_b32 v160, a90\n"
"v_accvgpr_read_b32 v204, a91\n"
"v_accvgpr_read_b32 v240, a92\n"
"v_accvgpr_read_b32 v244, a93\n"
"v_accvgpr_read_b32 v248, a94\n"
"v_accvgpr_read_b32 v252, a95\n"
"v_mov_b32 v48, 0\n"
"v_mov_b32 v49, 0\n"
"v_mov_b32 v50, 0\n"
"v_mov_b32 v51, 0\n"
"ds_write_b128 v169, v[48:51] offset:0\n"
"ds_write_b128 v169, v[48:51] offset:7488\n"
"s_sub_u32 s72, 63, s65\n"
"v_cmp_eq_u32 vcc, s72, v1\n"
"s_lshl_b32 s19, s96, 12\n"
"s_add_u32 s19, s19, 4194304\n"
"s_lshl_b32 s9, s98, 12\n"
"s_add_u32 s9, s9, 4194304\n"
"s_waitcnt vmcnt(9)\n"
"v_add_u32 v52, 36992, v171\n"
"ds_write_b128 v168, a[80:83] offset:0\n"
"v_accvgpr_read_b32 v54, a64\n"
"v_accvgpr_read_b32 v55, a65\n"
"v_accvgpr_read_b32 v56, a66\n"
"v_accvgpr_read_b32 v57, a67\n"
"v_accvgpr_read_b32 v58, a68\n"
"v_accvgpr_read_b32 v59, a69\n"
"v_lshlrev_b32 v195, 16, v195\n"
"v_lshlrev_b32 v196, 16, v196\n"
"v_cndmask_b32 v196, 0, v196, vcc\n"
"ds_write_b128 v52, v[56:59] offset:12864\n"
"v_alignbit_b32 v60, v55, v54, 16\n"
"v_alignbit_b32 v61, v56, v55, 16\n"
"v_alignbit_b32 v62, v57, v56, 16\n"
"v_alignbit_b32 v63, v58, v57, 16\n"
"v_add_f32 v195, v195, v196\n"
"ds_write_b128 v52, v[60:63] offset:0\n"
"v_alignbit_b32 v64, v56, v55, 16\n"
"v_alignbit_b32 v65, v57, v56, 16\n"
"v_alignbit_b32 v66, v58, v57, 16\n"
"v_alignbit_b32 v67, v59, v58, 16\n"
"v_cvt_pk_bf16_f32 v195, v195, v195\n"
"ds_write_b128 v52, v[64:67] offset:8576\n"
"v_mov_b32 v68, v55\n"
"v_mov_b32 v69, v56\n"
"v_mov_b32 v70, v57\n"
"v_mov_b32 v71, v58\n"
"ds_write_b16 v170, v195 offset:0\n"
"ds_write_b128 v52, v[68:71] offset:4288\n"
"ds_write_b128 v168, a[84:87] offset:7488\n"
"v_accvgpr_read_b32 v54, a72\n"
"v_accvgpr_read_b32 v55, a73\n"
"v_accvgpr_read_b32 v56, a74\n"
"v_accvgpr_read_b32 v57, a75\n"
"v_accvgpr_read_b32 v58, a76\n"
"v_accvgpr_read_b32 v59, a77\n"
"v_lshlrev_b32 v197, 16, v197\n"
"v_lshlrev_b32 v198, 16, v198\n"
"v_cndmask_b32 v198, 0, v198, vcc\n"
"ds_write_b128 v52, v[56:59] offset:29952\n"
"v_alignbit_b32 v60, v55, v54, 16\n"
"v_alignbit_b32 v61, v56, v55, 16\n"
"v_alignbit_b32 v62, v57, v56, 16\n"
"v_alignbit_b32 v63, v58, v57, 16\n"
"v_add_f32 v197, v197, v198\n"
"ds_write_b128 v52, v[60:63] offset:17088\n"
"v_alignbit_b32 v64, v56, v55, 16\n"
"v_alignbit_b32 v65, v57, v56, 16\n"
"v_alignbit_b32 v66, v58, v57, 16\n"
"v_alignbit_b32 v67, v59, v58, 16\n"
"v_cvt_pk_bf16_f32 v197, v197, v197\n"
"ds_write_b128 v52, v[64:67] offset:25664\n"
"v_mov_b32 v68, v55\n"
"v_mov_b32 v69, v56\n"
"v_mov_b32 v70, v57\n"
"v_mov_b32 v71, v58\n"
"ds_write_b16 v170, v197 offset:17088\n"
"ds_write_b128 v52, v[68:71] offset:21376\n"
"s_waitcnt lgkmcnt(0)\n"
"s_barrier\n"
"ds_read_b128 a[80:83], v165 offset:4064\n"
"ds_read_b64 a[8:9], v164 offset:64\n"
"ds_read_b64 a[10:11], v164 offset:72\n"
"ds_read_b64 a[12:13], v164 offset:96\n"
"ds_read_b64 a[14:15], v164 offset:104\n"
"ds_read_b64 a[16:17], v164 offset:128\n"
"ds_read_b64 a[18:19], v164 offset:136\n"
"ds_read_b64 a[20:21], v164 offset:160\n"
"ds_read_b64 a[22:23], v164 offset:168\n"
"ds_read_b128 a[144:147], v165 offset:11552\n"
"ds_read_b64 a[116:117], v172 offset:64\n"
"ds_read_b64 a[118:119], v172 offset:72\n"
"ds_read_b128 a[84:87], v165 offset:4000\n"
"ds_read_b64 a[0:1], v164 offset:0\n"
"s_waitcnt lgkmcnt(7)\n"
"v_mfma_f32_16x16x32_bf16 v[8:11], a[8:11], a[80:83], 0\n"
"ds_read_b64 a[2:3], v164 offset:8\n"
"ds_read_b64 a[4:5], v164 offset:32\n"
"v_mfma_f32_16x16x32_bf16 v[12:15], a[12:15], a[80:83], 0\n"
"ds_read_b64 a[6:7], v164 offset:40\n"
"ds_read_b128 a[148:151], v165 offset:11488\n"
"v_mfma_f32_16x16x32_bf16 v[16:19], a[16:19], a[80:83], 0\n"
"ds_read_b64 a[112:113], v172 offset:0\n"
"ds_read_b64 a[114:115], v172 offset:8\n"
"s_waitcnt lgkmcnt(5)\n"
"v_mfma_f32_16x16x32_bf16 v[20:23], a[20:23], a[80:83], 0\n"
"ds_read_b128 a[88:91], v165 offset:6624\n"
"ds_read_b128 a[152:155], v165 offset:14112\n"
"v_mfma_f32_16x16x32_bf16 v[40:43], a[116:119], a[144:147], 0\n"
"ds_read_b128 a[92:95], v165 offset:6560\n"
"ds_read_b128 a[156:159], v165 offset:14048\n"
"v_mfma_f32_16x16x32_bf16 v[8:11], a[0:3], a[84:87], v[8:11]\n"
"ds_read_b128 a[96:99], v165 offset:3904\n"
"ds_read_b64 a[24:25], v164 offset:192\n"
"s_waitcnt lgkmcnt(9)\n"
"v_mfma_f32_16x16x32_bf16 v[12:15], a[4:7], a[84:87], v[12:15]\n"
"ds_read_b64 a[26:27], v164 offset:200\n"
"ds_read_b64 a[28:29], v164 offset:224\n"
"v_mfma_f32_16x16x32_bf16 v[16:19], a[8:11], a[84:87], v[16:19]\n"
"ds_read_b64 a[30:31], v164 offset:232\n"
"ds_read_b64 a[32:33], v164 offset:256\n"
"v_mfma_f32_16x16x32_bf16 v[20:23], a[12:15], a[84:87], v[20:23]\n"
"ds_read_b64 a[34:35], v164 offset:264\n"
"s_lshl_b32 s71, s67, 2\n"
"s_waitcnt lgkmcnt(10)\n"
"v_mfma_f32_16x16x32_bf16 v[40:43], a[112:115], a[148:151], v[40:43]\n"
"ds_read_b64 a[36:37], v164 offset:288\n"
"ds_read_b64 a[38:39], v164 offset:296\n"
"v_mfma_f32_16x16x32_bf16 v[24:27], a[8:11], a[88:91], 0\n"
"ds_read_b128 a[160:163], v165 offset:11392\n"
"ds_read_b64 a[124:125], v172 offset:192\n"
"v_mfma_f32_16x16x32_bf16 v[28:31], a[12:15], a[88:91], 0\n"
"s_add_u32 s71, s71, s65\n"
"s_lshl_b32 s71, s71, 11\n"
"v_mfma_f32_16x16x32_bf16 v[32:35], a[16:19], a[88:91], 0\n"
"s_add_u32 s12, s71, 786432\n"
"s_lshl_b32 s71, s66, 7\n"
"v_mfma_f32_16x16x32_bf16 v[36:39], a[20:23], a[88:91], 0\n"
"s_add_u32 s27, s71, s12\n"
"v_and_b32 v76, 15, v1\n"
"s_waitcnt lgkmcnt(12)\n"
"v_mfma_f32_16x16x32_bf16 v[44:47], a[116:119], a[152:155], 0\n"
"ds_read_b64 a[126:127], v172 offset:200\n"
"ds_read_b128 a[100:103], v165 offset:3840\n"
"v_mfma_f32_16x16x32_bf16 v[24:27], a[0:3], a[92:95], v[24:27]\n"
"v_lshrrev_b32 v77, 4, v1\n"
"v_lshrrev_b32 v78, 1, v76\n"
"v_mfma_f32_16x16x32_bf16 v[28:31], a[4:7], a[92:95], v[28:31]\n"
"v_xor_b32 v77, v77, v78\n"
"v_lshlrev_b32 v77, 4, v77\n"
"v_mfma_f32_16x16x32_bf16 v[32:35], a[8:11], a[92:95], v[32:35]\n"
"v_lshl_add_u32 v77, v76, 7, v77\n"
"s_lshl_b32 s71, s44, 12\n"
"v_mfma_f32_16x16x32_bf16 v[36:39], a[12:15], a[92:95], v[36:39]\n"
"v_add_u32 v184, s71, v77\n"
"s_lshl_b32 s71, s45, 12\n"
"s_waitcnt lgkmcnt(8)\n"
"v_mfma_f32_16x16x32_bf16 v[44:47], a[112:115], a[156:159], v[44:47]\n"
"ds_read_b128 a[164:167], v165 offset:11328\n"
"ds_read_b64 a[120:121], v172 offset:128\n"
"v_mfma_f32_16x16x32_bf16 v[8:11], a[24:27], a[96:99], v[8:11]\n"
"ds_read_b64 a[122:123], v172 offset:136\n"
"ds_read_b128 a[104:107], v165 offset:6464\n"
"v_mfma_f32_16x16x32_bf16 v[12:15], a[28:31], a[96:99], v[12:15]\n"
"ds_read_b128 a[168:171], v165 offset:13952\n"
"ds_read_b128 a[108:111], v165 offset:6400\n"
"s_waitcnt lgkmcnt(7)\n"
"v_mfma_f32_16x16x32_bf16 v[16:19], a[32:35], a[96:99], v[16:19]\n"
"ds_read_b128 a[172:175], v165 offset:13888\n"
"ds_read_b128 a[88:91], v165 offset:4064\n"
"v_mfma_f32_16x16x32_bf16 v[20:23], a[36:39], a[96:99], v[20:23]\n"
"ds_read_b64 a[48:49], v164 offset:2112\n"
"ds_read_b64 a[50:51], v164 offset:2120\n"
"v_mfma_f32_16x16x32_bf16 v[40:43], a[124:127], a[160:163], v[40:43]\n"
"ds_read_b64 a[52:53], v164 offset:2144\n"
"ds_read_b64 a[54:55], v164 offset:2152\n"
"s_waitcnt lgkmcnt(12)\n"
"v_mfma_f32_16x16x32_bf16 v[8:11], a[16:19], a[100:103], v[8:11]\n"
"ds_read_b64 a[56:57], v164 offset:2176\n"
"ds_read_b64 a[58:59], v164 offset:2184\n"
"v_mfma_f32_16x16x32_bf16 v[12:15], a[20:23], a[100:103], v[12:15]\n"
"v_add_u32 v185, s71, v77\n"
"s_lshl_b32 s71, s46, 12\n"
"v_mfma_f32_16x16x32_bf16 v[16:19], a[24:27], a[100:103], v[16:19]\n"
"v_add_u32 v186, s71, v77\n"
"s_lshl_b32 s71, s47, 12\n"
"v_mfma_f32_16x16x32_bf16 v[20:23], a[28:31], a[100:103], v[20:23]\n"
"v_add_u32 v187, s71, v77\n"
"v_xor_b32 v188, 64, v184\n"
"s_waitcnt lgkmcnt(10)\n"
"v_mfma_f32_16x16x32_bf16 v[40:43], a[120:123], a[164:167], v[40:43]\n"
"ds_read_b64 a[60:61], v164 offset:2208\n"
"ds_read_b64 a[62:63], v164 offset:2216\n"
"v_mfma_f32_16x16x32_bf16 v[24:27], a[24:27], a[104:107], v[24:27]\n"
"ds_read_b128 a[152:155], v165 offset:11552\n"
"ds_read_b64 a[132:133], v172 offset:2112\n"
"v_mfma_f32_16x16x32_bf16 v[28:31], a[28:31], a[104:107], v[28:31]\n"
"v_xor_b32 v189, 64, v185\n"
"v_xor_b32 v190, 64, v186\n"
"v_mfma_f32_16x16x32_bf16 v[32:35], a[32:35], a[104:107], v[32:35]\n"
"v_xor_b32 v191, 64, v187\n"
"v_add_u32 v176, 86016, v184\n"
"v_mfma_f32_16x16x32_bf16 v[36:39], a[36:39], a[104:107], v[36:39]\n"
"v_add_u32 v177, 86016, v185\n"
"v_add_u32 v178, 86016, v186\n"
"s_waitcnt lgkmcnt(12)\n"
"v_mfma_f32_16x16x32_bf16 v[44:47], a[124:127], a[168:171], v[44:47]\n"
"ds_read_b64 a[134:135], v172 offset:2120\n"
"ds_read_b128 a[92:95], v165 offset:4000\n"
"v_mfma_f32_16x16x32_bf16 v[24:27], a[16:19], a[108:111], v[24:27]\n"
"v_add_u32 v179, 86016, v187\n"
"v_add_u32 v180, 86016, v188\n"
"v_mfma_f32_16x16x32_bf16 v[28:31], a[20:23], a[108:111], v[28:31]\n"
"v_add_u32 v181, 86016, v189\n"
"v_add_u32 v182, 86016, v190\n"
"v_mfma_f32_16x16x32_bf16 v[32:35], a[24:27], a[108:111], v[32:35]\n"
"v_add_u32 v183, 86016, v191\n"
"v_lshlrev_b32 v76, 4, v1\n"
"v_mfma_f32_16x16x32_bf16 v[36:39], a[28:31], a[108:111], v[36:39]\n"
"v_add_u32 v192, 118784, v76\n"
"v_add_u32 v193, 32768, v76\n"
"s_waitcnt lgkmcnt(8)\n"
"v_mfma_f32_16x16x32_bf16 v[44:47], a[120:123], a[172:175], v[44:47]\n"
"ds_read_b64 a[40:41], v164 offset:2048\n"
"ds_read_b64 a[42:43], v164 offset:2056\n"
"v_mfma_f32_16x16x32_bf16 v[24:27], a[48:51], a[88:91], v[24:27]\n"
"ds_read_b64 a[44:45], v164 offset:2080\n"
"ds_read_b64 a[46:47], v164 offset:2088\n"
"v_mfma_f32_16x16x32_bf16 v[28:31], a[52:55], a[88:91], v[28:31]\n"
"ds_read_b128 a[156:159], v165 offset:11488\n"
"ds_read_b64 a[128:129], v172 offset:2048\n"
"s_waitcnt lgkmcnt(7)\n"
"v_mfma_f32_16x16x32_bf16 v[32:35], a[56:59], a[88:91], v[32:35]\n"
"ds_read_b64 a[130:131], v172 offset:2056\n"
"ds_read_b128 a[104:107], v165 offset:3904\n"
"v_mfma_f32_16x16x32_bf16 v[36:39], a[60:63], a[88:91], v[36:39]\n"
"ds_read_b64 a[64:65], v164 offset:2240\n"
"ds_read_b64 a[66:67], v164 offset:2248\n"
"v_mfma_f32_16x16x32_bf16 v[44:47], a[132:135], a[152:155], v[44:47]\n"
"ds_read_b64 a[68:69], v164 offset:2272\n"
"ds_read_b64 a[70:71], v164 offset:2280\n"
"s_waitcnt lgkmcnt(8)\n"
"v_mfma_f32_16x16x32_bf16 v[24:27], a[40:43], a[92:95], v[24:27]\n"
"ds_read_b64 a[72:73], v164 offset:2304\n"
"ds_read_b64 a[74:75], v164 offset:2312\n"
"v_mfma_f32_16x16x32_bf16 v[28:31], a[44:47], a[92:95], v[28:31]\n"
"ds_read_b64 a[76:77], v164 offset:2336\n"
"ds_read_b64 a[78:79], v164 offset:2344\n"
"v_mfma_f32_16x16x32_bf16 v[32:35], a[48:51], a[92:95], v[32:35]\n"
"ds_read_b128 a[168:171], v165 offset:11392\n"
"ds_read_b64 a[140:141], v172 offset:2240\n"
"v_mfma_f32_16x16x32_bf16 v[36:39], a[52:55], a[92:95], v[36:39]\n"
"s_mul_i32 s75, s67, 9\n"
"s_add_u32 s76, s75, 8\n"
"s_waitcnt lgkmcnt(6)\n"
"v_mfma_f32_16x16x32_bf16 v[44:47], a[128:131], a[156:159], v[44:47]\n"
"ds_read_b64 a[142:143], v172 offset:2248\n"
"ds_read_b128 a[108:111], v165 offset:3840\n"
"v_mfma_f32_16x16x32_bf16 v[24:27], a[64:67], a[104:107], v[24:27]\n"
"ds_read_b128 a[172:175], v165 offset:11328\n"
"ds_read_b64 a[136:137], v172 offset:2176\n"
"v_mfma_f32_16x16x32_bf16 v[28:31], a[68:71], a[104:107], v[28:31]\n"
"ds_read_b64 a[138:139], v172 offset:2184\n"
"ds_read_b128 a[80:83], v165 offset:2784\n"
"s_waitcnt lgkmcnt(5)\n"
"v_mfma_f32_16x16x32_bf16 v[32:35], a[72:75], a[104:107], v[32:35]\n"
"ds_read_b64 a[8:9], v164 offset:1088\n"
"ds_read_b64 a[10:11], v164 offset:1096\n"
"v_mfma_f32_16x16x32_bf16 v[36:39], a[76:79], a[104:107], v[36:39]\n"
"ds_read_b64 a[12:13], v164 offset:1120\n"
"ds_read_b64 a[14:15], v164 offset:1128\n"
"v_mfma_f32_16x16x32_bf16 v[44:47], a[140:143], a[168:171], v[44:47]\n"
"ds_read_b64 a[16:17], v164 offset:1152\n"
"ds_read_b64 a[18:19], v164 offset:1160\n"
"s_waitcnt lgkmcnt(10)\n"
"v_mfma_f32_16x16x32_bf16 v[24:27], a[56:59], a[108:111], v[24:27]\n"
"ds_read_b64 a[20:21], v164 offset:1184\n"
"ds_read_b64 a[22:23], v164 offset:1192\n"
"v_mfma_f32_16x16x32_bf16 v[28:31], a[60:63], a[108:111], v[28:31]\n"
"ds_read_b128 a[144:147], v165 offset:10272\n"
"ds_read_b64 a[116:117], v172 offset:1088\n"
"v_mfma_f32_16x16x32_bf16 v[32:35], a[64:67], a[108:111], v[32:35]\n"
"s_lshl_b32 s76, s76, 16\n"
"s_lshl_b32 s77, s65, 10\n"
"v_mfma_f32_16x16x32_bf16 v[36:39], a[68:71], a[108:111], v[36:39]\n"
"s_add_u32 s76, s76, s77\n"
"v_add_u32 v194, s76, v76\n"
"s_waitcnt lgkmcnt(6)\n"
"v_mfma_f32_16x16x32_bf16 v[44:47], a[136:139], a[172:175], v[44:47]\n"
"ds_read_b64 a[118:119], v172 offset:1096\n"
"ds_read_b128 a[84:87], v165 offset:2720\n"
"v_mfma_f32_16x16x32_bf16 v[8:11], a[8:11], a[80:83], v[8:11]\n"
"ds_read_b64 a[0:1], v164 offset:1024\n"
"ds_read_b64 a[2:3], v164 offset:1032\n"
"v_mfma_f32_16x16x32_bf16 v[12:15], a[12:15], a[80:83], v[12:15]\n"
"ds_read_b64 a[4:5], v164 offset:1056\n"
"ds_read_b64 a[6:7], v164 offset:1064\n"
"s_waitcnt lgkmcnt(5)\n"
"v_mfma_f32_16x16x32_bf16 v[16:19], a[16:19], a[80:83], v[16:19]\n"
"ds_read_b128 a[148:151], v165 offset:10208\n"
"ds_read_b64 a[112:113], v172 offset:1024\n"
"v_mfma_f32_16x16x32_bf16 v[20:23], a[20:23], a[80:83], v[20:23]\n"
"ds_read_b64 a[114:115], v172 offset:1032\n"
"ds_read_b128 a[88:91], v165 offset:5344\n"
"v_mfma_f32_16x16x32_bf16 v[40:43], a[116:119], a[144:147], v[40:43]\n"
"ds_read_b128 a[152:155], v165 offset:12832\n"
"ds_read_b128 a[92:95], v165 offset:5280\n"
"s_waitcnt lgkmcnt(6)\n"
"v_mfma_f32_16x16x32_bf16 v[8:11], a[0:3], a[84:87], v[8:11]\n"
"ds_read_b128 a[156:159], v165 offset:12768\n"
"ds_read_b128 a[96:99], v165 offset:2624\n"
"v_mfma_f32_16x16x32_bf16 v[12:15], a[4:7], a[84:87], v[12:15]\n"
"ds_read_b64 a[24:25], v164 offset:1216\n"
"ds_read_b64 a[26:27], v164 offset:1224\n"
"v_mfma_f32_16x16x32_bf16 v[16:19], a[8:11], a[84:87], v[16:19]\n"
"ds_read_b64 a[28:29], v164 offset:1248\n"
"ds_read_b64 a[30:31], v164 offset:1256\n"
"v_mfma_f32_16x16x32_bf16 v[20:23], a[12:15], a[84:87], v[20:23]\n"
"ds_read_b64 a[32:33], v164 offset:1280\n"
"ds_read_b64 a[34:35], v164 offset:1288\n"
"s_waitcnt lgkmcnt(10)\n"
"v_mfma_f32_16x16x32_bf16 v[40:43], a[112:115], a[148:151], v[40:43]\n"
"ds_read_b64 a[36:37], v164 offset:1312\n"
"ds_read_b64 a[38:39], v164 offset:1320\n"
"v_mfma_f32_16x16x32_bf16 v[24:27], a[8:11], a[88:91], v[24:27]\n"
"ds_read_b128 a[160:163], v165 offset:10112\n"
"ds_read_b64 a[124:125], v172 offset:1216\n"
"v_mfma_f32_16x16x32_bf16 v[28:31], a[12:15], a[88:91], v[28:31]\n"
"s_lshl_b32 s76, s65, 1\n"
"s_add_u32 s76, s76, s75\n"
"v_mfma_f32_16x16x32_bf16 v[32:35], a[16:19], a[88:91], v[32:35]\n"
"s_lshl_b32 s76, s76, 16\n"
"v_add_u32 v199, s76, v76\n"
"v_mfma_f32_16x16x32_bf16 v[36:39], a[20:23], a[88:91], v[36:39]\n"
"v_add_u32 v200, 0x10000, v199\n"
"s_mov_b32 s25, 10485760\n"
"s_waitcnt lgkmcnt(12)\n"
"v_mfma_f32_16x16x32_bf16 v[44:47], a[116:119], a[152:155], v[44:47]\n"
"ds_read_b64 a[126:127], v172 offset:1224\n"
"ds_read_b128 a[100:103], v165 offset:2560\n"
"v_mfma_f32_16x16x32_bf16 v[24:27], a[0:3], a[92:95], v[24:27]\n"
"s_mov_b32 s26, 10485760\n"
"s_waitcnt vmcnt(0)\n"
"v_mfma_f32_16x16x32_bf16 v[28:31], a[4:7], a[92:95], v[28:31]\n"
"v_mov_b32 v4, s68\n"
"v_mov_b32 v5, s69\n"
"v_min_u32 v6, 1, v1\n"
"v_lshlrev_b32 v6, 30, v6\n"
"buffer_store_dwordx2 v[4:5], v6, s[36:39], s27 offen sc1\n"
"v_mfma_f32_16x16x32_bf16 v[32:35], a[8:11], a[92:95], v[32:35]\n"
"v_mfma_f32_16x16x32_bf16 v[36:39], a[12:15], a[92:95], v[36:39]\n"
"s_add_u32 m0, s6, 0\n"
"s_nop 0\n"
"buffer_load_dwordx4 v208, s[48:51], s24 offen offset:0 lds\n"
"s_waitcnt lgkmcnt(8)\n"
"v_mfma_f32_16x16x32_bf16 v[44:47], a[112:115], a[156:159], v[44:47]\n"
"ds_read_b128 a[164:167], v165 offset:10048\n"
"ds_read_b64 a[120:121], v172 offset:1152\n"
"v_mfma_f32_16x16x32_bf16 v[8:11], a[24:27], a[96:99], v[8:11]\n"
"ds_read_b64 a[122:123], v172 offset:1160\n"
"ds_read_b128 a[104:107], v165 offset:5184\n"
"v_mfma_f32_16x16x32_bf16 v[12:15], a[28:31], a[96:99], v[12:15]\n"
"ds_read_b128 a[168:171], v165 offset:12672\n"
"ds_read_b128 a[108:111], v165 offset:5120\n"
"s_waitcnt lgkmcnt(7)\n"
"v_mfma_f32_16x16x32_bf16 v[16:19], a[32:35], a[96:99], v[16:19]\n"
"ds_read_b128 a[172:175], v165 offset:12608\n"
"ds_read_b128 a[88:91], v165 offset:2784\n"
"v_mfma_f32_16x16x32_bf16 v[20:23], a[36:39], a[96:99], v[20:23]\n"
"ds_read_b64 a[48:49], v164 offset:3136\n"
"ds_read_b64 a[50:51], v164 offset:3144\n"
"v_mfma_f32_16x16x32_bf16 v[40:43], a[124:127], a[160:163], v[40:43]\n"
"ds_read_b64 a[52:53], v164 offset:3168\n"
"ds_read_b64 a[54:55], v164 offset:3176\n"
"s_waitcnt lgkmcnt(12)\n"
"v_mfma_f32_16x16x32_bf16 v[8:11], a[16:19], a[100:103], v[8:11]\n"
"ds_read_b64 a[56:57], v164 offset:3200\n"
"ds_read_b64 a[58:59], v164 offset:3208\n"
"v_mfma_f32_16x16x32_bf16 v[12:15], a[20:23], a[100:103], v[12:15]\n"
"buffer_load_dwordx4 v212, s[48:51], s24 offen offset:1024 lds\n"
"v_mfma_f32_16x16x32_bf16 v[16:19], a[24:27], a[100:103], v[16:19]\n"
"v_mfma_f32_16x16x32_bf16 v[20:23], a[28:31], a[100:103], v[20:23]\n"
"buffer_load_dwordx4 v216, s[48:51], s24 offen offset:2048 lds\n"
"s_waitcnt lgkmcnt(10)\n"
"v_mfma_f32_16x16x32_bf16 v[40:43], a[120:123], a[164:167], v[40:43]\n"
"ds_read_b64 a[60:61], v164 offset:3232\n"
"ds_read_b64 a[62:63], v164 offset:3240\n"
"v_mfma_f32_16x16x32_bf16 v[24:27], a[24:27], a[104:107], v[24:27]\n"
"ds_read_b128 a[152:155], v165 offset:10272\n"
"ds_read_b64 a[132:133], v172 offset:3136\n"
"v_mfma_f32_16x16x32_bf16 v[28:31], a[28:31], a[104:107], v[28:31]\n"
"buffer_load_dwordx4 v220, s[48:51], s24 offen offset:3072 lds\n"
"s_add_u32 s24, s24, 128\n"
"v_mfma_f32_16x16x32_bf16 v[32:35], a[32:35], a[104:107], v[32:35]\n"
"v_mfma_f32_16x16x32_bf16 v[36:39], a[36:39], a[104:107], v[36:39]\n"
"s_add_u32 m0, s6, 16384\n"
"s_nop 0\n"
"buffer_load_dwordx4 v208, s[48:51], s24 offen offset:0 lds\n"
"s_waitcnt lgkmcnt(12)\n"
"v_mfma_f32_16x16x32_bf16 v[44:47], a[124:127], a[168:171], v[44:47]\n"
"ds_read_b64 a[134:135], v172 offset:3144\n"
"ds_read_b128 a[92:95], v165 offset:2720\n"
"v_mfma_f32_16x16x32_bf16 v[24:27], a[16:19], a[108:111], v[24:27]\n"
"buffer_load_dwordx4 v212, s[48:51], s24 offen offset:1024 lds\n"
"v_mfma_f32_16x16x32_bf16 v[28:31], a[20:23], a[108:111], v[28:31]\n"
"v_mfma_f32_16x16x32_bf16 v[32:35], a[24:27], a[108:111], v[32:35]\n"
"buffer_load_dwordx4 v216, s[48:51], s24 offen offset:2048 lds\n"
"v_mfma_f32_16x16x32_bf16 v[36:39], a[28:31], a[108:111], v[36:39]\n"
"s_waitcnt lgkmcnt(8)\n"
"v_mfma_f32_16x16x32_bf16 v[44:47], a[120:123], a[172:175], v[44:47]\n"
"ds_read_b64 a[40:41], v164 offset:3072\n"
"ds_read_b64 a[42:43], v164 offset:3080\n"
"v_mfma_f32_16x16x32_bf16 v[24:27], a[48:51], a[88:91], v[24:27]\n"
"ds_read_b64 a[44:45], v164 offset:3104\n"
"ds_read_b64 a[46:47], v164 offset:3112\n"
"v_mfma_f32_16x16x32_bf16 v[28:31], a[52:55], a[88:91], v[28:31]\n"
"ds_read_b128 a[156:159], v165 offset:10208\n"
"ds_read_b64 a[128:129], v172 offset:3072\n"
"s_waitcnt lgkmcnt(7)\n"
"v_mfma_f32_16x16x32_bf16 v[32:35], a[56:59], a[88:91], v[32:35]\n"
"ds_read_b64 a[130:131], v172 offset:3080\n"
"ds_read_b128 a[104:107], v165 offset:2624\n"
"v_mfma_f32_16x16x32_bf16 v[36:39], a[60:63], a[88:91], v[36:39]\n"
"ds_read_b64 a[64:65], v164 offset:3264\n"
"ds_read_b64 a[66:67], v164 offset:3272\n"
"v_mfma_f32_16x16x32_bf16 v[44:47], a[132:135], a[152:155], v[44:47]\n"
"ds_read_b64 a[68:69], v164 offset:3296\n"
"ds_read_b64 a[70:71], v164 offset:3304\n"
"s_waitcnt lgkmcnt(8)\n"
"v_mfma_f32_16x16x32_bf16 v[24:27], a[40:43], a[92:95], v[24:27]\n"
"ds_read_b64 a[72:73], v164 offset:3328\n"
"ds_read_b64 a[74:75], v164 offset:3336\n"
"v_mfma_f32_16x16x32_bf16 v[28:31], a[44:47], a[92:95], v[28:31]\n"
"ds_read_b64 a[76:77], v164 offset:3360\n"
"ds_read_b64 a[78:79], v164 offset:3368\n"
"v_mfma_f32_16x16x32_bf16 v[32:35], a[48:51], a[92:95], v[32:35]\n"
"ds_read_b128 a[168:171], v165 offset:10112\n"
"ds_read_b64 a[140:141], v172 offset:3264\n"
"v_mfma_f32_16x16x32_bf16 v[36:39], a[52:55], a[92:95], v[36:39]\n"
"buffer_load_dwordx4 v220, s[48:51], s24 offen offset:3072 lds\n"
"s_add_u32 s24, s24, 128\n"
"s_waitcnt lgkmcnt(6)\n"
"v_mfma_f32_16x16x32_bf16 v[44:47], a[128:131], a[156:159], v[44:47]\n"
"ds_read_b64 a[142:143], v172 offset:3272\n"
"ds_read_b128 a[108:111], v165 offset:2560\n"
"v_mfma_f32_16x16x32_bf16 v[24:27], a[64:67], a[104:107], v[24:27]\n"
"ds_read_b128 a[172:175], v165 offset:10048\n"
"ds_read_b64 a[136:137], v172 offset:3200\n"
"v_mfma_f32_16x16x32_bf16 v[28:31], a[68:71], a[104:107], v[28:31]\n"
"ds_read_b64 a[138:139], v172 offset:3208\n"
"v_lshrrev_b32 v3, 2, v1\n"
"s_waitcnt lgkmcnt(4)\n"
"v_mfma_f32_16x16x32_bf16 v[32:35], a[72:75], a[104:107], v[32:35]\n"
"v_lshlrev_b32 v3, 7, v3\n"
"v_lshlrev_b32 v152, 16, v152\n"
"v_mfma_f32_16x16x32_bf16 v[36:39], a[76:79], a[104:107], v[36:39]\n"
"v_lshlrev_b32 v156, 16, v156\n"
"v_lshlrev_b32 v160, 16, v160\n"
"v_mfma_f32_16x16x32_bf16 v[44:47], a[140:143], a[168:171], v[44:47]\n"
"v_lshlrev_b32 v204, 16, v204\n"
"v_lshlrev_b32 v240, 16, v240\n"
"s_waitcnt lgkmcnt(3)\n"
"v_mfma_f32_16x16x32_bf16 v[24:27], a[56:59], a[108:111], v[24:27]\n"
"v_lshlrev_b32 v244, 16, v244\n"
"v_lshlrev_b32 v248, 16, v248\n"
"v_mfma_f32_16x16x32_bf16 v[28:31], a[60:63], a[108:111], v[28:31]\n"
"v_lshlrev_b32 v252, 16, v252\n"
"v_mov_b32 v153, v152\n"
"v_mfma_f32_16x16x32_bf16 v[32:35], a[64:67], a[108:111], v[32:35]\n"
"v_mov_b32 v157, v156\n"
"v_mov_b32 v161, v160\n"
"v_mfma_f32_16x16x32_bf16 v[36:39], a[68:71], a[108:111], v[36:39]\n"
"v_mov_b32 v205, v204\n"
"v_mov_b32 v241, v240\n"
"s_waitcnt lgkmcnt(0)\n"
"v_mfma_f32_16x16x32_bf16 v[44:47], a[136:139], a[172:175], v[44:47]\n"
"v_mov_b32 v245, v244\n"
"v_mov_b32 v249, v248\n"
"v_mov_b32 v253, v252\n"
"v_mov_b32 v154, v152\n"
"v_mov_b32 v158, v156\n"
"v_mov_b32 v162, v160\n"
"v_mov_b32 v206, v204\n"
"v_mov_b32 v242, v240\n"
"v_mov_b32 v246, v244\n"
"v_mov_b32 v250, v248\n"
"v_mov_b32 v254, v252\n"
"v_mov_b32 v155, v152\n"
"v_mov_b32 v159, v156\n"
"v_mov_b32 v163, v160\n"
"v_mov_b32 v207, v204\n"
"v_mov_b32 v243, v240\n"
"v_mov_b32 v247, v244\n"
"v_mov_b32 v251, v248\n"
"v_mov_b32 v255, v252\n"
"buffer_load_dwordx2 v[202:203], v3, s[36:39], s12 offen sc1\n"
"s_barrier\n"
"ds_write_b128 v166, v[8:11] offset:0\n"
"ds_write_b128 v166, v[12:15] offset:64\n"
"ds_write_b128 v166, v[16:19] offset:128\n"
"ds_write_b128 v166, v[20:23] offset:192\n"
"ds_write_b128 v173, v[40:43] offset:0\n"
"ds_write_b128 v166, v[24:27] offset:4352\n"
"ds_write_b128 v166, v[28:31] offset:4416\n"
"ds_write_b128 v166, v[32:35] offset:4480\n"
"ds_write_b128 v166, v[36:39] offset:4544\n"
"ds_write_b128 v173, v[44:47] offset:1280\n"
"s_waitcnt vmcnt(0)\n"
"poll4c_%=:\n"
"v_xor_b32 v5, s68, v202\n"
"v_xor_b32 v6, s69, v203\n"
"v_or_b32 v4, v5, v6\n"
"v_cmp_ne_u32 vcc, 0, v4\n"
"s_cbranch_vccz poll4d_%=\n"
"s_sub_u32 s70, s70, 1\n"
"s_cbranch_scc1 poll4t_%=\n"
"s_sleep 2\n"
"buffer_load_dwordx2 v[202:203], v3, s[36:39], s12 offen sc1\n"
"s_waitcnt vmcnt(0)\n"
"s_branch poll4c_%=\n"
"poll4t_%=:\n"
"s_mov_b32 s70, 0\n"
"poll4d_%=:\n"
"s_add_u32 m0, s7, 118784\n"
"s_nop 0\n"
"buffer_load_dwordx4 v194, s[36:39], s26 offen sc1 lds\n"
"s_add_u32 s26, s26, 4096\n"
"buffer_load_dwordx4 a[72:75], v199, s[36:39], s25 offen offset:0 sc1\n"
"buffer_load_dwordx4 a[76:79], v200, s[36:39], s25 offen offset:0 sc1\n"
"buffer_load_dwordx4 a[80:83], v199, s[36:39], s25 offen offset:1024 sc1\n"
"buffer_load_dwordx4 a[84:87], v200, s[36:39], s25 offen offset:1024 sc1\n"
"buffer_load_dwordx4 a[88:91], v199, s[36:39], s25 offen offset:2048 sc1\n"
"buffer_load_dwordx4 a[92:95], v200, s[36:39], s25 offen offset:2048 sc1\n"
"buffer_load_dwordx4 a[96:99], v199, s[36:39], s25 offen offset:3072 sc1\n"
"buffer_load_dwordx4 a[100:103], v200, s[36:39], s25 offen offset:3072 sc1\n"
"s_add_u32 s25, s25, 4096\n"
"s_add_u32 m0, s7, 155648\n"
"s_nop 0\n"
"buffer_load_dwordx4 v194, s[36:39], s26 offen sc1 lds\n"
"s_add_u32 s26, s26, 4096\n"
"s_waitcnt lgkmcnt(0)\n"
"s_barrier\n"
"ds_read_b128 v[8:11], v167 offset:0\n"
"ds_read_b128 v[12:15], v167 offset:16\n"
"ds_read_b128 v[16:19], v167 offset:8704\n"
"ds_read_b128 v[20:23], v167 offset:8720\n"
"ds_read_b128 v[24:27], v167 offset:17408\n"
"ds_read_b128 v[28:31], v167 offset:17424\n"
"ds_read_b128 v[32:35], v167 offset:26112\n"
"ds_read_b128 v[36:39], v167 offset:26128\n"
"ds_read_b128 v[40:43], v174 offset:0\n"
"ds_read_b128 v[44:47], v174 offset:16\n"
"ds_read_b128 v[48:51], v174 offset:2560\n"
"ds_read_b128 v[52:55], v174 offset:2576\n"
"ds_read_b128 v[56:59], v174 offset:5120\n"
"ds_read_b128 v[60:63], v174 offset:5136\n"
"ds_read_b128 v[64:67], v174 offset:7680\n"
"ds_read_b128 v[68:71], v174 offset:7696\n"
"s_waitcnt lgkmcnt(8)\n"
"v_pk_add_f32 v[8:9], v[8:9], v[16:17]\n"
"v_pk_add_f32 v[24:25], v[24:25], v[32:33]\n"
"buffer_load_dwordx4 a[104:107], v199, s[36:39], s25 offen offset:0 sc1\n"
"v_pk_add_f32 v[10:11], v[10:11], v[18:19]\n"
"v_pk_add_f32 v[26:27], v[26:27], v[34:35]\n"
"buffer_load_dwordx4 a[108:111], v200, s[36:39], s25 offen offset:0 sc1\n"
"v_pk_add_f32 v[12:13], v[12:13], v[20:21]\n"
"v_pk_add_f32 v[28:29], v[28:29], v[36:37]\n"
"buffer_load_dwordx4 a[112:115], v199, s[36:39], s25 offen offset:1024 sc1\n"
"v_pk_add_f32 v[14:15], v[14:15], v[22:23]\n"
"v_pk_add_f32 v[30:31], v[30:31], v[38:39]\n"
"buffer_load_dwordx4 a[116:119], v200, s[36:39], s25 offen offset:1024 sc1\n"
"v_pk_add_f32 v[8:9], v[8:9], v[24:25]\n"
"v_pk_add_f32 v[10:11], v[10:11], v[26:27]\n"
"v_pk_add_f32 v[12:13], v[12:13], v[28:29]\n"
"v_pk_add_f32 v[14:15], v[14:15], v[30:31]\n"
"v_cvt_pk_bf16_f32 v16, v8, v9\n"
"v_cvt_pk_bf16_f32 v17, v10, v11\n"
"v_cvt_pk_bf16_f32 v18, v12, v13\n"
"v_cvt_pk_bf16_f32 v19, v14, v15\n"
"buffer_store_dwordx4 v[16:19], v171, s[36:39], s19 offen sc1\n"
"s_waitcnt lgkmcnt(0)\n"
"v_pk_add_f32 v[40:41], v[40:41], v[48:49]\n"
"v_pk_add_f32 v[56:57], v[56:57], v[64:65]\n"
"buffer_load_dwordx4 a[120:123], v199, s[36:39], s25 offen offset:2048 sc1\n"
"v_pk_add_f32 v[42:43], v[42:43], v[50:51]\n"
"v_pk_add_f32 v[58:59], v[58:59], v[66:67]\n"
"buffer_load_dwordx4 a[124:127], v200, s[36:39], s25 offen offset:2048 sc1\n"
"v_pk_add_f32 v[44:45], v[44:45], v[52:53]\n"
"v_pk_add_f32 v[60:61], v[60:61], v[68:69]\n"
"buffer_load_dwordx4 a[128:131], v199, s[36:39], s25 offen offset:3072 sc1\n"
"v_pk_add_f32 v[46:47], v[46:47], v[54:55]\n"
"v_pk_add_f32 v[62:63], v[62:63], v[70:71]\n"
"buffer_load_dwordx4 a[132:135], v200, s[36:39], s25 offen offset:3072 sc1\n"
"v_pk_add_f32 v[40:41], v[40:41], v[56:57]\n"
"v_pk_add_f32 v[42:43], v[42:43], v[58:59]\n"
"v_pk_add_f32 v[44:45], v[44:45], v[60:61]\n"
"v_pk_add_f32 v[46:47], v[46:47], v[62:63]\n"
"v_cvt_pk_bf16_f32 v48, v40, v41\n"
"v_cvt_pk_bf16_f32 v49, v42, v43\n"
"v_cvt_pk_bf16_f32 v50, v44, v45\n"
"v_cvt_pk_bf16_f32 v51, v46, v47\n"
"buffer_store_dwordx4 v[48:51], v175, s[36:39], s9 offen sc1\n"
"s_add_u32 s25, s25, 4096\n"
"s_add_u32 m0, s7, 32768\n"
"s_nop 0\n"
"buffer_load_dwordx4 v194, s[36:39], s26 offen sc1 lds\n"
"s_add_u32 s26, s26, 4096\n"
"s_waitcnt vmcnt(20)\n"
"s_barrier\n"
"ds_read_b128 a[0:3], v176 offset:0\n"
"ds_read_b128 a[4:7], v176 offset:2048\n"
"ds_read_b128 a[8:11], v177 offset:0\n"
"ds_read_b128 a[12:15], v177 offset:2048\n"
"ds_read_b128 a[16:19], v178 offset:0\n"
"ds_read_b128 a[20:23], v178 offset:2048\n"
"ds_read_b128 a[24:27], v179 offset:0\n"
"ds_read_b128 a[28:31], v179 offset:2048\n"
"ds_read_b128 a[64:67], v192 offset:0\n"
"s_waitcnt vmcnt(18)\n"
"s_waitcnt lgkmcnt(0)\n"
"v_mfma_f32_16x16x32_bf16 v[8:11], a[72:75], a[0:3], v[152:155]\n"
"ds_read_b128 a[32:35], v180 offset:0\n"
"v_mfma_f32_16x16x32_bf16 v[40:43], a[76:79], a[0:3], v[152:155]\n"
"ds_read_b128 a[36:39], v180 offset:2048\n"
"v_mfma_f32_16x16x32_bf16 v[44:47], a[76:79], a[4:7], v[156:159]\n"
"s_add_u32 m0, s6, 36864\n"
"s_nop 0\n"
"buffer_load_dwordx4 v208, s[48:51], s24 offen offset:0 lds\n"
"v_mfma_f32_16x16x32_bf16 v[12:15], a[72:75], a[4:7], v[156:159]\n"
"ds_read_b128 a[40:43], v181 offset:0\n"
"v_mfma_f32_16x16x32_bf16 v[16:19], a[72:75], a[8:11], v[160:163]\n"
"ds_read_b128 a[44:47], v181 offset:2048\n"
"v_mfma_f32_16x16x32_bf16 v[48:51], a[76:79], a[8:11], v[160:163]\n"
"v_mfma_f32_16x16x32_bf16 v[52:55], a[76:79], a[12:15], v[204:207]\n"
"ds_read_b128 a[48:51], v182 offset:0\n"
"v_mfma_f32_16x16x32_bf16 v[20:23], a[72:75], a[12:15], v[204:207]\n"
"ds_read_b128 a[52:55], v182 offset:2048\n"
"v_mfma_f32_16x16x32_bf16 v[24:27], a[72:75], a[16:19], v[240:243]\n"
"v_mfma_f32_16x16x32_bf16 v[56:59], a[76:79], a[16:19], v[240:243]\n"
"ds_read_b128 a[56:59], v183 offset:0\n"
"v_mfma_f32_16x16x32_bf16 v[60:63], a[76:79], a[20:23], v[244:247]\n"
"ds_read_b128 a[60:63], v183 offset:2048\n"
"v_mfma_f32_16x16x32_bf16 v[28:31], a[72:75], a[20:23], v[244:247]\n"
"buffer_load_dwordx4 v212, s[48:51], s24 offen offset:1024 lds\n"
"v_mfma_f32_16x16x32_bf16 v[32:35], a[72:75], a[24:27], v[248:251]\n"
"ds_read_b128 a[68:71], v192 offset:1024\n"
"v_mfma_f32_16x16x32_bf16 v[64:67], a[76:79], a[24:27], v[248:251]\n"
"v_mfma_f32_16x16x32_bf16 v[68:71], a[76:79], a[28:31], v[252:255]\n"
"buffer_load_dwordx4 v216, s[48:51], s24 offen offset:2048 lds\n"
"v_mfma_f32_16x16x32_bf16 v[36:39], a[72:75], a[28:31], v[252:255]\n"
"v_mfma_f32_16x16x32_bf16 v[72:75], a[64:67], a[0:3], v[152:155]\n"
"v_mfma_f32_16x16x32_bf16 v[76:79], a[64:67], a[4:7], v[156:159]\n"
"s_waitcnt vmcnt(19)\n"
"s_waitcnt lgkmcnt(0)\n"
"v_mfma_f32_16x16x32_bf16 v[8:11], a[80:83], a[32:35], v[8:11]\n"
"ds_read_b128 a[0:3], v176 offset:16384\n"
"v_mul_f32 v248, v80, v80\n"
"v_mfma_f32_16x16x32_bf16 v[40:43], a[84:87], a[32:35], v[40:43]\n"
"ds_read_b128 a[4:7], v176 offset:18432\n"
"v_mul_f32 v249, v81, v81\n"
"v_mfma_f32_16x16x32_bf16 v[44:47], a[84:87], a[36:39], v[44:47]\n"
"buffer_load_dwordx4 v220, s[48:51], s24 offen offset:3072 lds\n"
"s_add_u32 s24, s24, 128\n"
"v_mfma_f32_16x16x32_bf16 v[12:15], a[80:83], a[36:39], v[12:15]\n"
"ds_read_b128 a[8:11], v177 offset:16384\n"
"v_mul_f32 v250, v82, v82\n"
"v_mfma_f32_16x16x32_bf16 v[16:19], a[80:83], a[40:43], v[16:19]\n"
"ds_read_b128 a[12:15], v177 offset:18432\n"
"v_mul_f32 v251, v83, v83\n"
"v_mfma_f32_16x16x32_bf16 v[48:51], a[84:87], a[40:43], v[48:51]\n"
"v_fma_f32 v248, v248, v210, v209\n"
"v_fma_f32 v249, v249, v210, v209\n"
"v_mfma_f32_16x16x32_bf16 v[52:55], a[84:87], a[44:47], v[52:55]\n"
"ds_read_b128 a[16:19], v178 offset:16384\n"
"v_fma_f32 v250, v250, v210, v209\n"
"v_mfma_f32_16x16x32_bf16 v[20:23], a[80:83], a[44:47], v[20:23]\n"
"ds_read_b128 a[20:23], v178 offset:18432\n"
"v_fma_f32 v251, v251, v210, v209\n"
"v_mfma_f32_16x16x32_bf16 v[24:27], a[80:83], a[48:51], v[24:27]\n"
"buffer_load_dwordx4 a[72:75], v199, s[36:39], s25 offen offset:0 sc1\n"
"v_mul_f32 v248, v80, v248\n"
"v_mfma_f32_16x16x32_bf16 v[56:59], a[84:87], a[48:51], v[56:59]\n"
"ds_read_b128 a[24:27], v179 offset:16384\n"
"v_mul_f32 v249, v81, v249\n"
"v_mfma_f32_16x16x32_bf16 v[60:63], a[84:87], a[52:55], v[60:63]\n"
"ds_read_b128 a[28:31], v179 offset:18432\n"
"v_mul_f32 v250, v82, v250\n"
"v_mfma_f32_16x16x32_bf16 v[28:31], a[80:83], a[52:55], v[28:31]\n"
"s_add_u32 m0, s6, 53248\n"
"s_nop 0\n"
"buffer_load_dwordx4 v208, s[48:51], s24 offen offset:0 lds\n"
"v_mfma_f32_16x16x32_bf16 v[32:35], a[80:83], a[56:59], v[32:35]\n"
"ds_read_b128 a[64:67], v192 offset:2048\n"
"v_mul_f32 v251, v83, v251\n"
"v_mfma_f32_16x16x32_bf16 v[64:67], a[84:87], a[56:59], v[64:67]\n"
"v_exp_f32 v248, v248\n"
"v_mfma_f32_16x16x32_bf16 v[68:71], a[84:87], a[60:63], v[68:71]\n"
"buffer_load_dwordx4 v212, s[48:51], s24 offen offset:1024 lds\n"
"v_mul_f32 v252, v112, v112\n"
"v_mfma_f32_16x16x32_bf16 v[36:39], a[80:83], a[60:63], v[36:39]\n"
"v_mul_f32 v253, v113, v113\n"
"v_mfma_f32_16x16x32_bf16 v[72:75], a[68:71], a[32:35], v[72:75]\n"
"buffer_load_dwordx4 a[76:79], v200, s[36:39], s25 offen offset:0 sc1\n"
"v_mfma_f32_16x16x32_bf16 v[76:79], a[68:71], a[36:39], v[76:79]\n"
"v_exp_f32 v249, v249\n"
"s_waitcnt vmcnt(22)\n"
"s_waitcnt lgkmcnt(0)\n"
"v_mfma_f32_16x16x32_bf16 v[8:11], a[88:91], a[0:3], v[8:11]\n"
"ds_read_b128 a[32:35], v180 offset:16384\n"
"v_mul_f32 v254, v114, v114\n"
"v_mfma_f32_16x16x32_bf16 v[40:43], a[92:95], a[0:3], v[40:43]\n"
"ds_read_b128 a[36:39], v180 offset:18432\n"
"v_mul_f32 v255, v115, v115\n"
"v_mfma_f32_16x16x32_bf16 v[44:47], a[92:95], a[4:7], v[44:47]\n"
"buffer_load_dwordx4 v216, s[48:51], s24 offen offset:2048 lds\n"
"v_mfma_f32_16x16x32_bf16 v[12:15], a[88:91], a[4:7], v[12:15]\n"
"ds_read_b128 a[40:43], v181 offset:16384\n"
"v_mfma_f32_16x16x32_bf16 v[16:19], a[88:91], a[8:11], v[16:19]\n"
"ds_read_b128 a[44:47], v181 offset:18432\n"
"v_mfma_f32_16x16x32_bf16 v[48:51], a[92:95], a[8:11], v[48:51]\n"
"v_exp_f32 v250, v250\n"
"v_mfma_f32_16x16x32_bf16 v[52:55], a[92:95], a[12:15], v[52:55]\n"
"ds_read_b128 a[48:51], v182 offset:16384\n"
"v_fma_f32 v252, v252, v210, v209\n"
"v_mfma_f32_16x16x32_bf16 v[20:23], a[88:91], a[12:15], v[20:23]\n"
"ds_read_b128 a[52:55], v182 offset:18432\n"
"v_fma_f32 v253, v253, v210, v209\n"
"v_mfma_f32_16x16x32_bf16 v[24:27], a[88:91], a[16:19], v[24:27]\n"
"buffer_load_dwordx4 a[80:83], v199, s[36:39], s25 offen offset:1024 sc1\n"
"v_mfma_f32_16x16x32_bf16 v[56:59], a[92:95], a[16:19], v[56:59]\n"
"ds_read_b128 a[56:59], v183 offset:16384\n"
"v_mfma_f32_16x16x32_bf16 v[60:63], a[92:95], a[20:23], v[60:63]\n"
"ds_read_b128 a[60:63], v183 offset:18432\n"
"v_mfma_f32_16x16x32_bf16 v[28:31], a[88:91], a[20:23], v[28:31]\n"
"buffer_load_dwordx4 v220, s[48:51], s24 offen offset:3072 lds\n"
"s_add_u32 s24, s24, 128\n"
"v_mfma_f32_16x16x32_bf16 v[32:35], a[88:91], a[24:27], v[32:35]\n"
"ds_read_b128 a[68:71], v192 offset:3072\n"
"v_mfma_f32_16x16x32_bf16 v[64:67], a[92:95], a[24:27], v[64:67]\n"
"v_exp_f32 v251, v251\n"
"v_mfma_f32_16x16x32_bf16 v[68:71], a[92:95], a[28:31], v[68:71]\n"
"s_add_u32 m0, s7, 69632\n"
"s_nop 0\n"
"buffer_load_dwordx4 v194, s[36:39], s26 offen sc1 lds\n"
"s_add_u32 s26, s26, 4096\n"
"v_mfma_f32_16x16x32_bf16 v[36:39], a[88:91], a[28:31], v[36:39]\n"
"v_fma_f32 v254, v254, v210, v209\n"
"v_fma_f32 v255, v255, v210, v209\n"
"v_mfma_f32_16x16x32_bf16 v[72:75], a[64:67], a[0:3], v[72:75]\n"
"buffer_load_dwordx4 a[84:87], v200, s[36:39], s25 offen offset:1024 sc1\n"
"v_fma_f32 v248, v248, v211, v211\n"
"v_mfma_f32_16x16x32_bf16 v[76:79], a[64:67], a[4:7], v[76:79]\n"
"v_fma_f32 v249, v249, v211, v211\n"
"v_fma_f32 v250, v250, v211, v211\n"
"s_waitcnt vmcnt(25)\n"
"s_waitcnt vmcnt(24) lgkmcnt(0)\n"
"s_barrier\n"
"v_mfma_f32_16x16x32_bf16 v[8:11], a[96:99], a[32:35], v[8:11]\n"
"ds_read_b128 a[0:3], v176 offset:36864\n"
"v_fma_f32 v251, v251, v211, v211\n"
"v_mfma_f32_16x16x32_bf16 v[40:43], a[100:103], a[32:35], v[40:43]\n"
"ds_read_b128 a[4:7], v176 offset:38912\n"
"v_mul_f32 v252, v112, v252\n"
"v_mfma_f32_16x16x32_bf16 v[44:47], a[100:103], a[36:39], v[44:47]\n"
"s_add_u32 m0, s6, 86016\n"
"s_nop 0\n"
"buffer_load_dwordx4 v208, s[48:51], s24 offen offset:0 lds\n"
"v_mfma_f32_16x16x32_bf16 v[12:15], a[96:99], a[36:39], v[12:15]\n"
"ds_read_b128 a[8:11], v177 offset:36864\n"
"v_mul_f32 v253, v113, v253\n"
"v_mfma_f32_16x16x32_bf16 v[16:19], a[96:99], a[40:43], v[16:19]\n"
"ds_read_b128 a[12:15], v177 offset:38912\n"
"v_mul_f32 v254, v114, v254\n"
"v_mfma_f32_16x16x32_bf16 v[48:51], a[100:103], a[40:43], v[48:51]\n"
"v_mul_f32 v255, v115, v255\n"
"v_mfma_f32_16x16x32_bf16 v[52:55], a[100:103], a[44:47], v[52:55]\n"
"ds_read_b128 a[16:19], v178 offset:36864\n"
"v_mfma_f32_16x16x32_bf16 v[20:23], a[96:99], a[44:47], v[20:23]\n"
"ds_read_b128 a[20:23], v178 offset:38912\n"
"v_mfma_f32_16x16x32_bf16 v[24:27], a[96:99], a[48:51], v[24:27]\n"
"buffer_load_dwordx4 a[88:91], v199, s[36:39], s25 offen offset:2048 sc1\n"
"v_mfma_f32_16x16x32_bf16 v[56:59], a[100:103], a[48:51], v[56:59]\n"
"ds_read_b128 a[24:27], v179 offset:36864\n"
"v_mfma_f32_16x16x32_bf16 v[60:63], a[100:103], a[52:55], v[60:63]\n"
"ds_read_b128 a[28:31], v179 offset:38912\n"
"v_mfma_f32_16x16x32_bf16 v[28:31], a[96:99], a[52:55], v[28:31]\n"
"buffer_load_dwordx4 v212, s[48:51], s24 offen offset:1024 lds\n"
"v_mfma_f32_16x16x32_bf16 v[32:35], a[96:99], a[56:59], v[32:35]\n"
"ds_read_b128 a[64:67], v192 offset:36864\n"
"v_mfma_f32_16x16x32_bf16 v[64:67], a[100:103], a[56:59], v[64:67]\n"
"v_rcp_f32 v248, v248\n"
"v_mfma_f32_16x16x32_bf16 v[68:71], a[100:103], a[60:63], v[68:71]\n"
"buffer_load_dwordx4 v216, s[48:51], s24 offen offset:2048 lds\n"
"v_mfma_f32_16x16x32_bf16 v[36:39], a[96:99], a[60:63], v[36:39]\n"
"v_rcp_f32 v249, v249\n"
"v_mfma_f32_16x16x32_bf16 v[72:75], a[68:71], a[32:35], v[72:75]\n"
"buffer_load_dwordx4 a[92:95], v200, s[36:39], s25 offen offset:2048 sc1\n"
"v_mfma_f32_16x16x32_bf16 v[76:79], a[68:71], a[36:39], v[76:79]\n"
"v_rcp_f32 v250, v250\n"
"s_waitcnt vmcnt(27)\n"
"s_waitcnt lgkmcnt(0)\n"
"v_mfma_f32_16x16x32_bf16 v[8:11], a[104:107], a[0:3], v[8:11]\n"
"ds_read_b128 a[32:35], v180 offset:36864\n"
"v_mfma_f32_16x16x32_bf16 v[40:43], a[108:111], a[0:3], v[40:43]\n"
"ds_read_b128 a[36:39], v180 offset:38912\n"
"v_mfma_f32_16x16x32_bf16 v[44:47], a[108:111], a[4:7], v[44:47]\n"
"buffer_load_dwordx4 v220, s[48:51], s24 offen offset:3072 lds\n"
"s_add_u32 s24, s24, 128\n"
"v_mfma_f32_16x16x32_bf16 v[12:15], a[104:107], a[4:7], v[12:15]\n"
"ds_read_b128 a[40:43], v181 offset:36864\n"
"v_mfma_f32_16x16x32_bf16 v[16:19], a[104:107], a[8:11], v[16:19]\n"
"ds_read_b128 a[44:47], v181 offset:38912\n"
"v_mfma_f32_16x16x32_bf16 v[48:51], a[108:111], a[8:11], v[48:51]\n"
"v_rcp_f32 v251, v251\n"
"v_mfma_f32_16x16x32_bf16 v[52:55], a[108:111], a[12:15], v[52:55]\n"
"ds_read_b128 a[48:51], v182 offset:36864\n"
"v_mfma_f32_16x16x32_bf16 v[20:23], a[104:107], a[12:15], v[20:23]\n"
"ds_read_b128 a[52:55], v182 offset:38912\n"
"v_mfma_f32_16x16x32_bf16 v[24:27], a[104:107], a[16:19], v[24:27]\n"
"buffer_load_dwordx4 a[96:99], v199, s[36:39], s25 offen offset:3072 sc1\n"
"v_mfma_f32_16x16x32_bf16 v[56:59], a[108:111], a[16:19], v[56:59]\n"
"ds_read_b128 a[56:59], v183 offset:36864\n"
"v_mfma_f32_16x16x32_bf16 v[60:63], a[108:111], a[20:23], v[60:63]\n"
"ds_read_b128 a[60:63], v183 offset:38912\n"
"v_mfma_f32_16x16x32_bf16 v[28:31], a[104:107], a[20:23], v[28:31]\n"
"s_add_u32 m0, s6, 102400\n"
"s_nop 0\n"
"buffer_load_dwordx4 v208, s[48:51], s24 offen offset:0 lds\n"
"v_mfma_f32_16x16x32_bf16 v[32:35], a[104:107], a[24:27], v[32:35]\n"
"ds_read_b128 a[68:71], v192 offset:37888\n"
"v_mfma_f32_16x16x32_bf16 v[64:67], a[108:111], a[24:27], v[64:67]\n"
"v_exp_f32 v252, v252\n"
"v_mfma_f32_16x16x32_bf16 v[68:71], a[108:111], a[28:31], v[68:71]\n"
"v_mul_f32 v80, v80, v248\n"
"v_mfma_f32_16x16x32_bf16 v[36:39], a[104:107], a[28:31], v[36:39]\n"
"v_exp_f32 v253, v253\n"
"v_mfma_f32_16x16x32_bf16 v[72:75], a[64:67], a[0:3], v[72:75]\n"
"buffer_load_dwordx4 a[100:103], v200, s[36:39], s25 offen offset:3072 sc1\n"
"s_add_u32 s25, s25, 4096\n"
"v_mfma_f32_16x16x32_bf16 v[76:79], a[64:67], a[4:7], v[76:79]\n"
"v_mul_f32 v81, v81, v249\n"
"s_waitcnt vmcnt(29)\n"
"s_waitcnt lgkmcnt(0)\n"
"v_mfma_f32_16x16x32_bf16 v[8:11], a[112:115], a[32:35], v[8:11]\n"
"ds_read_b128 a[0:3], v176 offset:53248\n"
"v_mfma_f32_16x16x32_bf16 v[40:43], a[116:119], a[32:35], v[40:43]\n"
"ds_read_b128 a[4:7], v176 offset:55296\n"
"v_mfma_f32_16x16x32_bf16 v[44:47], a[116:119], a[36:39], v[44:47]\n"
"buffer_load_dwordx4 v212, s[48:51], s24 offen offset:1024 lds\n"
"v_mfma_f32_16x16x32_bf16 v[12:15], a[112:115], a[36:39], v[12:15]\n"
"ds_read_b128 a[8:11], v177 offset:53248\n"
"v_mfma_f32_16x16x32_bf16 v[16:19], a[112:115], a[40:43], v[16:19]\n"
"ds_read_b128 a[12:15], v177 offset:55296\n"
"v_mfma_f32_16x16x32_bf16 v[48:51], a[116:119], a[40:43], v[48:51]\n"
"v_exp_f32 v254, v254\n"
"v_mfma_f32_16x16x32_bf16 v[52:55], a[116:119], a[44:47], v[52:55]\n"
"ds_read_b128 a[16:19], v178 offset:53248\n"
"v_mul_f32 v82, v82, v250\n"
"v_mfma_f32_16x16x32_bf16 v[20:23], a[112:115], a[44:47], v[20:23]\n"
"ds_read_b128 a[20:23], v178 offset:55296\n"
"v_mfma_f32_16x16x32_bf16 v[24:27], a[112:115], a[48:51], v[24:27]\n"
"buffer_load_dwordx4 a[104:107], v199, s[36:39], s25 offen offset:0 sc1\n"
"v_mfma_f32_16x16x32_bf16 v[56:59], a[116:119], a[48:51], v[56:59]\n"
"ds_read_b128 a[24:27], v179 offset:53248\n"
"v_mfma_f32_16x16x32_bf16 v[60:63], a[116:119], a[52:55], v[60:63]\n"
"ds_read_b128 a[28:31], v179 offset:55296\n"
"v_mfma_f32_16x16x32_bf16 v[28:31], a[112:115], a[52:55], v[28:31]\n"
"buffer_load_dwordx4 v216, s[48:51], s24 offen offset:2048 lds\n"
"v_mfma_f32_16x16x32_bf16 v[32:35], a[112:115], a[56:59], v[32:35]\n"
"ds_read_b128 a[64:67], v192 offset:38912\n"
"v_mfma_f32_16x16x32_bf16 v[64:67], a[116:119], a[56:59], v[64:67]\n"
"v_exp_f32 v255, v255\n"
"v_mfma_f32_16x16x32_bf16 v[68:71], a[116:119], a[60:63], v[68:71]\n"
"v_mul_f32 v83, v83, v251\n"
"v_fma_f32 v252, v252, v211, v211\n"
"v_mfma_f32_16x16x32_bf16 v[36:39], a[112:115], a[60:63], v[36:39]\n"
"v_fma_f32 v253, v253, v211, v211\n"
"v_fma_f32 v254, v254, v211, v211\n"
"v_mfma_f32_16x16x32_bf16 v[72:75], a[68:71], a[32:35], v[72:75]\n"
"buffer_load_dwordx4 a[108:111], v200, s[36:39], s25 offen offset:0 sc1\n"
"v_fma_f32 v255, v255, v211, v211\n"
"v_mfma_f32_16x16x32_bf16 v[76:79], a[68:71], a[36:39], v[76:79]\n"
"v_rcp_f32 v252, v252\n"
"s_waitcnt vmcnt(30)\n"
"s_waitcnt lgkmcnt(0)\n"
"v_mfma_f32_16x16x32_bf16 v[8:11], a[120:123], a[0:3], v[8:11]\n"
"ds_read_b128 a[32:35], v180 offset:53248\n"
"v_mfma_f32_16x16x32_bf16 v[40:43], a[124:127], a[0:3], v[40:43]\n"
"ds_read_b128 a[36:39], v180 offset:55296\n"
"v_mfma_f32_16x16x32_bf16 v[44:47], a[124:127], a[4:7], v[44:47]\n"
"buffer_load_dwordx4 v220, s[48:51], s24 offen offset:3072 lds\n"
"s_add_u32 s24, s24, 128\n"
"v_mfma_f32_16x16x32_bf16 v[12:15], a[120:123], a[4:7], v[12:15]\n"
"ds_read_b128 a[40:43], v181 offset:53248\n"
"v_mfma_f32_16x16x32_bf16 v[16:19], a[120:123], a[8:11], v[16:19]\n"
"ds_read_b128 a[44:47], v181 offset:55296\n"
"v_mfma_f32_16x16x32_bf16 v[48:51], a[124:127], a[8:11], v[48:51]\n"
"v_rcp_f32 v253, v253\n"
"v_mfma_f32_16x16x32_bf16 v[52:55], a[124:127], a[12:15], v[52:55]\n"
"ds_read_b128 a[48:51], v182 offset:53248\n"
"v_mfma_f32_16x16x32_bf16 v[20:23], a[120:123], a[12:15], v[20:23]\n"
"ds_read_b128 a[52:55], v182 offset:55296\n"
"v_mfma_f32_16x16x32_bf16 v[24:27], a[120:123], a[16:19], v[24:27]\n"
"buffer_load_dwordx4 a[112:115], v199, s[36:39], s25 offen offset:1024 sc1\n"
"v_mfma_f32_16x16x32_bf16 v[56:59], a[124:127], a[16:19], v[56:59]\n"
"ds_read_b128 a[56:59], v183 offset:53248\n"
"v_mfma_f32_16x16x32_bf16 v[60:63], a[124:127], a[20:23], v[60:63]\n"
"ds_read_b128 a[60:63], v183 offset:55296\n"
"v_mfma_f32_16x16x32_bf16 v[28:31], a[120:123], a[20:23], v[28:31]\n"
"s_add_u32 m0, s7, 118784\n"
"s_nop 0\n"
"buffer_load_dwordx4 v194, s[36:39], s26 offen sc1 lds\n"
"s_add_u32 s26, s26, 4096\n"
"v_mfma_f32_16x16x32_bf16 v[32:35], a[120:123], a[24:27], v[32:35]\n"
"ds_read_b128 a[68:71], v192 offset:39936\n"
"v_mfma_f32_16x16x32_bf16 v[64:67], a[124:127], a[24:27], v[64:67]\n"
"v_rcp_f32 v254, v254\n"
"v_mfma_f32_16x16x32_bf16 v[68:71], a[124:127], a[28:31], v[68:71]\n"
"v_rcp_f32 v255, v255\n"
"v_mfma_f32_16x16x32_bf16 v[36:39], a[120:123], a[28:31], v[36:39]\n"
"v_mul_f32 v112, v112, v252\n"
"v_mul_f32 v113, v113, v253\n"
"v_mfma_f32_16x16x32_bf16 v[72:75], a[64:67], a[0:3], v[72:75]\n"
"buffer_load_dwordx4 a[116:119], v200, s[36:39], s25 offen offset:1024 sc1\n"
"v_mul_f32 v114, v114, v254\n"
"v_mfma_f32_16x16x32_bf16 v[76:79], a[64:67], a[4:7], v[76:79]\n"
"v_mul_f32 v115, v115, v255\n"
"v_mul_f32 v248, v144, v144\n"
"s_waitcnt vmcnt(32)\n"
"s_waitcnt vmcnt(30) lgkmcnt(0)\n"
"s_barrier\n"
"v_mfma_f32_16x16x32_bf16 v[8:11], a[128:131], a[32:35], v[8:11]\n"
"ds_read_b128 a[0:3], v184 offset:0\n"
"v_mul_f32 v249, v145, v145\n"
"v_mfma_f32_16x16x32_bf16 v[40:43], a[132:135], a[32:35], v[40:43]\n"
"ds_read_b128 a[4:7], v184 offset:2048\n"
"v_mul_f32 v250, v146, v146\n"
"v_mfma_f32_16x16x32_bf16 v[44:47], a[132:135], a[36:39], v[44:47]\n"
"s_add_u32 m0, s6, 122880\n"
"s_nop 0\n"
"buffer_load_dwordx4 v208, s[48:51], s24 offen offset:0 lds\n"
"v_mfma_f32_16x16x32_bf16 v[12:15], a[128:131], a[36:39], v[12:15]\n"
"ds_read_b128 a[8:11], v185 offset:0\n"
"v_mul_f32 v251, v147, v147\n"
"v_mfma_f32_16x16x32_bf16 v[16:19], a[128:131], a[40:43], v[16:19]\n"
"ds_read_b128 a[12:15], v185 offset:2048\n"
"v_fma_f32 v248, v248, v210, v209\n"
"v_mfma_f32_16x16x32_bf16 v[48:51], a[132:135], a[40:43], v[48:51]\n"
"v_fma_f32 v249, v249, v210, v209\n"
"v_fma_f32 v250, v250, v210, v209\n"
"v_mfma_f32_16x16x32_bf16 v[52:55], a[132:135], a[44:47], v[52:55]\n"
"ds_read_b128 a[16:19], v186 offset:0\n"
"v_fma_f32 v251, v251, v210, v209\n"
"v_mfma_f32_16x16x32_bf16 v[20:23], a[128:131], a[44:47], v[20:23]\n"
"ds_read_b128 a[20:23], v186 offset:2048\n"
"v_mul_f32 v248, v144, v248\n"
"v_mfma_f32_16x16x32_bf16 v[24:27], a[128:131], a[48:51], v[24:27]\n"
"buffer_load_dwordx4 a[120:123], v199, s[36:39], s25 offen offset:2048 sc1\n"
"v_mul_f32 v249, v145, v249\n"
"v_mfma_f32_16x16x32_bf16 v[56:59], a[132:135], a[48:51], v[56:59]\n"
"ds_read_b128 a[24:27], v187 offset:0\n"
"v_mul_f32 v250, v146, v250\n"
"v_mfma_f32_16x16x32_bf16 v[60:63], a[132:135], a[52:55], v[60:63]\n"
"ds_read_b128 a[28:31], v187 offset:2048\n"
"v_mul_f32 v251, v147, v251\n"
"v_mfma_f32_16x16x32_bf16 v[28:31], a[128:131], a[52:55], v[28:31]\n"
"buffer_load_dwordx4 v212, s[48:51], s24 offen offset:1024 lds\n"
"v_mfma_f32_16x16x32_bf16 v[32:35], a[128:131], a[56:59], v[32:35]\n"
"ds_read_b128 a[64:67], v193 offset:0\n"
"v_mfma_f32_16x16x32_bf16 v[64:67], a[132:135], a[56:59], v[64:67]\n"
"v_exp_f32 v248, v248\n"
"v_mfma_f32_16x16x32_bf16 v[68:71], a[132:135], a[60:63], v[68:71]\n"
"v_mul_f32 v252, v84, v84\n"
"v_mul_f32 v253, v85, v85\n"
"v_mfma_f32_16x16x32_bf16 v[36:39], a[128:131], a[60:63], v[36:39]\n"
"v_exp_f32 v249, v249\n"
"v_mfma_f32_16x16x32_bf16 v[72:75], a[68:71], a[32:35], v[72:75]\n"
"buffer_load_dwordx4 a[124:127], v200, s[36:39], s25 offen offset:2048 sc1\n"
"v_mul_f32 v254, v86, v86\n"
"v_mfma_f32_16x16x32_bf16 v[76:79], a[68:71], a[36:39], v[76:79]\n"
"v_mul_f32 v255, v87, v87\n"
"s_waitcnt vmcnt(26)\n"
"s_waitcnt lgkmcnt(0)\n"
"v_mfma_f32_16x16x32_bf16 v[8:11], a[72:75], a[0:3], v[8:11]\n"
"ds_read_b128 a[32:35], v188 offset:0\n"
"v_mfma_f32_16x16x32_bf16 v[40:43], a[76:79], a[0:3], v[40:43]\n"
"ds_read_b128 a[36:39], v188 offset:2048\n"
"v_mfma_f32_16x16x32_bf16 v[44:47], a[76:79], a[4:7], v[44:47]\n"
"buffer_load_dwordx4 v216, s[48:51], s24 offen offset:2048 lds\n"
"v_mfma_f32_16x16x32_bf16 v[12:15], a[72:75], a[4:7], v[12:15]\n"
"ds_read_b128 a[40:43], v189 offset:0\n"
"v_mfma_f32_16x16x32_bf16 v[16:19], a[72:75], a[8:11], v[16:19]\n"
"ds_read_b128 a[44:47], v189 offset:2048\n"
"v_mfma_f32_16x16x32_bf16 v[48:51], a[76:79], a[8:11], v[48:51]\n"
"v_exp_f32 v250, v250\n"
"v_mfma_f32_16x16x32_bf16 v[52:55], a[76:79], a[12:15], v[52:55]\n"
"ds_read_b128 a[48:51], v190 offset:0\n"
"v_fma_f32 v252, v252, v214, v213\n"
"v_mfma_f32_16x16x32_bf16 v[20:23], a[72:75], a[12:15], v[20:23]\n"
"ds_read_b128 a[52:55], v190 offset:2048\n"
"v_fma_f32 v253, v253, v214, v213\n"
"v_mfma_f32_16x16x32_bf16 v[24:27], a[72:75], a[16:19], v[24:27]\n"
"buffer_load_dwordx4 a[128:131], v199, s[36:39], s25 offen offset:3072 sc1\n"
"v_mfma_f32_16x16x32_bf16 v[56:59], a[76:79], a[16:19], v[56:59]\n"
"ds_read_b128 a[56:59], v191 offset:0\n"
"v_mfma_f32_16x16x32_bf16 v[60:63], a[76:79], a[20:23], v[60:63]\n"
"ds_read_b128 a[60:63], v191 offset:2048\n"
"v_mfma_f32_16x16x32_bf16 v[28:31], a[72:75], a[20:23], v[28:31]\n"
"buffer_load_dwordx4 v220, s[48:51], s24 offen offset:3072 lds\n"
"s_add_u32 s24, s24, 128\n"
"v_mfma_f32_16x16x32_bf16 v[32:35], a[72:75], a[24:27], v[32:35]\n"
"ds_read_b128 a[68:71], v193 offset:1024\n"
"v_mfma_f32_16x16x32_bf16 v[64:67], a[76:79], a[24:27], v[64:67]\n"
"v_exp_f32 v251, v251\n"
"v_mfma_f32_16x16x32_bf16 v[68:71], a[76:79], a[28:31], v[68:71]\n"
"v_fma_f32 v254, v254, v214, v213\n"
"v_fma_f32 v255, v255, v214, v213\n"
"v_mfma_f32_16x16x32_bf16 v[36:39], a[72:75], a[28:31], v[36:39]\n"
"v_fma_f32 v248, v248, v211, v211\n"
"v_fma_f32 v249, v249, v211, v211\n"
"v_mfma_f32_16x16x32_bf16 v[72:75], a[64:67], a[0:3], v[72:75]\n"
"buffer_load_dwordx4 a[132:135], v200, s[36:39], s25 offen offset:3072 sc1\n"
"s_add_u32 s25, s25, 4096\n"
"v_mfma_f32_16x16x32_bf16 v[76:79], a[64:67], a[4:7], v[76:79]\n"
"v_fma_f32 v250, v250, v211, v211\n"
"v_fma_f32 v251, v251, v211, v211\n"
"s_waitcnt vmcnt(25)\n"
"s_waitcnt lgkmcnt(0)\n"
"v_mfma_f32_16x16x32_bf16 v[8:11], a[80:83], a[32:35], v[8:11]\n"
"ds_read_b128 a[0:3], v184 offset:16384\n"
"v_mul_f32 v252, v84, v252\n"
"v_mfma_f32_16x16x32_bf16 v[40:43], a[84:87], a[32:35], v[40:43]\n"
"ds_read_b128 a[4:7], v184 offset:18432\n"
"v_mul_f32 v253, v85, v253\n"
"v_mfma_f32_16x16x32_bf16 v[44:47], a[84:87], a[36:39], v[44:47]\n"
"s_add_u32 m0, s6, 139264\n"
"s_nop 0\n"
"buffer_load_dwordx4 v208, s[48:51], s24 offen offset:0 lds\n"
"v_mfma_f32_16x16x32_bf16 v[12:15], a[80:83], a[36:39], v[12:15]\n"
"ds_read_b128 a[8:11], v185 offset:16384\n"
"v_mul_f32 v254, v86, v254\n"
"v_mfma_f32_16x16x32_bf16 v[16:19], a[80:83], a[40:43], v[16:19]\n"
"ds_read_b128 a[12:15], v185 offset:18432\n"
"v_mul_f32 v255, v87, v255\n"
"v_mfma_f32_16x16x32_bf16 v[48:51], a[84:87], a[40:43], v[48:51]\n"
"v_rcp_f32 v248, v248\n"
"v_mfma_f32_16x16x32_bf16 v[52:55], a[84:87], a[44:47], v[52:55]\n"
"ds_read_b128 a[16:19], v186 offset:16384\n"
"v_mfma_f32_16x16x32_bf16 v[20:23], a[80:83], a[44:47], v[20:23]\n"
"ds_read_b128 a[20:23], v186 offset:18432\n"
"v_mfma_f32_16x16x32_bf16 v[24:27], a[80:83], a[48:51], v[24:27]\n"
"buffer_load_dwordx4 a[72:75], v199, s[36:39], s25 offen offset:0 sc1\n"
"v_mfma_f32_16x16x32_bf16 v[56:59], a[84:87], a[48:51], v[56:59]\n"
"ds_read_b128 a[24:27], v187 offset:16384\n"
"v_mfma_f32_16x16x32_bf16 v[60:63], a[84:87], a[52:55], v[60:63]\n"
"ds_read_b128 a[28:31], v187 offset:18432\n"
"v_mfma_f32_16x16x32_bf16 v[28:31], a[80:83], a[52:55], v[28:31]\n"
"buffer_load_dwordx4 v212, s[48:51], s24 offen offset:1024 lds\n"
"v_mfma_f32_16x16x32_bf16 v[32:35], a[80:83], a[56:59], v[32:35]\n"
"ds_read_b128 a[64:67], v193 offset:2048\n"
"v_mfma_f32_16x16x32_bf16 v[64:67], a[84:87], a[56:59], v[64:67]\n"
"v_rcp_f32 v249, v249\n"
"v_mfma_f32_16x16x32_bf16 v[68:71], a[84:87], a[60:63], v[68:71]\n"
"v_rcp_f32 v250, v250\n"
"v_mfma_f32_16x16x32_bf16 v[36:39], a[80:83], a[60:63], v[36:39]\n"
"v_rcp_f32 v251, v251\n"
"v_mfma_f32_16x16x32_bf16 v[72:75], a[68:71], a[32:35], v[72:75]\n"
"buffer_load_dwordx4 a[76:79], v200, s[36:39], s25 offen offset:0 sc1\n"
"v_mfma_f32_16x16x32_bf16 v[76:79], a[68:71], a[36:39], v[76:79]\n"
"v_exp_f32 v252, v252\n"
"s_waitcnt vmcnt(24)\n"
"s_waitcnt lgkmcnt(0)\n"
"v_mfma_f32_16x16x32_bf16 v[8:11], a[88:91], a[0:3], v[8:11]\n"
"ds_read_b128 a[32:35], v188 offset:16384\n"
"v_mul_f32 v144, v144, v248\n"
"v_mfma_f32_16x16x32_bf16 v[40:43], a[92:95], a[0:3], v[40:43]\n"
"ds_read_b128 a[36:39], v188 offset:18432\n"
"v_mfma_f32_16x16x32_bf16 v[44:47], a[92:95], a[4:7], v[44:47]\n"
"buffer_load_dwordx4 v216, s[48:51], s24 offen offset:2048 lds\n"
"v_mfma_f32_16x16x32_bf16 v[12:15], a[88:91], a[4:7], v[12:15]\n"
"ds_read_b128 a[40:43], v189 offset:16384\n"
"v_mfma_f32_16x16x32_bf16 v[16:19], a[88:91], a[8:11], v[16:19]\n"
"ds_read_b128 a[44:47], v189 offset:18432\n"
"v_mfma_f32_16x16x32_bf16 v[48:51], a[92:95], a[8:11], v[48:51]\n"
"v_exp_f32 v253, v253\n"
"v_mfma_f32_16x16x32_bf16 v[52:55], a[92:95], a[12:15], v[52:55]\n"
"ds_read_b128 a[48:51], v190 offset:16384\n"
"v_mul_f32 v145, v145, v249\n"
"v_mfma_f32_16x16x32_bf16 v[20:23], a[88:91], a[12:15], v[20:23]\n"
"ds_read_b128 a[52:55], v190 offset:18432\n"
"v_mfma_f32_16x16x32_bf16 v[24:27], a[88:91], a[16:19], v[24:27]\n"
"buffer_load_dwordx4 a[80:83], v199, s[36:39], s25 offen offset:1024 sc1\n"
"v_mfma_f32_16x16x32_bf16 v[56:59], a[92:95], a[16:19], v[56:59]\n"
"ds_read_b128 a[56:59], v191 offset:16384\n"
"v_mfma_f32_16x16x32_bf16 v[60:63], a[92:95], a[20:23], v[60:63]\n"
"ds_read_b128 a[60:63], v191 offset:18432\n"
"v_mfma_f32_16x16x32_bf16 v[28:31], a[88:91], a[20:23], v[28:31]\n"
"buffer_load_dwordx4 v220, s[48:51], s24 offen offset:3072 lds\n"
"s_add_u32 s24, s24, 128\n"
"v_mfma_f32_16x16x32_bf16 v[32:35], a[88:91], a[24:27], v[32:35]\n"
"ds_read_b128 a[68:71], v193 offset:3072\n"
"v_mfma_f32_16x16x32_bf16 v[64:67], a[92:95], a[24:27], v[64:67]\n"
"v_exp_f32 v254, v254\n"
"v_mfma_f32_16x16x32_bf16 v[68:71], a[92:95], a[28:31], v[68:71]\n"
"s_add_u32 m0, s7, 155648\n"
"s_nop 0\n"
"buffer_load_dwordx4 v194, s[36:39], s26 offen sc1 lds\n"
"s_add_u32 s26, s26, 4096\n"
"v_mfma_f32_16x16x32_bf16 v[36:39], a[88:91], a[28:31], v[36:39]\n"
"v_mul_f32 v146, v146, v250\n"
"v_mfma_f32_16x16x32_bf16 v[72:75], a[64:67], a[0:3], v[72:75]\n"
"buffer_load_dwordx4 a[84:87], v200, s[36:39], s25 offen offset:1024 sc1\n"
"v_mfma_f32_16x16x32_bf16 v[76:79], a[64:67], a[4:7], v[76:79]\n"
"v_exp_f32 v255, v255\n"
"s_waitcnt vmcnt(25)\n"
"s_waitcnt vmcnt(35) lgkmcnt(0)\n"
"s_barrier\n"
"v_mfma_f32_16x16x32_bf16 v[8:11], a[96:99], a[32:35], v[8:11]\n"
"ds_read_b128 a[0:3], v184 offset:36864\n"
"v_mul_f32 v147, v147, v251\n"
"v_mfma_f32_16x16x32_bf16 v[40:43], a[100:103], a[32:35], v[40:43]\n"
"ds_read_b128 a[4:7], v184 offset:38912\n"
"v_fma_f32 v252, v252, v215, v215\n"
"v_mfma_f32_16x16x32_bf16 v[44:47], a[100:103], a[36:39], v[44:47]\n"
"s_add_u32 m0, s6, 0\n"
"s_nop 0\n"
"buffer_load_dwordx4 v208, s[48:51], s24 offen offset:0 lds\n"
"v_mfma_f32_16x16x32_bf16 v[12:15], a[96:99], a[36:39], v[12:15]\n"
"ds_read_b128 a[8:11], v185 offset:36864\n"
"v_fma_f32 v253, v253, v215, v215\n"
"v_mfma_f32_16x16x32_bf16 v[16:19], a[96:99], a[40:43], v[16:19]\n"
"ds_read_b128 a[12:15], v185 offset:38912\n"
"v_fma_f32 v254, v254, v215, v215\n"
"v_mfma_f32_16x16x32_bf16 v[48:51], a[100:103], a[40:43], v[48:51]\n"
"v_fma_f32 v255, v255, v215, v215\n"
"v_mfma_f32_16x16x32_bf16 v[52:55], a[100:103], a[44:47], v[52:55]\n"
"ds_read_b128 a[16:19], v186 offset:36864\n"
"v_mfma_f32_16x16x32_bf16 v[20:23], a[96:99], a[44:47], v[20:23]\n"
"ds_read_b128 a[20:23], v186 offset:38912\n"
"v_mfma_f32_16x16x32_bf16 v[24:27], a[96:99], a[48:51], v[24:27]\n"
"buffer_load_dwordx4 a[88:91], v199, s[36:39], s25 offen offset:2048 sc1\n"
"v_mfma_f32_16x16x32_bf16 v[56:59], a[100:103], a[48:51], v[56:59]\n"
"ds_read_b128 a[24:27], v187 offset:36864\n"
"v_mfma_f32_16x16x32_bf16 v[60:63], a[100:103], a[52:55], v[60:63]\n"
"ds_read_b128 a[28:31], v187 offset:38912\n"
"v_mfma_f32_16x16x32_bf16 v[28:31], a[96:99], a[52:55], v[28:31]\n"
"buffer_load_dwordx4 v212, s[48:51], s24 offen offset:1024 lds\n"
"v_mfma_f32_16x16x32_bf16 v[32:35], a[96:99], a[56:59], v[32:35]\n"
"ds_read_b128 a[64:67], v193 offset:36864\n"
"v_mfma_f32_16x16x32_bf16 v[64:67], a[100:103], a[56:59], v[64:67]\n"
"v_rcp_f32 v252, v252\n"
"v_mfma_f32_16x16x32_bf16 v[68:71], a[100:103], a[60:63], v[68:71]\n"
"v_rcp_f32 v253, v253\n"
"v_mfma_f32_16x16x32_bf16 v[36:39], a[96:99], a[60:63], v[36:39]\n"
"v_rcp_f32 v254, v254\n"
"v_mfma_f32_16x16x32_bf16 v[72:75], a[68:71], a[32:35], v[72:75]\n"
"buffer_load_dwordx4 a[92:95], v200, s[36:39], s25 offen offset:2048 sc1\n"
"v_mfma_f32_16x16x32_bf16 v[76:79], a[68:71], a[36:39], v[76:79]\n"
"v_rcp_f32 v255, v255\n"
"s_waitcnt vmcnt(25)\n"
"s_waitcnt lgkmcnt(0)\n"
"v_mfma_f32_16x16x32_bf16 v[8:11], a[104:107], a[0:3], v[8:11]\n"
"ds_read_b128 a[32:35], v188 offset:36864\n"
"v_mul_f32 v84, v84, v252\n"
"v_mfma_f32_16x16x32_bf16 v[40:43], a[108:111], a[0:3], v[40:43]\n"
"ds_read_b128 a[36:39], v188 offset:38912\n"
"v_mul_f32 v85, v85, v253\n"
"v_mfma_f32_16x16x32_bf16 v[44:47], a[108:111], a[4:7], v[44:47]\n"
"buffer_load_dwordx4 v216, s[48:51], s24 offen offset:2048 lds\n"
"v_mul_f32 v86, v86, v254\n"
"v_mfma_f32_16x16x32_bf16 v[12:15], a[104:107], a[4:7], v[12:15]\n"
"ds_read_b128 a[40:43], v189 offset:36864\n"
"v_mul_f32 v87, v87, v255\n"
"v_mfma_f32_16x16x32_bf16 v[16:19], a[104:107], a[8:11], v[16:19]\n"
"ds_read_b128 a[44:47], v189 offset:38912\n"
"v_mul_f32 v248, v116, v116\n"
"v_mfma_f32_16x16x32_bf16 v[48:51], a[108:111], a[8:11], v[48:51]\n"
"v_mul_f32 v249, v117, v117\n"
"v_mul_f32 v250, v118, v118\n"
"v_mfma_f32_16x16x32_bf16 v[52:55], a[108:111], a[12:15], v[52:55]\n"
"ds_read_b128 a[48:51], v190 offset:36864\n"
"v_mul_f32 v251, v119, v119\n"
"v_mfma_f32_16x16x32_bf16 v[20:23], a[104:107], a[12:15], v[20:23]\n"
"ds_read_b128 a[52:55], v190 offset:38912\n"
"v_fma_f32 v248, v248, v214, v213\n"
"v_mfma_f32_16x16x32_bf16 v[24:27], a[104:107], a[16:19], v[24:27]\n"
"buffer_load_dwordx4 a[96:99], v199, s[36:39], s25 offen offset:3072 sc1\n"
"v_fma_f32 v249, v249, v214, v213\n"
"v_mfma_f32_16x16x32_bf16 v[56:59], a[108:111], a[16:19], v[56:59]\n"
"ds_read_b128 a[56:59], v191 offset:36864\n"
"v_fma_f32 v250, v250, v214, v213\n"
"v_mfma_f32_16x16x32_bf16 v[60:63], a[108:111], a[20:23], v[60:63]\n"
"ds_read_b128 a[60:63], v191 offset:38912\n"
"v_fma_f32 v251, v251, v214, v213\n"
"v_mfma_f32_16x16x32_bf16 v[28:31], a[104:107], a[20:23], v[28:31]\n"
"buffer_load_dwordx4 v220, s[48:51], s24 offen offset:3072 lds\n"
"s_add_u32 s24, s24, 128\n"
"v_mfma_f32_16x16x32_bf16 v[32:35], a[104:107], a[24:27], v[32:35]\n"
"ds_read_b128 a[68:71], v193 offset:37888\n"
"s_waitcnt vmcnt(55)\n"
"v_mfma_f32_16x16x32_bf16 v[64:67], a[108:111], a[24:27], v[64:67]\n"
"s_lshl_b32 s71, s96, 2\n"
"s_add_u32 s71, s71, s65\n"
"v_mfma_f32_16x16x32_bf16 v[68:71], a[108:111], a[28:31], v[68:71]\n"
"s_lshl_b32 s71, s71, 3\n"
"s_add_u32 s71, s71, 53248\n"
"v_mfma_f32_16x16x32_bf16 v[36:39], a[104:107], a[28:31], v[36:39]\n"
"v_mov_b32 v4, s68\n"
"v_mov_b32 v5, s69\n"
"v_mfma_f32_16x16x32_bf16 v[72:75], a[64:67], a[0:3], v[72:75]\n"
"buffer_load_dwordx4 a[100:103], v200, s[36:39], s25 offen offset:3072 sc1\n"
"s_add_u32 s25, s25, 4096\n"
"v_mfma_f32_16x16x32_bf16 v[76:79], a[64:67], a[4:7], v[76:79]\n"
"v_min_u32 v6, 1, v1\n"
"s_waitcnt vmcnt(25)\n"
"s_waitcnt lgkmcnt(0)\n"
"v_mfma_f32_16x16x32_bf16 v[8:11], a[112:115], a[32:35], v[8:11]\n"
"ds_read_b128 a[0:3], v184 offset:53248\n"
"v_lshlrev_b32 v6, 30, v6\n"
"v_mfma_f32_16x16x32_bf16 v[40:43], a[116:119], a[32:35], v[40:43]\n"
"ds_read_b128 a[4:7], v184 offset:55296\n"
"buffer_store_dwordx2 v[4:5], v6, s[36:39], s71 offen sc1\n"
"v_mfma_f32_16x16x32_bf16 v[44:47], a[116:119], a[36:39], v[44:47]\n"
"s_add_u32 m0, s6, 16384\n"
"s_nop 0\n"
"buffer_load_dwordx4 v208, s[48:51], s24 offen offset:0 lds\n"
"v_mfma_f32_16x16x32_bf16 v[12:15], a[112:115], a[36:39], v[12:15]\n"
"ds_read_b128 a[8:11], v185 offset:53248\n"
"s_lshl_b32 s71, s98, 2\n"
"v_mfma_f32_16x16x32_bf16 v[16:19], a[112:115], a[40:43], v[16:19]\n"
"ds_read_b128 a[12:15], v185 offset:55296\n"
"s_and_b32 s72, s64, 3\n"
"v_mfma_f32_16x16x32_bf16 v[48:51], a[116:119], a[40:43], v[48:51]\n"
"s_add_u32 s71, s71, s72\n"
"s_lshl_b32 s71, s71, 3\n"
"v_mfma_f32_16x16x32_bf16 v[52:55], a[116:119], a[44:47], v[52:55]\n"
"ds_read_b128 a[16:19], v186 offset:53248\n"
"s_add_u32 s71, s71, 53248\n"
"v_mfma_f32_16x16x32_bf16 v[20:23], a[112:115], a[44:47], v[20:23]\n"
"ds_read_b128 a[20:23], v186 offset:55296\n"
"s_min_u32 s72, s65, 1\n"
"v_mfma_f32_16x16x32_bf16 v[24:27], a[112:115], a[48:51], v[24:27]\n"
"buffer_load_dwordx4 a[104:107], v199, s[36:39], s25 offen offset:0 sc1\n"
"s_lshl_b32 s72, s72, 30\n"
"v_mfma_f32_16x16x32_bf16 v[56:59], a[116:119], a[48:51], v[56:59]\n"
"ds_read_b128 a[24:27], v187 offset:53248\n"
"v_mov_b32 v4, s68\n"
"v_mfma_f32_16x16x32_bf16 v[60:63], a[116:119], a[52:55], v[60:63]\n"
"ds_read_b128 a[28:31], v187 offset:55296\n"
"v_mov_b32 v5, s69\n"
"v_mfma_f32_16x16x32_bf16 v[28:31], a[112:115], a[52:55], v[28:31]\n"
"buffer_load_dwordx4 v212, s[48:51], s24 offen offset:1024 lds\n"
"v_min_u32 v6, 1, v1\n"
"v_mfma_f32_16x16x32_bf16 v[32:35], a[112:115], a[56:59], v[32:35]\n"
"ds_read_b128 a[64:67], v193 offset:38912\n"
"v_lshlrev_b32 v6, 30, v6\n"
"v_mfma_f32_16x16x32_bf16 v[64:67], a[116:119], a[56:59], v[64:67]\n"
"v_or_b32 v6, s72, v6\n"
"buffer_store_dwordx2 v[4:5], v6, s[36:39], s71 offen sc1\n"
"v_mfma_f32_16x16x32_bf16 v[68:71], a[116:119], a[60:63], v[68:71]\n"
"v_mfma_f32_16x16x32_bf16 v[36:39], a[112:115], a[60:63], v[36:39]\n"
"v_mfma_f32_16x16x32_bf16 v[72:75], a[68:71], a[32:35], v[72:75]\n"
"buffer_load_dwordx4 a[108:111], v200, s[36:39], s25 offen offset:0 sc1\n"
"v_mfma_f32_16x16x32_bf16 v[76:79], a[68:71], a[36:39], v[76:79]\n"
"s_waitcnt vmcnt(27)\n"
"s_waitcnt lgkmcnt(0)\n"
"v_mfma_f32_16x16x32_bf16 v[8:11], a[120:123], a[0:3], v[8:11]\n"
"ds_read_b128 a[32:35], v188 offset:53248\n"
"buffer_load_dwordx4 v[4:7], v228, s[36:39], s16 offen sc1\n"
"v_mfma_f32_16x16x32_bf16 v[40:43], a[124:127], a[0:3], v[40:43]\n"
"ds_read_b128 a[36:39], v188 offset:55296\n"
"v_mul_f32 v248, v116, v248\n"
"v_mfma_f32_16x16x32_bf16 v[44:47], a[124:127], a[4:7], v[44:47]\n"
"buffer_load_dwordx4 v216, s[48:51], s24 offen offset:2048 lds\n"
"v_mul_f32 v249, v117, v249\n"
"v_mfma_f32_16x16x32_bf16 v[12:15], a[120:123], a[4:7], v[12:15]\n"
"ds_read_b128 a[40:43], v189 offset:53248\n"
"v_mul_f32 v250, v118, v250\n"
"v_mfma_f32_16x16x32_bf16 v[16:19], a[120:123], a[8:11], v[16:19]\n"
"ds_read_b128 a[44:47], v189 offset:55296\n"
"v_mul_f32 v251, v119, v251\n"
"v_mfma_f32_16x16x32_bf16 v[48:51], a[124:127], a[8:11], v[48:51]\n"
"v_exp_f32 v248, v248\n"
"v_mfma_f32_16x16x32_bf16 v[52:55], a[124:127], a[12:15], v[52:55]\n"
"ds_read_b128 a[48:51], v190 offset:53248\n"
"v_mul_f32 v252, v148, v148\n"
"v_mfma_f32_16x16x32_bf16 v[20:23], a[120:123], a[12:15], v[20:23]\n"
"ds_read_b128 a[52:55], v190 offset:55296\n"
"v_mul_f32 v253, v149, v149\n"
"v_mfma_f32_16x16x32_bf16 v[24:27], a[120:123], a[16:19], v[24:27]\n"
"buffer_load_dwordx4 a[112:115], v199, s[36:39], s25 offen offset:1024 sc1\n"
"v_mfma_f32_16x16x32_bf16 v[56:59], a[124:127], a[16:19], v[56:59]\n"
"ds_read_b128 a[56:59], v191 offset:53248\n"
"v_mfma_f32_16x16x32_bf16 v[60:63], a[124:127], a[20:23], v[60:63]\n"
"ds_read_b128 a[60:63], v191 offset:55296\n"
"v_mfma_f32_16x16x32_bf16 v[28:31], a[120:123], a[20:23], v[28:31]\n"
"buffer_load_dwordx4 v220, s[48:51], s24 offen offset:3072 lds\n"
"s_add_u32 s24, s24, 128\n"
"v_mfma_f32_16x16x32_bf16 v[32:35], a[120:123], a[24:27], v[32:35]\n"
"ds_read_b128 a[68:71], v193 offset:39936\n"
"v_mfma_f32_16x16x32_bf16 v[64:67], a[124:127], a[24:27], v[64:67]\n"
"v_exp_f32 v249, v249\n"
"v_mfma_f32_16x16x32_bf16 v[68:71], a[124:127], a[28:31], v[68:71]\n"
"s_add_u32 m0, s7, 32768\n"
"s_nop 0\n"
"buffer_load_dwordx4 v194, s[36:39], s26 offen sc1 lds\n"
"s_add_u32 s26, s26, 4096\n"
"v_mfma_f32_16x16x32_bf16 v[36:39], a[120:123], a[28:31], v[36:39]\n"
"v_mul_f32 v254, v150, v150\n"
"v_mul_f32 v255, v151, v151\n"
"v_mfma_f32_16x16x32_bf16 v[72:75], a[64:67], a[0:3], v[72:75]\n"
"buffer_load_dwordx4 a[116:119], v200, s[36:39], s25 offen offset:1024 sc1\n"
"v_mfma_f32_16x16x32_bf16 v[76:79], a[64:67], a[4:7], v[76:79]\n"
"v_exp_f32 v250, v250\n"
"s_waitcnt vmcnt(29)\n"
"s_waitcnt vmcnt(38) lgkmcnt(0)\n"
"s_barrier\n"
"v_mfma_f32_16x16x32_bf16 v[8:11], a[128:131], a[32:35], v[8:11]\n"
"ds_read_b128 a[0:3], v176 offset:0\n"
"v_fma_f32 v252, v252, v214, v213\n"
"v_mfma_f32_16x16x32_bf16 v[40:43], a[132:135], a[32:35], v[40:43]\n"
"ds_read_b128 a[4:7], v176 offset:2048\n"
"v_fma_f32 v253, v253, v214, v213\n"
"v_mfma_f32_16x16x32_bf16 v[44:47], a[132:135], a[36:39], v[44:47]\n"
"s_add_u32 m0, s6, 36864\n"
"s_nop 0\n"
"buffer_load_dwordx4 v208, s[48:51], s24 offen offset:0 lds\n"
"v_mfma_f32_16x16x32_bf16 v[12:15], a[128:131], a[36:39], v[12:15]\n"
"ds_read_b128 a[8:11], v177 offset:0\n"
"v_mfma_f32_16x16x32_bf16 v[16:19], a[128:131], a[40:43], v[16:19]\n"
"ds_read_b128 a[12:15], v177 offset:2048\n"
"v_mfma_f32_16x16x32_bf16 v[48:51], a[132:135], a[40:43], v[48:51]\n"
"v_exp_f32 v251, v251\n"
"v_mfma_f32_16x16x32_bf16 v[52:55], a[132:135], a[44:47], v[52:55]\n"
"ds_read_b128 a[16:19], v178 offset:0\n"
"v_fma_f32 v254, v254, v214, v213\n"
"v_mfma_f32_16x16x32_bf16 v[20:23], a[128:131], a[44:47], v[20:23]\n"
"ds_read_b128 a[20:23], v178 offset:2048\n"
"v_fma_f32 v255, v255, v214, v213\n"
"v_mfma_f32_16x16x32_bf16 v[24:27], a[128:131], a[48:51], v[24:27]\n"
"buffer_load_dwordx4 a[120:123], v199, s[36:39], s25 offen offset:2048 sc1\n"
"v_fma_f32 v248, v248, v215, v215\n"
"v_mfma_f32_16x16x32_bf16 v[56:59], a[132:135], a[48:51], v[56:59]\n"
"ds_read_b128 a[24:27], v179 offset:0\n"
"v_fma_f32 v249, v249, v215, v215\n"
"v_mfma_f32_16x16x32_bf16 v[60:63], a[132:135], a[52:55], v[60:63]\n"
"ds_read_b128 a[28:31], v179 offset:2048\n"
"v_fma_f32 v250, v250, v215, v215\n"
"v_mfma_f32_16x16x32_bf16 v[28:31], a[128:131], a[52:55], v[28:31]\n"
"buffer_load_dwordx4 v212, s[48:51], s24 offen offset:1024 lds\n"
"v_fma_f32 v251, v251, v215, v215\n"
"v_mfma_f32_16x16x32_bf16 v[32:35], a[128:131], a[56:59], v[32:35]\n"
"ds_read_b128 a[64:67], v192 offset:0\n"
"v_mul_f32 v252, v148, v252\n"
"v_mfma_f32_16x16x32_bf16 v[64:67], a[132:135], a[56:59], v[64:67]\n"
"v_mul_f32 v253, v149, v253\n"
"v_mul_f32 v254, v150, v254\n"
"v_mfma_f32_16x16x32_bf16 v[68:71], a[132:135], a[60:63], v[68:71]\n"
"v_mul_f32 v255, v151, v255\n"
"v_mfma_f32_16x16x32_bf16 v[36:39], a[128:131], a[60:63], v[36:39]\n"
"v_rcp_f32 v248, v248\n"
"v_mfma_f32_16x16x32_bf16 v[72:75], a[68:71], a[32:35], v[72:75]\n"
"buffer_load_dwordx4 a[124:127], v200, s[36:39], s25 offen offset:2048 sc1\n"
"v_mfma_f32_16x16x32_bf16 v[76:79], a[68:71], a[36:39], v[76:79]\n"
"v_rcp_f32 v249, v249\n"
"s_waitcnt vmcnt(29)\n"
"s_waitcnt lgkmcnt(0)\n"
"v_mfma_f32_16x16x32_bf16 v[8:11], a[72:75], a[0:3], v[8:11]\n"
"ds_read_b128 a[32:35], v180 offset:0\n"
"v_mfma_f32_16x16x32_bf16 v[40:43], a[76:79], a[0:3], v[40:43]\n"
"ds_read_b128 a[36:39], v180 offset:2048\n"
"v_mfma_f32_16x16x32_bf16 v[44:47], a[76:79], a[4:7], v[44:47]\n"
"buffer_load_dwordx4 v216, s[48:51], s24 offen offset:2048 lds\n"
"v_mfma_f32_16x16x32_bf16 v[12:15], a[72:75], a[4:7], v[12:15]\n"
"ds_read_b128 a[40:43], v181 offset:0\n"
"v_mfma_f32_16x16x32_bf16 v[16:19], a[72:75], a[8:11], v[16:19]\n"
"ds_read_b128 a[44:47], v181 offset:2048\n"
"v_mfma_f32_16x16x32_bf16 v[48:51], a[76:79], a[8:11], v[48:51]\n"
"v_rcp_f32 v250, v250\n"
"v_mfma_f32_16x16x32_bf16 v[52:55], a[76:79], a[12:15], v[52:55]\n"
"ds_read_b128 a[48:51], v182 offset:0\n"
"v_mfma_f32_16x16x32_bf16 v[20:23], a[72:75], a[12:15], v[20:23]\n"
"ds_read_b128 a[52:55], v182 offset:2048\n"
"v_mfma_f32_16x16x32_bf16 v[24:27], a[72:75], a[16:19], v[24:27]\n"
"buffer_load_dwordx4 a[128:131], v199, s[36:39], s25 offen offset:3072 sc1\n"
"v_mfma_f32_16x16x32_bf16 v[56:59], a[76:79], a[16:19], v[56:59]\n"
"ds_read_b128 a[56:59], v183 offset:0\n"
"v_mfma_f32_16x16x32_bf16 v[60:63], a[76:79], a[20:23], v[60:63]\n"
"ds_read_b128 a[60:63], v183 offset:2048\n"
"v_mfma_f32_16x16x32_bf16 v[28:31], a[72:75], a[20:23], v[28:31]\n"
"buffer_load_dwordx4 v220, s[48:51], s24 offen offset:3072 lds\n"
"s_add_u32 s24, s24, 128\n"
"v_mfma_f32_16x16x32_bf16 v[32:35], a[72:75], a[24:27], v[32:35]\n"
"ds_read_b128 a[68:71], v192 offset:1024\n"
"v_mfma_f32_16x16x32_bf16 v[64:67], a[76:79], a[24:27], v[64:67]\n"
"v_rcp_f32 v251, v251\n"
"v_mfma_f32_16x16x32_bf16 v[68:71], a[76:79], a[28:31], v[68:71]\n"
"v_exp_f32 v252, v252\n"
"v_mfma_f32_16x16x32_bf16 v[36:39], a[72:75], a[28:31], v[36:39]\n"
"v_mul_f32 v116, v116, v248\n"
"v_mfma_f32_16x16x32_bf16 v[72:75], a[64:67], a[0:3], v[72:75]\n"
"buffer_load_dwordx4 a[132:135], v200, s[36:39], s25 offen offset:3072 sc1\n"
"s_add_u32 s25, s25, 4096\n"
"v_mfma_f32_16x16x32_bf16 v[76:79], a[64:67], a[4:7], v[76:79]\n"
"v_exp_f32 v253, v253\n"
"s_waitcnt vmcnt(28)\n"
"s_waitcnt lgkmcnt(0)\n"
"v_mfma_f32_16x16x32_bf16 v[8:11], a[80:83], a[32:35], v[8:11]\n"
"ds_read_b128 a[0:3], v176 offset:16384\n"
"v_mul_f32 v117, v117, v249\n"
"v_mfma_f32_16x16x32_bf16 v[40:43], a[84:87], a[32:35], v[40:43]\n"
"ds_read_b128 a[4:7], v176 offset:18432\n"
"v_mfma_f32_16x16x32_bf16 v[44:47], a[84:87], a[36:39], v[44:47]\n"
"s_add_u32 m0, s6, 53248\n"
"s_nop 0\n"
"buffer_load_dwordx4 v208, s[48:51], s24 offen offset:0 lds\n"
"v_mfma_f32_16x16x32_bf16 v[12:15], a[80:83], a[36:39], v[12:15]\n"
"ds_read_b128 a[8:11], v177 offset:16384\n"
"v_mfma_f32_16x16x32_bf16 v[16:19], a[80:83], a[40:43], v[16:19]\n"
"ds_read_b128 a[12:15], v177 offset:18432\n"
"v_mfma_f32_16x16x32_bf16 v[48:51], a[84:87], a[40:43], v[48:51]\n"
"v_exp_f32 v254, v254\n"
"v_mfma_f32_16x16x32_bf16 v[52:55], a[84:87], a[44:47], v[52:55]\n"
"ds_read_b128 a[16:19], v178 offset:16384\n"
"v_mul_f32 v118, v118, v250\n"
"v_mfma_f32_16x16x32_bf16 v[20:23], a[80:83], a[44:47], v[20:23]\n"
"ds_read_b128 a[20:23], v178 offset:18432\n"
"v_mfma_f32_16x16x32_bf16 v[24:27], a[80:83], a[48:51], v[24:27]\n"
"buffer_load_dwordx4 a[72:75], v199, s[36:39], s25 offen offset:0 sc1\n"
"v_mfma_f32_16x16x32_bf16 v[56:59], a[84:87], a[48:51], v[56:59]\n"
"ds_read_b128 a[24:27], v179 offset:16384\n"
"v_mfma_f32_16x16x32_bf16 v[60:63], a[84:87], a[52:55], v[60:63]\n"
"ds_read_b128 a[28:31], v179 offset:18432\n"
"v_mfma_f32_16x16x32_bf16 v[28:31], a[80:83], a[52:55], v[28:31]\n"
"buffer_load_dwordx4 v212, s[48:51], s24 offen offset:1024 lds\n"
"v_mfma_f32_16x16x32_bf16 v[32:35], a[80:83], a[56:59], v[32:35]\n"
"ds_read_b128 a[64:67], v192 offset:2048\n"
"v_mfma_f32_16x16x32_bf16 v[64:67], a[84:87], a[56:59], v[64:67]\n"
"v_exp_f32 v255, v255\n"
"v_mfma_f32_16x16x32_bf16 v[68:71], a[84:87], a[60:63], v[68:71]\n"
"v_mul_f32 v119, v119, v251\n"
"v_fma_f32 v252, v252, v215, v215\n"
"v_mfma_f32_16x16x32_bf16 v[36:39], a[80:83], a[60:63], v[36:39]\n"
"v_fma_f32 v253, v253, v215, v215\n"
"v_fma_f32 v254, v254, v215, v215\n"
"v_mfma_f32_16x16x32_bf16 v[72:75], a[68:71], a[32:35], v[72:75]\n"
"buffer_load_dwordx4 a[76:79], v200, s[36:39], s25 offen offset:0 sc1\n"
"v_fma_f32 v255, v255, v215, v215\n"
"v_mfma_f32_16x16x32_bf16 v[76:79], a[68:71], a[36:39], v[76:79]\n"
"v_rcp_f32 v252, v252\n"
"s_waitcnt vmcnt(28)\n"
"s_waitcnt lgkmcnt(0)\n"
"v_mfma_f32_16x16x32_bf16 v[8:11], a[88:91], a[0:3], v[8:11]\n"
"ds_read_b128 a[32:35], v180 offset:16384\n"
"v_mfma_f32_16x16x32_bf16 v[40:43], a[92:95], a[0:3], v[40:43]\n"
"ds_read_b128 a[36:39], v180 offset:18432\n"
"v_mfma_f32_16x16x32_bf16 v[44:47], a[92:95], a[4:7], v[44:47]\n"
"buffer_load_dwordx4 v216, s[48:51], s24 offen offset:2048 lds\n"
"v_mfma_f32_16x16x32_bf16 v[12:15], a[88:91], a[4:7], v[12:15]\n"
"ds_read_b128 a[40:43], v181 offset:16384\n"
"v_mfma_f32_16x16x32_bf16 v[16:19], a[88:91], a[8:11], v[16:19]\n"
"ds_read_b128 a[44:47], v181 offset:18432\n"
"v_mfma_f32_16x16x32_bf16 v[48:51], a[92:95], a[8:11], v[48:51]\n"
"v_rcp_f32 v253, v253\n"
"v_mfma_f32_16x16x32_bf16 v[52:55], a[92:95], a[12:15], v[52:55]\n"
"ds_read_b128 a[48:51], v182 offset:16384\n"
"v_mfma_f32_16x16x32_bf16 v[20:23], a[88:91], a[12:15], v[20:23]\n"
"ds_read_b128 a[52:55], v182 offset:18432\n"
"v_mfma_f32_16x16x32_bf16 v[24:27], a[88:91], a[16:19], v[24:27]\n"
"buffer_load_dwordx4 a[80:83], v199, s[36:39], s25 offen offset:1024 sc1\n"
"v_mfma_f32_16x16x32_bf16 v[56:59], a[92:95], a[16:19], v[56:59]\n"
"ds_read_b128 a[56:59], v183 offset:16384\n"
"v_mfma_f32_16x16x32_bf16 v[60:63], a[92:95], a[20:23], v[60:63]\n"
"ds_read_b128 a[60:63], v183 offset:18432\n"
"v_mfma_f32_16x16x32_bf16 v[28:31], a[88:91], a[20:23], v[28:31]\n"
"buffer_load_dwordx4 v220, s[48:51], s24 offen offset:3072 lds\n"
"s_add_u32 s24, s24, 128\n"
"v_mfma_f32_16x16x32_bf16 v[32:35], a[88:91], a[24:27], v[32:35]\n"
"ds_read_b128 a[68:71], v192 offset:3072\n"
"v_mfma_f32_16x16x32_bf16 v[64:67], a[92:95], a[24:27], v[64:67]\n"
"v_rcp_f32 v254, v254\n"
"v_mfma_f32_16x16x32_bf16 v[68:71], a[92:95], a[28:31], v[68:71]\n"
"s_add_u32 m0, s7, 69632\n"
"s_nop 0\n"
"buffer_load_dwordx4 v194, s[36:39], s26 offen sc1 lds\n"
"s_add_u32 s26, s26, 4096\n"
"v_mfma_f32_16x16x32_bf16 v[36:39], a[88:91], a[28:31], v[36:39]\n"
"v_rcp_f32 v255, v255\n"
"v_mfma_f32_16x16x32_bf16 v[72:75], a[64:67], a[0:3], v[72:75]\n"
"buffer_load_dwordx4 a[84:87], v200, s[36:39], s25 offen offset:1024 sc1\n"
"v_mul_f32 v148, v148, v252\n"
"v_mfma_f32_16x16x32_bf16 v[76:79], a[64:67], a[4:7], v[76:79]\n"
"v_mul_f32 v149, v149, v253\n"
"v_mul_f32 v150, v150, v254\n"
"s_waitcnt vmcnt(29)\n"
"s_waitcnt vmcnt(38) lgkmcnt(0)\n"
"s_barrier\n"
"v_mfma_f32_16x16x32_bf16 v[8:11], a[96:99], a[32:35], v[8:11]\n"
"ds_read_b128 a[0:3], v176 offset:36864\n"
"v_mul_f32 v151, v151, v255\n"
"v_mfma_f32_16x16x32_bf16 v[40:43], a[100:103], a[32:35], v[40:43]\n"
"ds_read_b128 a[4:7], v176 offset:38912\n"
"v_mfma_f32_16x16x32_bf16 v[44:47], a[100:103], a[36:39], v[44:47]\n"
"s_add_u32 m0, s6, 86016\n"
"s_nop 0\n"
"buffer_load_dwordx4 v208, s[48:51], s24 offen offset:0 lds\n"
"v_mfma_f32_16x16x32_bf16 v[12:15], a[96:99], a[36:39], v[12:15]\n"
"ds_read_b128 a[8:11], v177 offset:36864\n"
"v_mfma_f32_16x16x32_bf16 v[16:19], a[96:99], a[40:43], v[16:19]\n"
"ds_read_b128 a[12:15], v177 offset:38912\n"
"v_mfma_f32_16x16x32_bf16 v[48:51], a[100:103], a[40:43], v[48:51]\n"
"v_mfma_f32_16x16x32_bf16 v[52:55], a[100:103], a[44:47], v[52:55]\n"
"ds_read_b128 a[16:19], v178 offset:36864\n"
"v_mfma_f32_16x16x32_bf16 v[20:23], a[96:99], a[44:47], v[20:23]\n"
"ds_read_b128 a[20:23], v178 offset:38912\n"
"v_mfma_f32_16x16x32_bf16 v[24:27], a[96:99], a[48:51], v[24:27]\n"
"buffer_load_dwordx4 a[88:91], v199, s[36:39], s25 offen offset:2048 sc1\n"
"v_mfma_f32_16x16x32_bf16 v[56:59], a[100:103], a[48:51], v[56:59]\n"
"ds_read_b128 a[24:27], v179 offset:36864\n"
"v_mfma_f32_16x16x32_bf16 v[60:63], a[100:103], a[52:55], v[60:63]\n"
"ds_read_b128 a[28:31], v179 offset:38912\n"
"v_mfma_f32_16x16x32_bf16 v[28:31], a[96:99], a[52:55], v[28:31]\n"
"buffer_load_dwordx4 v212, s[48:51], s24 offen offset:1024 lds\n"
"v_mfma_f32_16x16x32_bf16 v[32:35], a[96:99], a[56:59], v[32:35]\n"
"ds_read_b128 a[64:67], v192 offset:36864\n"
"v_mfma_f32_16x16x32_bf16 v[64:67], a[100:103], a[56:59], v[64:67]\n"
"v_mfma_f32_16x16x32_bf16 v[68:71], a[100:103], a[60:63], v[68:71]\n"
"v_mfma_f32_16x16x32_bf16 v[36:39], a[96:99], a[60:63], v[36:39]\n"
"v_mfma_f32_16x16x32_bf16 v[72:75], a[68:71], a[32:35], v[72:75]\n"
"buffer_load_dwordx4 a[92:95], v200, s[36:39], s25 offen offset:2048 sc1\n"
"v_mfma_f32_16x16x32_bf16 v[76:79], a[68:71], a[36:39], v[76:79]\n"
"s_waitcnt vmcnt(26)\n"
"poll5c_%=:\n"
"v_xor_b32 v202, s68, v4\n"
"v_xor_b32 v203, s69, v5\n"
"v_or_b32 v201, v202, v203\n"
"v_xor_b32 v202, s68, v6\n"
"v_xor_b32 v203, s69, v7\n"
"v_or3_b32 v201, v201, v202, v203\n"
"v_cmp_ne_u32 vcc, 0, v201\n"
"s_cbranch_vccz poll5d_%=\n"
"s_sub_u32 s70, s70, 1\n"
"s_cbranch_scc1 poll5t_%=\n"
"s_sleep 2\n"
"buffer_load_dwordx4 v[4:7], v228, s[36:39], s16 offen sc1\n"
"s_waitcnt vmcnt(0)\n"
"s_branch poll5c_%=\n"
"poll5t_%=:\n"
"s_mov_b32 s70, 0\n"
"poll5d_%=:\n"
"s_waitcnt vmcnt(27)\n"
"s_waitcnt lgkmcnt(0)\n"
"v_mfma_f32_16x16x32_bf16 v[8:11], a[104:107], a[0:3], v[8:11]\n"
"ds_read_b128 a[32:35], v180 offset:36864\n"
"v_mfma_f32_16x16x32_bf16 v[40:43], a[108:111], a[0:3], v[40:43]\n"
"ds_read_b128 a[36:39], v180 offset:38912\n"
"v_mfma_f32_16x16x32_bf16 v[44:47], a[108:111], a[4:7], v[44:47]\n"
"buffer_load_dwordx4 v216, s[48:51], s24 offen offset:2048 lds\n"
"v_mfma_f32_16x16x32_bf16 v[12:15], a[104:107], a[4:7], v[12:15]\n"
"ds_read_b128 a[40:43], v181 offset:36864\n"
"v_mfma_f32_16x16x32_bf16 v[16:19], a[104:107], a[8:11], v[16:19]\n"
"ds_read_b128 a[44:47], v181 offset:38912\n"
"v_mfma_f32_16x16x32_bf16 v[48:51], a[108:111], a[8:11], v[48:51]\n"
"v_mfma_f32_16x16x32_bf16 v[52:55], a[108:111], a[12:15], v[52:55]\n"
"ds_read_b128 a[48:51], v182 offset:36864\n"
"v_mfma_f32_16x16x32_bf16 v[20:23], a[104:107], a[12:15], v[20:23]\n"
"ds_read_b128 a[52:55], v182 offset:38912\n"
"v_mfma_f32_16x16x32_bf16 v[24:27], a[104:107], a[16:19], v[24:27]\n"
"buffer_load_dwordx4 a[96:99], v199, s[36:39], s25 offen offset:3072 sc1\n"
"v_mfma_f32_16x16x32_bf16 v[56:59], a[108:111], a[16:19], v[56:59]\n"
"ds_read_b128 a[56:59], v183 offset:36864\n"
"v_mfma_f32_16x16x32_bf16 v[60:63], a[108:111], a[20:23], v[60:63]\n"
"ds_read_b128 a[60:63], v183 offset:38912\n"
"v_mfma_f32_16x16x32_bf16 v[28:31], a[104:107], a[20:23], v[28:31]\n"
"buffer_load_dwordx4 v220, s[48:51], s24 offen offset:3072 lds\n"
"s_add_u32 s24, s24, 128\n"
"v_mfma_f32_16x16x32_bf16 v[32:35], a[104:107], a[24:27], v[32:35]\n"
"ds_read_b128 a[68:71], v192 offset:37888\n"
"v_mfma_f32_16x16x32_bf16 v[64:67], a[108:111], a[24:27], v[64:67]\n"
"v_mfma_f32_16x16x32_bf16 v[68:71], a[108:111], a[28:31], v[68:71]\n"
"v_mfma_f32_16x16x32_bf16 v[36:39], a[104:107], a[28:31], v[36:39]\n"
"v_mfma_f32_16x16x32_bf16 v[72:75], a[64:67], a[0:3], v[72:75]\n"
"buffer_load_dwordx4 a[100:103], v200, s[36:39], s25 offen offset:3072 sc1\n"
"s_add_u32 s25, s25, 4096\n"
"v_mfma_f32_16x16x32_bf16 v[76:79], a[64:67], a[4:7], v[76:79]\n"
"s_waitcnt vmcnt(25)\n"
"s_waitcnt lgkmcnt(0)\n"
"v_mfma_f32_16x16x32_bf16 v[8:11], a[112:115], a[32:35], v[8:11]\n"
"ds_read_b128 a[0:3], v176 offset:53248\n"
"v_mfma_f32_16x16x32_bf16 v[40:43], a[116:119], a[32:35], v[40:43]\n"
"ds_read_b128 a[4:7], v176 offset:55296\n"
"v_mfma_f32_16x16x32_bf16 v[44:47], a[116:119], a[36:39], v[44:47]\n"
"s_add_u32 m0, s6, 102400\n"
"s_nop 0\n"
"buffer_load_dwordx4 v208, s[48:51], s24 offen offset:0 lds\n"
"v_mfma_f32_16x16x32_bf16 v[12:15], a[112:115], a[36:39], v[12:15]\n"
"ds_read_b128 a[8:11], v177 offset:53248\n"
"v_mfma_f32_16x16x32_bf16 v[16:19], a[112:115], a[40:43], v[16:19]\n"
"ds_read_b128 a[12:15], v177 offset:55296\n"
"v_mfma_f32_16x16x32_bf16 v[48:51], a[116:119], a[40:43], v[48:51]\n"
"v_mfma_f32_16x16x32_bf16 v[52:55], a[116:119], a[44:47], v[52:55]\n"
"ds_read_b128 a[16:19], v178 offset:53248\n"
"v_mfma_f32_16x16x32_bf16 v[20:23], a[112:115], a[44:47], v[20:23]\n"
"ds_read_b128 a[20:23], v178 offset:55296\n"
"v_mfma_f32_16x16x32_bf16 v[24:27], a[112:115], a[48:51], v[24:27]\n"
"buffer_load_dwordx4 a[104:107], v199, s[36:39], s25 offen offset:0 sc1\n"
"v_mfma_f32_16x16x32_bf16 v[56:59], a[116:119], a[48:51], v[56:59]\n"
"ds_read_b128 a[24:27], v179 offset:53248\n"
"v_mfma_f32_16x16x32_bf16 v[60:63], a[116:119], a[52:55], v[60:63]\n"
"ds_read_b128 a[28:31], v179 offset:55296\n"
"v_mfma_f32_16x16x32_bf16 v[28:31], a[112:115], a[52:55], v[28:31]\n"
"buffer_load_dwordx4 v212, s[48:51], s24 offen offset:1024 lds\n"
"v_mfma_f32_16x16x32_bf16 v[32:35], a[112:115], a[56:59], v[32:35]\n"
"ds_read_b128 a[64:67], v192 offset:38912\n"
"v_mfma_f32_16x16x32_bf16 v[64:67], a[116:119], a[56:59], v[64:67]\n"
"v_mfma_f32_16x16x32_bf16 v[68:71], a[116:119], a[60:63], v[68:71]\n"
"v_mfma_f32_16x16x32_bf16 v[36:39], a[112:115], a[60:63], v[36:39]\n"
"v_mfma_f32_16x16x32_bf16 v[72:75], a[68:71], a[32:35], v[72:75]\n"
"buffer_load_dwordx4 a[108:111], v200, s[36:39], s25 offen offset:0 sc1\n"
"v_mfma_f32_16x16x32_bf16 v[76:79], a[68:71], a[36:39], v[76:79]\n"
"s_waitcnt vmcnt(25)\n"
"s_waitcnt lgkmcnt(0)\n"
"v_mfma_f32_16x16x32_bf16 v[8:11], a[120:123], a[0:3], v[8:11]\n"
"ds_read_b128 a[32:35], v180 offset:53248\n"
"v_mfma_f32_16x16x32_bf16 v[40:43], a[124:127], a[0:3], v[40:43]\n"
"ds_read_b128 a[36:39], v180 offset:55296\n"
"v_mfma_f32_16x16x32_bf16 v[44:47], a[124:127], a[4:7], v[44:47]\n"
"buffer_load_dwordx4 v216, s[48:51], s24 offen offset:2048 lds\n"
"v_mfma_f32_16x16x32_bf16 v[12:15], a[120:123], a[4:7], v[12:15]\n"
"ds_read_b128 a[40:43], v181 offset:53248\n"
"v_mfma_f32_16x16x32_bf16 v[16:19], a[120:123], a[8:11], v[16:19]\n"
"ds_read_b128 a[44:47], v181 offset:55296\n"
"v_mfma_f32_16x16x32_bf16 v[48:51], a[124:127], a[8:11], v[48:51]\n"
"v_mfma_f32_16x16x32_bf16 v[52:55], a[124:127], a[12:15], v[52:55]\n"
"ds_read_b128 a[48:51], v182 offset:53248\n"
"v_mfma_f32_16x16x32_bf16 v[20:23], a[120:123], a[12:15], v[20:23]\n"
"ds_read_b128 a[52:55], v182 offset:55296\n"
"v_mfma_f32_16x16x32_bf16 v[24:27], a[120:123], a[16:19], v[24:27]\n"
"buffer_load_dwordx4 a[112:115], v199, s[36:39], s25 offen offset:1024 sc1\n"
"v_mfma_f32_16x16x32_bf16 v[56:59], a[124:127], a[16:19], v[56:59]\n"
"ds_read_b128 a[56:59], v183 offset:53248\n"
"v_mfma_f32_16x16x32_bf16 v[60:63], a[124:127], a[20:23], v[60:63]\n"
"ds_read_b128 a[60:63], v183 offset:55296\n"
"v_mfma_f32_16x16x32_bf16 v[28:31], a[120:123], a[20:23], v[28:31]\n"
"buffer_load_dwordx4 v220, s[48:51], s24 offen offset:3072 lds\n"
"s_add_u32 s24, s24, 128\n"
"v_mfma_f32_16x16x32_bf16 v[32:35], a[120:123], a[24:27], v[32:35]\n"
"ds_read_b128 a[68:71], v192 offset:39936\n"
"v_mfma_f32_16x16x32_bf16 v[64:67], a[124:127], a[24:27], v[64:67]\n"
"v_mfma_f32_16x16x32_bf16 v[68:71], a[124:127], a[28:31], v[68:71]\n"
"s_add_u32 m0, s7, 118784\n"
"s_nop 0\n"
"buffer_load_dwordx4 v194, s[36:39], s26 offen sc1 lds\n"
"s_add_u32 s26, s26, 4096\n"
"v_mfma_f32_16x16x32_bf16 v[36:39], a[120:123], a[28:31], v[36:39]\n"
"v_mfma_f32_16x16x32_bf16 v[72:75], a[64:67], a[0:3], v[72:75]\n"
"buffer_load_dwordx4 a[116:119], v200, s[36:39], s25 offen offset:1024 sc1\n"
"v_mfma_f32_16x16x32_bf16 v[76:79], a[64:67], a[4:7], v[76:79]\n"
"s_waitcnt vmcnt(26)\n"
"s_waitcnt vmcnt(35) lgkmcnt(0)\n"
"s_barrier\n"
"v_mfma_f32_16x16x32_bf16 v[8:11], a[128:131], a[32:35], v[8:11]\n"
"ds_read_b128 a[0:3], v184 offset:0\n"
"v_mfma_f32_16x16x32_bf16 v[40:43], a[132:135], a[32:35], v[40:43]\n"
"ds_read_b128 a[4:7], v184 offset:2048\n"
"v_mfma_f32_16x16x32_bf16 v[44:47], a[132:135], a[36:39], v[44:47]\n"
"s_add_u32 m0, s6, 122880\n"
"s_nop 0\n"
"buffer_load_dwordx4 v208, s[48:51], s24 offen offset:0 lds\n"
"v_mfma_f32_16x16x32_bf16 v[12:15], a[128:131], a[36:39], v[12:15]\n"
"ds_read_b128 a[8:11], v185 offset:0\n"
"v_mfma_f32_16x16x32_bf16 v[16:19], a[128:131], a[40:43], v[16:19]\n"
"ds_read_b128 a[12:15], v185 offset:2048\n"
"v_mfma_f32_16x16x32_bf16 v[48:51], a[132:135], a[40:43], v[48:51]\n"
"v_mfma_f32_16x16x32_bf16 v[52:55], a[132:135], a[44:47], v[52:55]\n"
"ds_read_b128 a[16:19], v186 offset:0\n"
"v_mfma_f32_16x16x32_bf16 v[20:23], a[128:131], a[44:47], v[20:23]\n"
"ds_read_b128 a[20:23], v186 offset:2048\n"
"v_mfma_f32_16x16x32_bf16 v[24:27], a[128:131], a[48:51], v[24:27]\n"
"buffer_load_dwordx4 a[120:123], v199, s[36:39], s25 offen offset:2048 sc1\n"
"v_mfma_f32_16x16x32_bf16 v[56:59], a[132:135], a[48:51], v[56:59]\n"
"ds_read_b128 a[24:27], v187 offset:0\n"
"v_mfma_f32_16x16x32_bf16 v[60:63], a[132:135], a[52:55], v[60:63]\n"
"ds_read_b128 a[28:31], v187 offset:2048\n"
"v_mfma_f32_16x16x32_bf16 v[28:31], a[128:131], a[52:55], v[28:31]\n"
"buffer_load_dwordx4 v212, s[48:51], s24 offen offset:1024 lds\n"
"v_mfma_f32_16x16x32_bf16 v[32:35], a[128:131], a[56:59], v[32:35]\n"
"ds_read_b128 a[64:67], v193 offset:0\n"
"v_mfma_f32_16x16x32_bf16 v[64:67], a[132:135], a[56:59], v[64:67]\n"
"v_mfma_f32_16x16x32_bf16 v[68:71], a[132:135], a[60:63], v[68:71]\n"
"v_mfma_f32_16x16x32_bf16 v[36:39], a[128:131], a[60:63], v[36:39]\n"
"v_mfma_f32_16x16x32_bf16 v[72:75], a[68:71], a[32:35], v[72:75]\n"
"buffer_load_dwordx4 a[124:127], v200, s[36:39], s25 offen offset:2048 sc1\n"
"v_mfma_f32_16x16x32_bf16 v[76:79], a[68:71], a[36:39], v[76:79]\n"
"s_waitcnt vmcnt(26)\n"
"s_waitcnt lgkmcnt(0)\n"
"v_mfma_f32_16x16x32_bf16 v[8:11], a[72:75], a[0:3], v[8:11]\n"
"ds_read_b128 a[32:35], v188 offset:0\n"
"v_mfma_f32_16x16x32_bf16 v[40:43], a[76:79], a[0:3], v[40:43]\n"
"ds_read_b128 a[36:39], v188 offset:2048\n"
"v_mfma_f32_16x16x32_bf16 v[44:47], a[76:79], a[4:7], v[44:47]\n"
"buffer_load_dwordx4 v216, s[48:51], s24 offen offset:2048 lds\n"
"v_mfma_f32_16x16x32_bf16 v[12:15], a[72:75], a[4:7], v[12:15]\n"
"ds_read_b128 a[40:43], v189 offset:0\n"
"v_mfma_f32_16x16x32_bf16 v[16:19], a[72:75], a[8:11], v[16:19]\n"
"ds_read_b128 a[44:47], v189 offset:2048\n"
"v_mfma_f32_16x16x32_bf16 v[48:51], a[76:79], a[8:11], v[48:51]\n"
"s_getpc_b64 s[76:77]\n"
"ct2_%=:\n"
"s_and_b32 s76, s76, 0xffffff80\n"
"v_mfma_f32_16x16x32_bf16 v[52:55], a[76:79], a[12:15], v[52:55]\n"
"ds_read_b128 a[48:51], v190 offset:0\n"
"s_lshr_b32 s71, s64, 3\n"
"v_mfma_f32_16x16x32_bf16 v[20:23], a[72:75], a[12:15], v[20:23]\n"
"ds_read_b128 a[52:55], v190 offset:2048\n"
"s_lshl_b32 s71, s71, 10\n"
"v_mfma_f32_16x16x32_bf16 v[24:27], a[72:75], a[16:19], v[24:27]\n"
"buffer_load_dwordx4 a[128:131], v199, s[36:39], s25 offen offset:3072 sc1\n"
"s_lshl_b32 s72, s65, 8\n"
"v_mfma_f32_16x16x32_bf16 v[56:59], a[76:79], a[16:19], v[56:59]\n"
"ds_read_b128 a[56:59], v191 offset:0\n"
"s_add_u32 s71, s71, s72\n"
"v_mfma_f32_16x16x32_bf16 v[60:63], a[76:79], a[20:23], v[60:63]\n"
"ds_read_b128 a[60:63], v191 offset:2048\n"
"s_add_u32 s71, s71, 6144\n"
"v_mfma_f32_16x16x32_bf16 v[28:31], a[72:75], a[20:23], v[28:31]\n"
"buffer_load_dwordx4 v220, s[48:51], s24 offen offset:3072 lds\n"
"s_add_u32 s24, s24, 128\n"
"v_mfma_f32_16x16x32_bf16 v[32:35], a[72:75], a[24:27], v[32:35]\n"
"ds_read_b128 a[68:71], v193 offset:1024\n"
"v_lshrrev_b32 v2, 5, v1\n"
"v_mfma_f32_16x16x32_bf16 v[64:67], a[76:79], a[24:27], v[64:67]\n"
"v_lshl_add_u32 v2, v2, 7, s71\n"
"s_mov_b32 s72, kend_%=-ct2_%=-4\n"
"v_mfma_f32_16x16x32_bf16 v[68:71], a[76:79], a[28:31], v[68:71]\n"
"v_min_u32 v2, s72, v2\n"
"global_load_dword v201, v2, s[76:77]\n"
"v_mfma_f32_16x16x32_bf16 v[36:39], a[72:75], a[28:31], v[36:39]\n"
"v_mfma_f32_16x16x32_bf16 v[72:75], a[64:67], a[0:3], v[72:75]\n"
"buffer_load_dwordx4 a[132:135], v200, s[36:39], s25 offen offset:3072 sc1\n"
"s_add_u32 s25, s25, 4096\n"
"v_mfma_f32_16x16x32_bf16 v[76:79], a[64:67], a[4:7], v[76:79]\n"
"s_waitcnt vmcnt(26)\n"
"s_waitcnt lgkmcnt(0)\n"
"v_mfma_f32_16x16x32_bf16 v[8:11], a[80:83], a[32:35], v[8:11]\n"
"ds_read_b128 a[0:3], v184 offset:16384\n"
"v_mfma_f32_16x16x32_bf16 v[40:43], a[84:87], a[32:35], v[40:43]\n"
"ds_read_b128 a[4:7], v184 offset:18432\n"
"v_mfma_f32_16x16x32_bf16 v[44:47], a[84:87], a[36:39], v[44:47]\n"
"s_add_u32 m0, s6, 139264\n"
"s_nop 0\n"
"buffer_load_dwordx4 v208, s[48:51], s24 offen offset:0 lds\n"
"v_mfma_f32_16x16x32_bf16 v[12:15], a[80:83], a[36:39], v[12:15]\n"
"ds_read_b128 a[8:11], v185 offset:16384\n"
"v_mfma_f32_16x16x32_bf16 v[16:19], a[80:83], a[40:43], v[16:19]\n"
"ds_read_b128 a[12:15], v185 offset:18432\n"
"v_mfma_f32_16x16x32_bf16 v[48:51], a[84:87], a[40:43], v[48:51]\n"
"v_mfma_f32_16x16x32_bf16 v[52:55], a[84:87], a[44:47], v[52:55]\n"
"ds_read_b128 a[16:19], v186 offset:16384\n"
"v_mfma_f32_16x16x32_bf16 v[20:23], a[80:83], a[44:47], v[20:23]\n"
"ds_read_b128 a[20:23], v186 offset:18432\n"
"v_mfma_f32_16x16x32_bf16 v[24:27], a[80:83], a[48:51], v[24:27]\n"
"buffer_load_dwordx4 a[72:75], v199, s[36:39], s25 offen offset:0 sc1\n"
"v_mfma_f32_16x16x32_bf16 v[56:59], a[84:87], a[48:51], v[56:59]\n"
"ds_read_b128 a[24:27], v187 offset:16384\n"
"v_mfma_f32_16x16x32_bf16 v[60:63], a[84:87], a[52:55], v[60:63]\n"
"ds_read_b128 a[28:31], v187 offset:18432\n"
"v_mfma_f32_16x16x32_bf16 v[28:31], a[80:83], a[52:55], v[28:31]\n"
"buffer_load_dwordx4 v212, s[48:51], s24 offen offset:1024 lds\n"
"v_mfma_f32_16x16x32_bf16 v[32:35], a[80:83], a[56:59], v[32:35]\n"
"ds_read_b128 a[64:67], v193 offset:2048\n"
"v_mfma_f32_16x16x32_bf16 v[64:67], a[84:87], a[56:59], v[64:67]\n"
"v_mfma_f32_16x16x32_bf16 v[68:71], a[84:87], a[60:63], v[68:71]\n"
"v_mfma_f32_16x16x32_bf16 v[36:39], a[80:83], a[60:63], v[36:39]\n"
"v_mfma_f32_16x16x32_bf16 v[72:75], a[68:71], a[32:35], v[72:75]\n"
"buffer_load_dwordx4 a[76:79], v200, s[36:39], s25 offen offset:0 sc1\n"
"v_mfma_f32_16x16x32_bf16 v[76:79], a[68:71], a[36:39], v[76:79]\n"
"s_waitcnt vmcnt(26)\n"
"s_waitcnt lgkmcnt(0)\n"
"v_mfma_f32_16x16x32_bf16 v[8:11], a[88:91], a[0:3], v[8:11]\n"
"ds_read_b128 a[32:35], v188 offset:16384\n"
"v_mfma_f32_16x16x32_bf16 v[40:43], a[92:95], a[0:3], v[40:43]\n"
"ds_read_b128 a[36:39], v188 offset:18432\n"
"v_mfma_f32_16x16x32_bf16 v[44:47], a[92:95], a[4:7], v[44:47]\n"
"buffer_load_dwordx4 v216, s[48:51], s24 offen offset:2048 lds\n"
"v_mfma_f32_16x16x32_bf16 v[12:15], a[88:91], a[4:7], v[12:15]\n"
"ds_read_b128 a[40:43], v189 offset:16384\n"
"v_mfma_f32_16x16x32_bf16 v[16:19], a[88:91], a[8:11], v[16:19]\n"
"ds_read_b128 a[44:47], v189 offset:18432\n"
"v_mfma_f32_16x16x32_bf16 v[48:51], a[92:95], a[8:11], v[48:51]\n"
"v_mfma_f32_16x16x32_bf16 v[52:55], a[92:95], a[12:15], v[52:55]\n"
"ds_read_b128 a[48:51], v190 offset:16384\n"
"v_mfma_f32_16x16x32_bf16 v[20:23], a[88:91], a[12:15], v[20:23]\n"
"ds_read_b128 a[52:55], v190 offset:18432\n"
"v_mfma_f32_16x16x32_bf16 v[24:27], a[88:91], a[16:19], v[24:27]\n"
"buffer_load_dwordx4 a[80:83], v199, s[36:39], s25 offen offset:1024 sc1\n"
"v_mfma_f32_16x16x32_bf16 v[56:59], a[92:95], a[16:19], v[56:59]\n"
"ds_read_b128 a[56:59], v191 offset:16384\n"
"v_mfma_f32_16x16x32_bf16 v[60:63], a[92:95], a[20:23], v[60:63]\n"
"ds_read_b128 a[60:63], v191 offset:18432\n"
"v_mfma_f32_16x16x32_bf16 v[28:31], a[88:91], a[20:23], v[28:31]\n"
"buffer_load_dwordx4 v220, s[48:51], s24 offen offset:3072 lds\n"
"s_add_u32 s24, s24, 128\n"
"v_mfma_f32_16x16x32_bf16 v[32:35], a[88:91], a[24:27], v[32:35]\n"
"ds_read_b128 a[68:71], v193 offset:3072\n"
"v_mfma_f32_16x16x32_bf16 v[64:67], a[92:95], a[24:27], v[64:67]\n"
"v_mfma_f32_16x16x32_bf16 v[68:71], a[92:95], a[28:31], v[68:71]\n"
"s_add_u32 m0, s7, 155648\n"
"s_nop 0\n"
"buffer_load_dwordx4 v194, s[36:39], s26 offen sc1 lds\n"
"s_add_u32 s26, s26, 4096\n"
"v_mfma_f32_16x16x32_bf16 v[36:39], a[88:91], a[28:31], v[36:39]\n"
"v_mfma_f32_16x16x32_bf16 v[72:75], a[64:67], a[0:3], v[72:75]\n"
"buffer_load_dwordx4 a[84:87], v200, s[36:39], s25 offen offset:1024 sc1\n"
"v_mfma_f32_16x16x32_bf16 v[76:79], a[64:67], a[4:7], v[76:79]\n"
"s_waitcnt vmcnt(27)\n"
"s_waitcnt vmcnt(36) lgkmcnt(0)\n"
"s_barrier\n"
"v_mfma_f32_16x16x32_bf16 v[8:11], a[96:99], a[32:35], v[8:11]\n"
"ds_read_b128 a[0:3], v184 offset:36864\n"
"v_mfma_f32_16x16x32_bf16 v[40:43], a[100:103], a[32:35], v[40:43]\n"
"ds_read_b128 a[4:7], v184 offset:38912\n"
"v_mfma_f32_16x16x32_bf16 v[44:47], a[100:103], a[36:39], v[44:47]\n"
"s_add_u32 m0, s6, 0\n"
"s_nop 0\n"
"buffer_load_dwordx4 v208, s[48:51], s24 offen offset:0 lds\n"
"v_mfma_f32_16x16x32_bf16 v[12:15], a[96:99], a[36:39], v[12:15]\n"
"ds_read_b128 a[8:11], v185 offset:36864\n"
"v_mfma_f32_16x16x32_bf16 v[16:19], a[96:99], a[40:43], v[16:19]\n"
"ds_read_b128 a[12:15], v185 offset:38912\n"
"v_mfma_f32_16x16x32_bf16 v[48:51], a[100:103], a[40:43], v[48:51]\n"
"v_mfma_f32_16x16x32_bf16 v[52:55], a[100:103], a[44:47], v[52:55]\n"
"ds_read_b128 a[16:19], v186 offset:36864\n"
"v_mfma_f32_16x16x32_bf16 v[20:23], a[96:99], a[44:47], v[20:23]\n"
"ds_read_b128 a[20:23], v186 offset:38912\n"
"v_mfma_f32_16x16x32_bf16 v[24:27], a[96:99], a[48:51], v[24:27]\n"
"buffer_load_dwordx4 a[88:91], v199, s[36:39], s25 offen offset:2048 sc1\n"
"v_mfma_f32_16x16x32_bf16 v[56:59], a[100:103], a[48:51], v[56:59]\n"
"ds_read_b128 a[24:27], v187 offset:36864\n"
"v_mfma_f32_16x16x32_bf16 v[60:63], a[100:103], a[52:55], v[60:63]\n"
"ds_read_b128 a[28:31], v187 offset:38912\n"
"v_mfma_f32_16x16x32_bf16 v[28:31], a[96:99], a[52:55], v[28:31]\n"
"buffer_load_dwordx4 v212, s[48:51], s24 offen offset:1024 lds\n"
"v_mfma_f32_16x16x32_bf16 v[32:35], a[96:99], a[56:59], v[32:35]\n"
"ds_read_b128 a[64:67], v193 offset:36864\n"
"v_mfma_f32_16x16x32_bf16 v[64:67], a[100:103], a[56:59], v[64:67]\n"
"v_mfma_f32_16x16x32_bf16 v[68:71], a[100:103], a[60:63], v[68:71]\n"
"v_mfma_f32_16x16x32_bf16 v[36:39], a[96:99], a[60:63], v[36:39]\n"
"v_mfma_f32_16x16x32_bf16 v[72:75], a[68:71], a[32:35], v[72:75]\n"
"buffer_load_dwordx4 a[92:95], v200, s[36:39], s25 offen offset:2048 sc1\n"
"v_mfma_f32_16x16x32_bf16 v[76:79], a[68:71], a[36:39], v[76:79]\n"
"s_waitcnt vmcnt(27)\n"
"s_waitcnt lgkmcnt(0)\n"
"v_mfma_f32_16x16x32_bf16 v[8:11], a[104:107], a[0:3], v[8:11]\n"
"ds_read_b128 a[32:35], v188 offset:36864\n"
"v_mfma_f32_16x16x32_bf16 v[40:43], a[108:111], a[0:3], v[40:43]\n"
"ds_read_b128 a[36:39], v188 offset:38912\n"
"v_mfma_f32_16x16x32_bf16 v[44:47], a[108:111], a[4:7], v[44:47]\n"
"buffer_load_dwordx4 v216, s[48:51], s24 offen offset:2048 lds\n"
"v_mfma_f32_16x16x32_bf16 v[12:15], a[104:107], a[4:7], v[12:15]\n"
"ds_read_b128 a[40:43], v189 offset:36864\n"
"v_mfma_f32_16x16x32_bf16 v[16:19], a[104:107], a[8:11], v[16:19]\n"
"ds_read_b128 a[44:47], v189 offset:38912\n"
"v_mfma_f32_16x16x32_bf16 v[48:51], a[108:111], a[8:11], v[48:51]\n"
"v_mfma_f32_16x16x32_bf16 v[52:55], a[108:111], a[12:15], v[52:55]\n"
"ds_read_b128 a[48:51], v190 offset:36864\n"
"v_mfma_f32_16x16x32_bf16 v[20:23], a[104:107], a[12:15], v[20:23]\n"
"ds_read_b128 a[52:55], v190 offset:38912\n"
"v_mfma_f32_16x16x32_bf16 v[24:27], a[104:107], a[16:19], v[24:27]\n"
"buffer_load_dwordx4 a[96:99], v199, s[36:39], s25 offen offset:3072 sc1\n"
"v_mfma_f32_16x16x32_bf16 v[56:59], a[108:111], a[16:19], v[56:59]\n"
"ds_read_b128 a[56:59], v191 offset:36864\n"
"v_mfma_f32_16x16x32_bf16 v[60:63], a[108:111], a[20:23], v[60:63]\n"
"ds_read_b128 a[60:63], v191 offset:38912\n"
"v_mfma_f32_16x16x32_bf16 v[28:31], a[104:107], a[20:23], v[28:31]\n"
"buffer_load_dwordx4 v220, s[48:51], s24 offen offset:3072 lds\n"
"s_add_u32 s24, s24, 128\n"
"v_mfma_f32_16x16x32_bf16 v[32:35], a[104:107], a[24:27], v[32:35]\n"
"ds_read_b128 a[68:71], v193 offset:37888\n"
"v_mfma_f32_16x16x32_bf16 v[64:67], a[108:111], a[24:27], v[64:67]\n"
"v_mfma_f32_16x16x32_bf16 v[68:71], a[108:111], a[28:31], v[68:71]\n"
"v_mfma_f32_16x16x32_bf16 v[36:39], a[104:107], a[28:31], v[36:39]\n"
"v_mfma_f32_16x16x32_bf16 v[72:75], a[64:67], a[0:3], v[72:75]\n"
"buffer_load_dwordx4 a[100:103], v200, s[36:39], s25 offen offset:3072 sc1\n"
"s_add_u32 s25, s25, 4096\n"
"v_mfma_f32_16x16x32_bf16 v[76:79], a[64:67], a[4:7], v[76:79]\n"
"s_waitcnt vmcnt(26)\n"
"s_waitcnt lgkmcnt(0)\n"
"v_mfma_f32_16x16x32_bf16 v[8:11], a[112:115], a[32:35], v[8:11]\n"
"ds_read_b128 a[0:3], v184 offset:53248\n"
"v_mfma_f32_16x16x32_bf16 v[40:43], a[116:119], a[32:35], v[40:43]\n"
"ds_read_b128 a[4:7], v184 offset:55296\n"
"v_mfma_f32_16x16x32_bf16 v[44:47], a[116:119], a[36:39], v[44:47]\n"
"s_add_u32 m0, s6, 16384\n"
"s_nop 0\n"
"buffer_load_dwordx4 v208, s[48:51], s24 offen offset:0 lds\n"
"v_mfma_f32_16x16x32_bf16 v[12:15], a[112:115], a[36:39], v[12:15]\n"
"ds_read_b128 a[8:11], v185 offset:53248\n"
"v_mfma_f32_16x16x32_bf16 v[16:19], a[112:115], a[40:43], v[16:19]\n"
"ds_read_b128 a[12:15], v185 offset:55296\n"
"v_mfma_f32_16x16x32_bf16 v[48:51], a[116:119], a[40:43], v[48:51]\n"
"v_mfma_f32_16x16x32_bf16 v[52:55], a[116:119], a[44:47], v[52:55]\n"
"ds_read_b128 a[16:19], v186 offset:53248\n"
"v_mfma_f32_16x16x32_bf16 v[20:23], a[112:115], a[44:47], v[20:23]\n"
"ds_read_b128 a[20:23], v186 offset:55296\n"
"v_mfma_f32_16x16x32_bf16 v[24:27], a[112:115], a[48:51], v[24:27]\n"
"buffer_load_dwordx4 a[104:107], v199, s[36:39], s25 offen offset:0 sc1\n"
"v_mfma_f32_16x16x32_bf16 v[56:59], a[116:119], a[48:51], v[56:59]\n"
"ds_read_b128 a[24:27], v187 offset:53248\n"
"v_mfma_f32_16x16x32_bf16 v[60:63], a[116:119], a[52:55], v[60:63]\n"
"ds_read_b128 a[28:31], v187 offset:55296\n"
"v_mfma_f32_16x16x32_bf16 v[28:31], a[112:115], a[52:55], v[28:31]\n"
"buffer_load_dwordx4 v212, s[48:51], s24 offen offset:1024 lds\n"
"v_mfma_f32_16x16x32_bf16 v[32:35], a[112:115], a[56:59], v[32:35]\n"
"ds_read_b128 a[64:67], v193 offset:38912\n"
"v_mfma_f32_16x16x32_bf16 v[64:67], a[116:119], a[56:59], v[64:67]\n"
"v_mfma_f32_16x16x32_bf16 v[68:71], a[116:119], a[60:63], v[68:71]\n"
"v_mfma_f32_16x16x32_bf16 v[36:39], a[112:115], a[60:63], v[36:39]\n"
"v_mfma_f32_16x16x32_bf16 v[72:75], a[68:71], a[32:35], v[72:75]\n"
"buffer_load_dwordx4 a[108:111], v200, s[36:39], s25 offen offset:0 sc1\n"
"v_mfma_f32_16x16x32_bf16 v[76:79], a[68:71], a[36:39], v[76:79]\n"
"s_waitcnt vmcnt(26)\n"
"s_waitcnt lgkmcnt(0)\n"
"v_mfma_f32_16x16x32_bf16 v[8:11], a[120:123], a[0:3], v[8:11]\n"
"ds_read_b128 a[32:35], v188 offset:53248\n"
"s_mul_i32 s75, s65, 9216\n"
"v_mfma_f32_16x16x32_bf16 v[40:43], a[124:127], a[0:3], v[40:43]\n"
"ds_read_b128 a[36:39], v188 offset:55296\n"
"s_sub_u32 s41, s75, 4096\n"
"v_mfma_f32_16x16x32_bf16 v[44:47], a[124:127], a[4:7], v[44:47]\n"
"buffer_load_dwordx4 v216, s[48:51], s24 offen offset:2048 lds\n"
"s_mul_i32 s75, s65, 7168\n"
"v_mfma_f32_16x16x32_bf16 v[12:15], a[120:123], a[4:7], v[12:15]\n"
"ds_read_b128 a[40:43], v189 offset:53248\n"
"s_add_u32 s42, s75, 128000\n"
"v_mfma_f32_16x16x32_bf16 v[16:19], a[120:123], a[8:11], v[16:19]\n"
"ds_read_b128 a[44:47], v189 offset:55296\n"
"s_mov_b32 s40, 7405568\n"
"v_mfma_f32_16x16x32_bf16 v[48:51], a[124:127], a[8:11], v[48:51]\n"
"v_mfma_f32_16x16x32_bf16 v[52:55], a[124:127], a[12:15], v[52:55]\n"
"ds_read_b128 a[48:51], v190 offset:53248\n"
"v_mfma_f32_16x16x32_bf16 v[20:23], a[120:123], a[12:15], v[20:23]\n"
"ds_read_b128 a[52:55], v190 offset:55296\n"
"v_mfma_f32_16x16x32_bf16 v[24:27], a[120:123], a[16:19], v[24:27]\n"
"buffer_load_dwordx4 a[112:115], v199, s[36:39], s25 offen offset:1024 sc1\n"
"v_mfma_f32_16x16x32_bf16 v[56:59], a[124:127], a[16:19], v[56:59]\n"
"ds_read_b128 a[56:59], v191 offset:53248\n"
"v_mfma_f32_16x16x32_bf16 v[60:63], a[124:127], a[20:23], v[60:63]\n"
"ds_read_b128 a[60:63], v191 offset:55296\n"
"v_mfma_f32_16x16x32_bf16 v[28:31], a[120:123], a[20:23], v[28:31]\n"
"buffer_load_dwordx4 v220, s[48:51], s24 offen offset:3072 lds\n"
"s_add_u32 s24, s24, 128\n"
"v_mfma_f32_16x16x32_bf16 v[32:35], a[120:123], a[24:27], v[32:35]\n"
"ds_read_b128 a[68:71], v193 offset:39936\n"
"v_mfma_f32_16x16x32_bf16 v[64:67], a[124:127], a[24:27], v[64:67]\n"
"v_mfma_f32_16x16x32_bf16 v[68:71], a[124:127], a[28:31], v[68:71]\n"
"s_add_u32 m0, s7, 32768\n"
"s_nop 0\n"
"buffer_load_dwordx4 v194, s[36:39], s26 offen sc1 lds\n"
"s_add_u32 s26, s26, 4096\n"
"v_mfma_f32_16x16x32_bf16 v[36:39], a[120:123], a[28:31], v[36:39]\n"
"v_mfma_f32_16x16x32_bf16 v[72:75], a[64:67], a[0:3], v[72:75]\n"
"buffer_load_dwordx4 a[116:119], v200, s[36:39], s25 offen offset:1024 sc1\n"
"v_mfma_f32_16x16x32_bf16 v[76:79], a[64:67], a[4:7], v[76:79]\n"
"s_waitcnt vmcnt(26)\n"
"s_waitcnt vmcnt(36) lgkmcnt(0)\n"
"s_barrier\n"
"v_mfma_f32_16x16x32_bf16 v[8:11], a[128:131], a[32:35], v[8:11]\n"
"ds_read_b128 a[0:3], v176 offset:0\n"
"v_mfma_f32_16x16x32_bf16 v[40:43], a[132:135], a[32:35], v[40:43]\n"
"ds_read_b128 a[4:7], v176 offset:2048\n"
"v_mfma_f32_16x16x32_bf16 v[44:47], a[132:135], a[36:39], v[44:47]\n"
"s_add_u32 m0, s6, 36864\n"
"s_nop 0\n"
"buffer_load_dwordx4 v208, s[48:51], s24 offen offset:0 lds\n"
"v_mfma_f32_16x16x32_bf16 v[12:15], a[128:131], a[36:39], v[12:15]\n"
"ds_read_b128 a[8:11], v177 offset:0\n"
"v_mfma_f32_16x16x32_bf16 v[16:19], a[128:131], a[40:43], v[16:19]\n"
"ds_read_b128 a[12:15], v177 offset:2048\n"
"v_mfma_f32_16x16x32_bf16 v[48:51], a[132:135], a[40:43], v[48:51]\n"
"v_mfma_f32_16x16x32_bf16 v[52:55], a[132:135], a[44:47], v[52:55]\n"
"ds_read_b128 a[16:19], v178 offset:0\n"
"v_mfma_f32_16x16x32_bf16 v[20:23], a[128:131], a[44:47], v[20:23]\n"
"ds_read_b128 a[20:23], v178 offset:2048\n"
"v_mfma_f32_16x16x32_bf16 v[24:27], a[128:131], a[48:51], v[24:27]\n"
"buffer_load_dwordx4 a[120:123], v199, s[36:39], s25 offen offset:2048 sc1\n"
"v_mfma_f32_16x16x32_bf16 v[56:59], a[132:135], a[48:51], v[56:59]\n"
"ds_read_b128 a[24:27], v179 offset:0\n"
"v_mfma_f32_16x16x32_bf16 v[60:63], a[132:135], a[52:55], v[60:63]\n"
"ds_read_b128 a[28:31], v179 offset:2048\n"
"v_mfma_f32_16x16x32_bf16 v[28:31], a[128:131], a[52:55], v[28:31]\n"
"buffer_load_dwordx4 v212, s[48:51], s24 offen offset:1024 lds\n"
"v_mfma_f32_16x16x32_bf16 v[32:35], a[128:131], a[56:59], v[32:35]\n"
"ds_read_b128 a[64:67], v192 offset:0\n"
"v_mfma_f32_16x16x32_bf16 v[64:67], a[132:135], a[56:59], v[64:67]\n"
"v_mfma_f32_16x16x32_bf16 v[68:71], a[132:135], a[60:63], v[68:71]\n"
"v_mfma_f32_16x16x32_bf16 v[36:39], a[128:131], a[60:63], v[36:39]\n"
"v_mfma_f32_16x16x32_bf16 v[72:75], a[68:71], a[32:35], v[72:75]\n"
"buffer_load_dwordx4 a[124:127], v200, s[36:39], s25 offen offset:2048 sc1\n"
"v_mfma_f32_16x16x32_bf16 v[76:79], a[68:71], a[36:39], v[76:79]\n"
"s_waitcnt vmcnt(26)\n"
"s_waitcnt lgkmcnt(0)\n"
"v_mfma_f32_16x16x32_bf16 v[8:11], a[72:75], a[0:3], v[8:11]\n"
"ds_read_b128 a[32:35], v180 offset:0\n"
"v_mfma_f32_16x16x32_bf16 v[40:43], a[76:79], a[0:3], v[40:43]\n"
"ds_read_b128 a[36:39], v180 offset:2048\n"
"v_mfma_f32_16x16x32_bf16 v[44:47], a[76:79], a[4:7], v[44:47]\n"
"buffer_load_dwordx4 v216, s[48:51], s24 offen offset:2048 lds\n"
"v_mfma_f32_16x16x32_bf16 v[12:15], a[72:75], a[4:7], v[12:15]\n"
"ds_read_b128 a[40:43], v181 offset:0\n"
"v_mfma_f32_16x16x32_bf16 v[16:19], a[72:75], a[8:11], v[16:19]\n"
"ds_read_b128 a[44:47], v181 offset:2048\n"
"v_mfma_f32_16x16x32_bf16 v[48:51], a[76:79], a[8:11], v[48:51]\n"
"v_mfma_f32_16x16x32_bf16 v[52:55], a[76:79], a[12:15], v[52:55]\n"
"ds_read_b128 a[48:51], v182 offset:0\n"
"v_mfma_f32_16x16x32_bf16 v[20:23], a[72:75], a[12:15], v[20:23]\n"
"ds_read_b128 a[52:55], v182 offset:2048\n"
"v_mfma_f32_16x16x32_bf16 v[24:27], a[72:75], a[16:19], v[24:27]\n"
"buffer_load_dwordx4 a[128:131], v199, s[36:39], s25 offen offset:3072 sc1\n"
"v_mfma_f32_16x16x32_bf16 v[56:59], a[76:79], a[16:19], v[56:59]\n"
"ds_read_b128 a[56:59], v183 offset:0\n"
"v_mfma_f32_16x16x32_bf16 v[60:63], a[76:79], a[20:23], v[60:63]\n"
"ds_read_b128 a[60:63], v183 offset:2048\n"
"v_mfma_f32_16x16x32_bf16 v[28:31], a[72:75], a[20:23], v[28:31]\n"
"buffer_load_dwordx4 v220, s[48:51], s24 offen offset:3072 lds\n"
"s_add_u32 s24, s24, 128\n"
"v_mfma_f32_16x16x32_bf16 v[32:35], a[72:75], a[24:27], v[32:35]\n"
"ds_read_b128 a[68:71], v192 offset:1024\n"
"v_mfma_f32_16x16x32_bf16 v[64:67], a[76:79], a[24:27], v[64:67]\n"
"v_mfma_f32_16x16x32_bf16 v[68:71], a[76:79], a[28:31], v[68:71]\n"
"v_mfma_f32_16x16x32_bf16 v[36:39], a[72:75], a[28:31], v[36:39]\n"
"v_mfma_f32_16x16x32_bf16 v[72:75], a[64:67], a[0:3], v[72:75]\n"
"buffer_load_dwordx4 a[132:135], v200, s[36:39], s25 offen offset:3072 sc1\n"
"s_add_u32 s25, s25, 4096\n"
"v_mfma_f32_16x16x32_bf16 v[76:79], a[64:67], a[4:7], v[76:79]\n"
"s_waitcnt vmcnt(25)\n"
"s_waitcnt lgkmcnt(0)\n"
"v_mfma_f32_16x16x32_bf16 v[8:11], a[80:83], a[32:35], v[8:11]\n"
"ds_read_b128 a[0:3], v176 offset:16384\n"
"v_mfma_f32_16x16x32_bf16 v[40:43], a[84:87], a[32:35], v[40:43]\n"
"ds_read_b128 a[4:7], v176 offset:18432\n"
"v_mfma_f32_16x16x32_bf16 v[44:47], a[84:87], a[36:39], v[44:47]\n"
"s_add_u32 m0, s6, 53248\n"
"s_nop 0\n"
"buffer_load_dwordx4 v208, s[48:51], s24 offen offset:0 lds\n"
"v_mfma_f32_16x16x32_bf16 v[12:15], a[80:83], a[36:39], v[12:15]\n"
"ds_read_b128 a[8:11], v177 offset:16384\n"
"v_mfma_f32_16x16x32_bf16 v[16:19], a[80:83], a[40:43], v[16:19]\n"
"ds_read_b128 a[12:15], v177 offset:18432\n"
"v_mfma_f32_16x16x32_bf16 v[48:51], a[84:87], a[40:43], v[48:51]\n"
"v_mfma_f32_16x16x32_bf16 v[52:55], a[84:87], a[44:47], v[52:55]\n"
"ds_read_b128 a[16:19], v178 offset:16384\n"
"v_mfma_f32_16x16x32_bf16 v[20:23], a[80:83], a[44:47], v[20:23]\n"
"ds_read_b128 a[20:23], v178 offset:18432\n"
"v_mfma_f32_16x16x32_bf16 v[24:27], a[80:83], a[48:51], v[24:27]\n"
"buffer_load_dwordx4 a[72:75], v199, s[36:39], s25 offen offset:0 sc1\n"
"v_mfma_f32_16x16x32_bf16 v[56:59], a[84:87], a[48:51], v[56:59]\n"
"ds_read_b128 a[24:27], v179 offset:16384\n"
"v_mfma_f32_16x16x32_bf16 v[60:63], a[84:87], a[52:55], v[60:63]\n"
"ds_read_b128 a[28:31], v179 offset:18432\n"
"v_mfma_f32_16x16x32_bf16 v[28:31], a[80:83], a[52:55], v[28:31]\n"
"buffer_load_dwordx4 v212, s[48:51], s24 offen offset:1024 lds\n"
"v_mfma_f32_16x16x32_bf16 v[32:35], a[80:83], a[56:59], v[32:35]\n"
"ds_read_b128 a[64:67], v192 offset:2048\n"
"v_mfma_f32_16x16x32_bf16 v[64:67], a[84:87], a[56:59], v[64:67]\n"
"v_mfma_f32_16x16x32_bf16 v[68:71], a[84:87], a[60:63], v[68:71]\n"
"v_mfma_f32_16x16x32_bf16 v[36:39], a[80:83], a[60:63], v[36:39]\n"
"v_mfma_f32_16x16x32_bf16 v[72:75], a[68:71], a[32:35], v[72:75]\n"
"buffer_load_dwordx4 a[76:79], v200, s[36:39], s25 offen offset:0 sc1\n"
"v_mfma_f32_16x16x32_bf16 v[76:79], a[68:71], a[36:39], v[76:79]\n"
"s_waitcnt vmcnt(25)\n"
"s_waitcnt lgkmcnt(0)\n"
"v_mfma_f32_16x16x32_bf16 v[8:11], a[88:91], a[0:3], v[8:11]\n"
"ds_read_b128 a[32:35], v180 offset:16384\n"
"v_mfma_f32_16x16x32_bf16 v[40:43], a[92:95], a[0:3], v[40:43]\n"
"ds_read_b128 a[36:39], v180 offset:18432\n"
"v_mfma_f32_16x16x32_bf16 v[44:47], a[92:95], a[4:7], v[44:47]\n"
"buffer_load_dwordx4 v216, s[48:51], s24 offen offset:2048 lds\n"
"v_mfma_f32_16x16x32_bf16 v[12:15], a[88:91], a[4:7], v[12:15]\n"
"ds_read_b128 a[40:43], v181 offset:16384\n"
"v_mfma_f32_16x16x32_bf16 v[16:19], a[88:91], a[8:11], v[16:19]\n"
"ds_read_b128 a[44:47], v181 offset:18432\n"
"v_mfma_f32_16x16x32_bf16 v[48:51], a[92:95], a[8:11], v[48:51]\n"
"v_mfma_f32_16x16x32_bf16 v[52:55], a[92:95], a[12:15], v[52:55]\n"
"ds_read_b128 a[48:51], v182 offset:16384\n"
"v_mfma_f32_16x16x32_bf16 v[20:23], a[88:91], a[12:15], v[20:23]\n"
"ds_read_b128 a[52:55], v182 offset:18432\n"
"v_mfma_f32_16x16x32_bf16 v[24:27], a[88:91], a[16:19], v[24:27]\n"
"buffer_load_dwordx4 a[80:83], v199, s[36:39], s25 offen offset:1024 sc1\n"
"v_mfma_f32_16x16x32_bf16 v[56:59], a[92:95], a[16:19], v[56:59]\n"
"ds_read_b128 a[56:59], v183 offset:16384\n"
"v_mfma_f32_16x16x32_bf16 v[60:63], a[92:95], a[20:23], v[60:63]\n"
"ds_read_b128 a[60:63], v183 offset:18432\n"
"v_mfma_f32_16x16x32_bf16 v[28:31], a[88:91], a[20:23], v[28:31]\n"
"buffer_load_dwordx4 v220, s[48:51], s24 offen offset:3072 lds\n"
"s_add_u32 s24, s24, 128\n"
"v_mfma_f32_16x16x32_bf16 v[32:35], a[88:91], a[24:27], v[32:35]\n"
"ds_read_b128 a[68:71], v192 offset:3072\n"
"v_mfma_f32_16x16x32_bf16 v[64:67], a[92:95], a[24:27], v[64:67]\n"
"v_mfma_f32_16x16x32_bf16 v[68:71], a[92:95], a[28:31], v[68:71]\n"
"s_add_u32 m0, s7, 69632\n"
"s_nop 0\n"
"buffer_load_dwordx4 v194, s[36:39], s26 offen sc1 lds\n"
"s_add_u32 s26, s26, 4096\n"
"v_mfma_f32_16x16x32_bf16 v[36:39], a[88:91], a[28:31], v[36:39]\n"
"v_mfma_f32_16x16x32_bf16 v[72:75], a[64:67], a[0:3], v[72:75]\n"
"buffer_load_dwordx4 a[84:87], v200, s[36:39], s25 offen offset:1024 sc1\n"
"v_mfma_f32_16x16x32_bf16 v[76:79], a[64:67], a[4:7], v[76:79]\n"
"s_waitcnt vmcnt(26)\n"
"s_waitcnt vmcnt(35) lgkmcnt(0)\n"
"s_barrier\n"
"v_mfma_f32_16x16x32_bf16 v[8:11], a[96:99], a[32:35], v[8:11]\n"
"ds_read_b128 a[0:3], v176 offset:36864\n"
"v_mfma_f32_16x16x32_bf16 v[40:43], a[100:103], a[32:35], v[40:43]\n"
"ds_read_b128 a[4:7], v176 offset:38912\n"
"v_mfma_f32_16x16x32_bf16 v[44:47], a[100:103], a[36:39], v[44:47]\n"
"s_add_u32 m0, s6, 86016\n"
"s_nop 0\n"
"buffer_load_dwordx4 v208, s[48:51], s24 offen offset:0 lds\n"
"v_mfma_f32_16x16x32_bf16 v[12:15], a[96:99], a[36:39], v[12:15]\n"
"ds_read_b128 a[8:11], v177 offset:36864\n"
"v_mfma_f32_16x16x32_bf16 v[16:19], a[96:99], a[40:43], v[16:19]\n"
"ds_read_b128 a[12:15], v177 offset:38912\n"
"v_mfma_f32_16x16x32_bf16 v[48:51], a[100:103], a[40:43], v[48:51]\n"
"v_mfma_f32_16x16x32_bf16 v[52:55], a[100:103], a[44:47], v[52:55]\n"
"ds_read_b128 a[16:19], v178 offset:36864\n"
"v_mfma_f32_16x16x32_bf16 v[20:23], a[96:99], a[44:47], v[20:23]\n"
"ds_read_b128 a[20:23], v178 offset:38912\n"
"v_mfma_f32_16x16x32_bf16 v[24:27], a[96:99], a[48:51], v[24:27]\n"
"buffer_load_dwordx4 a[88:91], v199, s[36:39], s25 offen offset:2048 sc1\n"
"v_mfma_f32_16x16x32_bf16 v[56:59], a[100:103], a[48:51], v[56:59]\n"
"ds_read_b128 a[24:27], v179 offset:36864\n"
"v_mfma_f32_16x16x32_bf16 v[60:63], a[100:103], a[52:55], v[60:63]\n"
"ds_read_b128 a[28:31], v179 offset:38912\n"
"v_mfma_f32_16x16x32_bf16 v[28:31], a[96:99], a[52:55], v[28:31]\n"
"buffer_load_dwordx4 v212, s[48:51], s24 offen offset:1024 lds\n"
"v_mfma_f32_16x16x32_bf16 v[32:35], a[96:99], a[56:59], v[32:35]\n"
"ds_read_b128 a[64:67], v192 offset:36864\n"
"v_mfma_f32_16x16x32_bf16 v[64:67], a[100:103], a[56:59], v[64:67]\n"
"v_mfma_f32_16x16x32_bf16 v[68:71], a[100:103], a[60:63], v[68:71]\n"
"v_mfma_f32_16x16x32_bf16 v[36:39], a[96:99], a[60:63], v[36:39]\n"
"v_mfma_f32_16x16x32_bf16 v[72:75], a[68:71], a[32:35], v[72:75]\n"
"buffer_load_dwordx4 a[92:95], v200, s[36:39], s25 offen offset:2048 sc1\n"
"v_mfma_f32_16x16x32_bf16 v[76:79], a[68:71], a[36:39], v[76:79]\n"
"s_waitcnt vmcnt(26)\n"
"s_waitcnt lgkmcnt(0)\n"
"v_mfma_f32_16x16x32_bf16 v[8:11], a[104:107], a[0:3], v[8:11]\n"
"ds_read_b128 a[32:35], v180 offset:36864\n"
"v_mfma_f32_16x16x32_bf16 v[40:43], a[108:111], a[0:3], v[40:43]\n"
"ds_read_b128 a[36:39], v180 offset:38912\n"
"v_mfma_f32_16x16x32_bf16 v[44:47], a[108:111], a[4:7], v[44:47]\n"
"buffer_load_dwordx4 v216, s[48:51], s24 offen offset:2048 lds\n"
"v_mfma_f32_16x16x32_bf16 v[12:15], a[104:107], a[4:7], v[12:15]\n"
"ds_read_b128 a[40:43], v181 offset:36864\n"
"v_mfma_f32_16x16x32_bf16 v[16:19], a[104:107], a[8:11], v[16:19]\n"
"ds_read_b128 a[44:47], v181 offset:38912\n"
"v_mfma_f32_16x16x32_bf16 v[48:51], a[108:111], a[8:11], v[48:51]\n"
"v_mfma_f32_16x16x32_bf16 v[52:55], a[108:111], a[12:15], v[52:55]\n"
"ds_read_b128 a[48:51], v182 offset:36864\n"
"v_mfma_f32_16x16x32_bf16 v[20:23], a[104:107], a[12:15], v[20:23]\n"
"ds_read_b128 a[52:55], v182 offset:38912\n"
"v_mfma_f32_16x16x32_bf16 v[24:27], a[104:107], a[16:19], v[24:27]\n"
"buffer_load_dwordx4 a[96:99], v199, s[36:39], s25 offen offset:3072 sc1\n"
"v_mfma_f32_16x16x32_bf16 v[56:59], a[108:111], a[16:19], v[56:59]\n"
"ds_read_b128 a[56:59], v183 offset:36864\n"
"v_mfma_f32_16x16x32_bf16 v[60:63], a[108:111], a[20:23], v[60:63]\n"
"ds_read_b128 a[60:63], v183 offset:38912\n"
"v_mfma_f32_16x16x32_bf16 v[28:31], a[104:107], a[20:23], v[28:31]\n"
"buffer_load_dwordx4 v220, s[48:51], s24 offen offset:3072 lds\n"
"s_add_u32 s24, s24, 128\n"
"v_mfma_f32_16x16x32_bf16 v[32:35], a[104:107], a[24:27], v[32:35]\n"
"ds_read_b128 a[68:71], v192 offset:37888\n"
"v_mfma_f32_16x16x32_bf16 v[64:67], a[108:111], a[24:27], v[64:67]\n"
"v_mfma_f32_16x16x32_bf16 v[68:71], a[108:111], a[28:31], v[68:71]\n"
"v_mfma_f32_16x16x32_bf16 v[36:39], a[104:107], a[28:31], v[36:39]\n"
"v_mfma_f32_16x16x32_bf16 v[72:75], a[64:67], a[0:3], v[72:75]\n"
"buffer_load_dwordx4 a[100:103], v200, s[36:39], s25 offen offset:3072 sc1\n"
"s_add_u32 s25, s25, 4096\n"
"v_mfma_f32_16x16x32_bf16 v[76:79], a[64:67], a[4:7], v[76:79]\n"
"s_waitcnt vmcnt(25)\n"
"s_waitcnt lgkmcnt(0)\n"
"v_mfma_f32_16x16x32_bf16 v[8:11], a[112:115], a[32:35], v[8:11]\n"
"ds_read_b128 a[0:3], v176 offset:53248\n"
"v_mfma_f32_16x16x32_bf16 v[40:43], a[116:119], a[32:35], v[40:43]\n"
"ds_read_b128 a[4:7], v176 offset:55296\n"
"v_mfma_f32_16x16x32_bf16 v[44:47], a[116:119], a[36:39], v[44:47]\n"
"s_add_u32 m0, s6, 102400\n"
"s_nop 0\n"
"buffer_load_dwordx4 v208, s[48:51], s24 offen offset:0 lds\n"
"v_mfma_f32_16x16x32_bf16 v[12:15], a[112:115], a[36:39], v[12:15]\n"
"ds_read_b128 a[8:11], v177 offset:53248\n"
"v_mfma_f32_16x16x32_bf16 v[16:19], a[112:115], a[40:43], v[16:19]\n"
"ds_read_b128 a[12:15], v177 offset:55296\n"
"v_mfma_f32_16x16x32_bf16 v[48:51], a[116:119], a[40:43], v[48:51]\n"
"v_mfma_f32_16x16x32_bf16 v[52:55], a[116:119], a[44:47], v[52:55]\n"
"ds_read_b128 a[16:19], v178 offset:53248\n"
"v_mfma_f32_16x16x32_bf16 v[20:23], a[112:115], a[44:47], v[20:23]\n"
"ds_read_b128 a[20:23], v178 offset:55296\n"
"v_mfma_f32_16x16x32_bf16 v[24:27], a[112:115], a[48:51], v[24:27]\n"
"buffer_load_dwordx4 a[104:107], v199, s[36:39], s25 offen offset:0 sc1\n"
"v_mfma_f32_16x16x32_bf16 v[56:59], a[116:119], a[48:51], v[56:59]\n"
"ds_read_b128 a[24:27], v179 offset:53248\n"
"v_mfma_f32_16x16x32_bf16 v[60:63], a[116:119], a[52:55], v[60:63]\n"
"ds_read_b128 a[28:31], v179 offset:55296\n"
"v_mfma_f32_16x16x32_bf16 v[28:31], a[112:115], a[52:55], v[28:31]\n"
"buffer_load_dwordx4 v212, s[48:51], s24 offen offset:1024 lds\n"
"v_mfma_f32_16x16x32_bf16 v[32:35], a[112:115], a[56:59], v[32:35]\n"
"ds_read_b128 a[64:67], v192 offset:38912\n"
"v_mfma_f32_16x16x32_bf16 v[64:67], a[116:119], a[56:59], v[64:67]\n"
"v_mfma_f32_16x16x32_bf16 v[68:71], a[116:119], a[60:63], v[68:71]\n"
"v_mfma_f32_16x16x32_bf16 v[36:39], a[112:115], a[60:63], v[36:39]\n"
"v_mfma_f32_16x16x32_bf16 v[72:75], a[68:71], a[32:35], v[72:75]\n"
"buffer_load_dwordx4 a[108:111], v200, s[36:39], s25 offen offset:0 sc1\n"
"v_mfma_f32_16x16x32_bf16 v[76:79], a[68:71], a[36:39], v[76:79]\n"
"s_waitcnt vmcnt(25)\n"
"s_waitcnt lgkmcnt(0)\n"
"v_mfma_f32_16x16x32_bf16 v[8:11], a[120:123], a[0:3], v[8:11]\n"
"ds_read_b128 a[32:35], v180 offset:53248\n"
"v_mfma_f32_16x16x32_bf16 v[40:43], a[124:127], a[0:3], v[40:43]\n"
"ds_read_b128 a[36:39], v180 offset:55296\n"
"v_mfma_f32_16x16x32_bf16 v[44:47], a[124:127], a[4:7], v[44:47]\n"
"buffer_load_dwordx4 v216, s[48:51], s24 offen offset:2048 lds\n"
"v_mfma_f32_16x16x32_bf16 v[12:15], a[120:123], a[4:7], v[12:15]\n"
"ds_read_b128 a[40:43], v181 offset:53248\n"
"v_mfma_f32_16x16x32_bf16 v[16:19], a[120:123], a[8:11], v[16:19]\n"
"ds_read_b128 a[44:47], v181 offset:55296\n"
"v_mfma_f32_16x16x32_bf16 v[48:51], a[124:127], a[8:11], v[48:51]\n"
"v_mfma_f32_16x16x32_bf16 v[52:55], a[124:127], a[12:15], v[52:55]\n"
"ds_read_b128 a[48:51], v182 offset:53248\n"
"v_mfma_f32_16x16x32_bf16 v[20:23], a[120:123], a[12:15], v[20:23]\n"
"ds_read_b128 a[52:55], v182 offset:55296\n"
"v_mfma_f32_16x16x32_bf16 v[24:27], a[120:123], a[16:19], v[24:27]\n"
"buffer_load_dwordx4 a[112:115], v199, s[36:39], s25 offen offset:1024 sc1\n"
"v_mfma_f32_16x16x32_bf16 v[56:59], a[124:127], a[16:19], v[56:59]\n"
"ds_read_b128 a[56:59], v183 offset:53248\n"
"v_mfma_f32_16x16x32_bf16 v[60:63], a[124:127], a[20:23], v[60:63]\n"
"ds_read_b128 a[60:63], v183 offset:55296\n"
"v_mfma_f32_16x16x32_bf16 v[28:31], a[120:123], a[20:23], v[28:31]\n"
"buffer_load_dwordx4 v220, s[48:51], s24 offen offset:3072 lds\n"
"s_add_u32 s24, s24, 128\n"
"v_mfma_f32_16x16x32_bf16 v[32:35], a[120:123], a[24:27], v[32:35]\n"
"ds_read_b128 a[68:71], v192 offset:39936\n"
"v_mfma_f32_16x16x32_bf16 v[64:67], a[124:127], a[24:27], v[64:67]\n"
"v_mfma_f32_16x16x32_bf16 v[68:71], a[124:127], a[28:31], v[68:71]\n"
"s_add_u32 m0, s7, 118784\n"
"s_nop 0\n"
"buffer_load_dwordx4 v194, s[36:39], s26 offen sc1 lds\n"
"s_add_u32 s26, s26, 4096\n"
"v_mfma_f32_16x16x32_bf16 v[36:39], a[120:123], a[28:31], v[36:39]\n"
"v_mfma_f32_16x16x32_bf16 v[72:75], a[64:67], a[0:3], v[72:75]\n"
"buffer_load_dwordx4 a[116:119], v200, s[36:39], s25 offen offset:1024 sc1\n"
"v_mfma_f32_16x16x32_bf16 v[76:79], a[64:67], a[4:7], v[76:79]\n"
"s_waitcnt vmcnt(26)\n"
"s_waitcnt vmcnt(35) lgkmcnt(0)\n"
"s_barrier\n"
"v_mfma_f32_16x16x32_bf16 v[8:11], a[128:131], a[32:35], v[8:11]\n"
"ds_read_b128 a[0:3], v184 offset:0\n"
"v_mfma_f32_16x16x32_bf16 v[40:43], a[132:135], a[32:35], v[40:43]\n"
"ds_read_b128 a[4:7], v184 offset:2048\n"
"v_mfma_f32_16x16x32_bf16 v[44:47], a[132:135], a[36:39], v[44:47]\n"
"s_add_u32 m0, s6, 122880\n"
"s_nop 0\n"
"buffer_load_dwordx4 v208, s[48:51], s24 offen offset:0 lds\n"
"v_mfma_f32_16x16x32_bf16 v[12:15], a[128:131], a[36:39], v[12:15]\n"
"ds_read_b128 a[8:11], v185 offset:0\n"
"v_mfma_f32_16x16x32_bf16 v[16:19], a[128:131], a[40:43], v[16:19]\n"
"ds_read_b128 a[12:15], v185 offset:2048\n"
"v_mfma_f32_16x16x32_bf16 v[48:51], a[132:135], a[40:43], v[48:51]\n"
"v_mfma_f32_16x16x32_bf16 v[52:55], a[132:135], a[44:47], v[52:55]\n"
"ds_read_b128 a[16:19], v186 offset:0\n"
"v_mfma_f32_16x16x32_bf16 v[20:23], a[128:131], a[44:47], v[20:23]\n"
"ds_read_b128 a[20:23], v186 offset:2048\n"
"v_mfma_f32_16x16x32_bf16 v[24:27], a[128:131], a[48:51], v[24:27]\n"
"buffer_load_dwordx4 a[120:123], v199, s[36:39], s25 offen offset:2048 sc1\n"
"v_mfma_f32_16x16x32_bf16 v[56:59], a[132:135], a[48:51], v[56:59]\n"
"ds_read_b128 a[24:27], v187 offset:0\n"
"v_mfma_f32_16x16x32_bf16 v[60:63], a[132:135], a[52:55], v[60:63]\n"
"ds_read_b128 a[28:31], v187 offset:2048\n"
"v_mfma_f32_16x16x32_bf16 v[28:31], a[128:131], a[52:55], v[28:31]\n"
"buffer_load_dwordx4 v212, s[48:51], s24 offen offset:1024 lds\n"
"v_mfma_f32_16x16x32_bf16 v[32:35], a[128:131], a[56:59], v[32:35]\n"
"ds_read_b128 a[64:67], v193 offset:0\n"
"v_mfma_f32_16x16x32_bf16 v[64:67], a[132:135], a[56:59], v[64:67]\n"
"v_mfma_f32_16x16x32_bf16 v[68:71], a[132:135], a[60:63], v[68:71]\n"
"v_mfma_f32_16x16x32_bf16 v[36:39], a[128:131], a[60:63], v[36:39]\n"
"v_mfma_f32_16x16x32_bf16 v[72:75], a[68:71], a[32:35], v[72:75]\n"
"buffer_load_dwordx4 a[124:127], v200, s[36:39], s25 offen offset:2048 sc1\n"
"v_mfma_f32_16x16x32_bf16 v[76:79], a[68:71], a[36:39], v[76:79]\n"
"s_waitcnt vmcnt(26)\n"
"s_waitcnt lgkmcnt(0)\n"
"v_mfma_f32_16x16x32_bf16 v[8:11], a[72:75], a[0:3], v[8:11]\n"
"ds_read_b128 a[32:35], v188 offset:0\n"
"v_mfma_f32_16x16x32_bf16 v[40:43], a[76:79], a[0:3], v[40:43]\n"
"ds_read_b128 a[36:39], v188 offset:2048\n"
"v_mfma_f32_16x16x32_bf16 v[44:47], a[76:79], a[4:7], v[44:47]\n"
"buffer_load_dwordx4 v216, s[48:51], s24 offen offset:2048 lds\n"
"v_mfma_f32_16x16x32_bf16 v[12:15], a[72:75], a[4:7], v[12:15]\n"
"ds_read_b128 a[40:43], v189 offset:0\n"
"v_mfma_f32_16x16x32_bf16 v[16:19], a[72:75], a[8:11], v[16:19]\n"
"ds_read_b128 a[44:47], v189 offset:2048\n"
"v_mfma_f32_16x16x32_bf16 v[48:51], a[76:79], a[8:11], v[48:51]\n"
"v_mfma_f32_16x16x32_bf16 v[52:55], a[76:79], a[12:15], v[52:55]\n"
"ds_read_b128 a[48:51], v190 offset:0\n"
"v_mfma_f32_16x16x32_bf16 v[20:23], a[72:75], a[12:15], v[20:23]\n"
"ds_read_b128 a[52:55], v190 offset:2048\n"
"v_mfma_f32_16x16x32_bf16 v[24:27], a[72:75], a[16:19], v[24:27]\n"
"buffer_load_dwordx4 a[128:131], v199, s[36:39], s25 offen offset:3072 sc1\n"
"v_mfma_f32_16x16x32_bf16 v[56:59], a[76:79], a[16:19], v[56:59]\n"
"ds_read_b128 a[56:59], v191 offset:0\n"
"v_mfma_f32_16x16x32_bf16 v[60:63], a[76:79], a[20:23], v[60:63]\n"
"ds_read_b128 a[60:63], v191 offset:2048\n"
"v_mfma_f32_16x16x32_bf16 v[28:31], a[72:75], a[20:23], v[28:31]\n"
"buffer_load_dwordx4 v220, s[48:51], s24 offen offset:3072 lds\n"
"s_add_u32 s24, s24, 128\n"
"v_mfma_f32_16x16x32_bf16 v[32:35], a[72:75], a[24:27], v[32:35]\n"
"ds_read_b128 a[68:71], v193 offset:1024\n"
"v_mfma_f32_16x16x32_bf16 v[64:67], a[76:79], a[24:27], v[64:67]\n"
"v_mfma_f32_16x16x32_bf16 v[68:71], a[76:79], a[28:31], v[68:71]\n"
"v_mfma_f32_16x16x32_bf16 v[36:39], a[72:75], a[28:31], v[36:39]\n"
"v_mfma_f32_16x16x32_bf16 v[72:75], a[64:67], a[0:3], v[72:75]\n"
"buffer_load_dwordx4 a[132:135], v200, s[36:39], s25 offen offset:3072 sc1\n"
"s_add_u32 s25, s25, 4096\n"
"v_mfma_f32_16x16x32_bf16 v[76:79], a[64:67], a[4:7], v[76:79]\n"
"s_waitcnt vmcnt(25)\n"
"s_waitcnt lgkmcnt(0)\n"
"v_mfma_f32_16x16x32_bf16 v[8:11], a[80:83], a[32:35], v[8:11]\n"
"ds_read_b128 a[0:3], v184 offset:16384\n"
"v_mfma_f32_16x16x32_bf16 v[40:43], a[84:87], a[32:35], v[40:43]\n"
"ds_read_b128 a[4:7], v184 offset:18432\n"
"v_mfma_f32_16x16x32_bf16 v[44:47], a[84:87], a[36:39], v[44:47]\n"
"s_add_u32 m0, s6, 139264\n"
"s_nop 0\n"
"buffer_load_dwordx4 v208, s[48:51], s24 offen offset:0 lds\n"
"v_mfma_f32_16x16x32_bf16 v[12:15], a[80:83], a[36:39], v[12:15]\n"
"ds_read_b128 a[8:11], v185 offset:16384\n"
"v_mfma_f32_16x16x32_bf16 v[16:19], a[80:83], a[40:43], v[16:19]\n"
"ds_read_b128 a[12:15], v185 offset:18432\n"
"v_mfma_f32_16x16x32_bf16 v[48:51], a[84:87], a[40:43], v[48:51]\n"
"v_mfma_f32_16x16x32_bf16 v[52:55], a[84:87], a[44:47], v[52:55]\n"
"ds_read_b128 a[16:19], v186 offset:16384\n"
"v_mfma_f32_16x16x32_bf16 v[20:23], a[80:83], a[44:47], v[20:23]\n"
"ds_read_b128 a[20:23], v186 offset:18432\n"
"v_mfma_f32_16x16x32_bf16 v[24:27], a[80:83], a[48:51], v[24:27]\n"
"buffer_load_dwordx4 a[72:75], v199, s[36:39], s25 offen offset:0 sc1\n"
"v_mfma_f32_16x16x32_bf16 v[56:59], a[84:87], a[48:51], v[56:59]\n"
"ds_read_b128 a[24:27], v187 offset:16384\n"
"v_mfma_f32_16x16x32_bf16 v[60:63], a[84:87], a[52:55], v[60:63]\n"
"ds_read_b128 a[28:31], v187 offset:18432\n"
"v_mfma_f32_16x16x32_bf16 v[28:31], a[80:83], a[52:55], v[28:31]\n"
"buffer_load_dwordx4 v212, s[48:51], s24 offen offset:1024 lds\n"
"v_mfma_f32_16x16x32_bf16 v[32:35], a[80:83], a[56:59], v[32:35]\n"
"ds_read_b128 a[64:67], v193 offset:2048\n"
"v_mfma_f32_16x16x32_bf16 v[64:67], a[84:87], a[56:59], v[64:67]\n"
"v_mfma_f32_16x16x32_bf16 v[68:71], a[84:87], a[60:63], v[68:71]\n"
"v_mfma_f32_16x16x32_bf16 v[36:39], a[80:83], a[60:63], v[36:39]\n"
"v_mfma_f32_16x16x32_bf16 v[72:75], a[68:71], a[32:35], v[72:75]\n"
"buffer_load_dwordx4 a[76:79], v200, s[36:39], s25 offen offset:0 sc1\n"
"v_mfma_f32_16x16x32_bf16 v[76:79], a[68:71], a[36:39], v[76:79]\n"
"s_waitcnt vmcnt(25)\n"
"s_waitcnt lgkmcnt(0)\n"
"v_mfma_f32_16x16x32_bf16 v[8:11], a[88:91], a[0:3], v[8:11]\n"
"ds_read_b128 a[32:35], v188 offset:16384\n"
"v_mfma_f32_16x16x32_bf16 v[40:43], a[92:95], a[0:3], v[40:43]\n"
"ds_read_b128 a[36:39], v188 offset:18432\n"
"v_mfma_f32_16x16x32_bf16 v[44:47], a[92:95], a[4:7], v[44:47]\n"
"buffer_load_dwordx4 v216, s[48:51], s24 offen offset:2048 lds\n"
"v_mfma_f32_16x16x32_bf16 v[12:15], a[88:91], a[4:7], v[12:15]\n"
"ds_read_b128 a[40:43], v189 offset:16384\n"
"v_mfma_f32_16x16x32_bf16 v[16:19], a[88:91], a[8:11], v[16:19]\n"
"ds_read_b128 a[44:47], v189 offset:18432\n"
"v_mfma_f32_16x16x32_bf16 v[48:51], a[92:95], a[8:11], v[48:51]\n"
"v_mfma_f32_16x16x32_bf16 v[52:55], a[92:95], a[12:15], v[52:55]\n"
"ds_read_b128 a[48:51], v190 offset:16384\n"
"v_mfma_f32_16x16x32_bf16 v[20:23], a[88:91], a[12:15], v[20:23]\n"
"ds_read_b128 a[52:55], v190 offset:18432\n"
"v_mfma_f32_16x16x32_bf16 v[24:27], a[88:91], a[16:19], v[24:27]\n"
"buffer_load_dwordx4 a[80:83], v199, s[36:39], s25 offen offset:1024 sc1\n"
"v_mfma_f32_16x16x32_bf16 v[56:59], a[92:95], a[16:19], v[56:59]\n"
"ds_read_b128 a[56:59], v191 offset:16384\n"
"v_mfma_f32_16x16x32_bf16 v[60:63], a[92:95], a[20:23], v[60:63]\n"
"ds_read_b128 a[60:63], v191 offset:18432\n"
"v_mfma_f32_16x16x32_bf16 v[28:31], a[88:91], a[20:23], v[28:31]\n"
"buffer_load_dwordx4 v220, s[48:51], s24 offen offset:3072 lds\n"
"s_add_u32 s24, s24, 128\n"
"v_mfma_f32_16x16x32_bf16 v[32:35], a[88:91], a[24:27], v[32:35]\n"
"ds_read_b128 a[68:71], v193 offset:3072\n"
"v_mfma_f32_16x16x32_bf16 v[64:67], a[92:95], a[24:27], v[64:67]\n"
"v_mfma_f32_16x16x32_bf16 v[68:71], a[92:95], a[28:31], v[68:71]\n"
"s_add_u32 m0, s7, 155648\n"
"s_nop 0\n"
"buffer_load_dwordx4 v194, s[36:39], s26 offen sc1 lds\n"
"s_add_u32 s26, s26, 4096\n"
"v_mfma_f32_16x16x32_bf16 v[36:39], a[88:91], a[28:31], v[36:39]\n"
"v_mfma_f32_16x16x32_bf16 v[72:75], a[64:67], a[0:3], v[72:75]\n"
"buffer_load_dwordx4 a[84:87], v200, s[36:39], s25 offen offset:1024 sc1\n"
"v_mfma_f32_16x16x32_bf16 v[76:79], a[64:67], a[4:7], v[76:79]\n"
"s_waitcnt vmcnt(26)\n"
"s_waitcnt vmcnt(35) lgkmcnt(0)\n"
"s_barrier\n"
"v_mfma_f32_16x16x32_bf16 v[8:11], a[96:99], a[32:35], v[8:11]\n"
"ds_read_b128 a[0:3], v184 offset:36864\n"
"v_mfma_f32_16x16x32_bf16 v[40:43], a[100:103], a[32:35], v[40:43]\n"
"ds_read_b128 a[4:7], v184 offset:38912\n"
"v_mfma_f32_16x16x32_bf16 v[44:47], a[100:103], a[36:39], v[44:47]\n"
"s_add_u32 m0, s6, 0\n"
"s_nop 0\n"
"buffer_load_dwordx4 v208, s[48:51], s24 offen offset:0 lds\n"
"v_mfma_f32_16x16x32_bf16 v[12:15], a[96:99], a[36:39], v[12:15]\n"
"ds_read_b128 a[8:11], v185 offset:36864\n"
"v_mfma_f32_16x16x32_bf16 v[16:19], a[96:99], a[40:43], v[16:19]\n"
"ds_read_b128 a[12:15], v185 offset:38912\n"
"v_mfma_f32_16x16x32_bf16 v[48:51], a[100:103], a[40:43], v[48:51]\n"
"v_mfma_f32_16x16x32_bf16 v[52:55], a[100:103], a[44:47], v[52:55]\n"
"ds_read_b128 a[16:19], v186 offset:36864\n"
"v_mfma_f32_16x16x32_bf16 v[20:23], a[96:99], a[44:47], v[20:23]\n"
"ds_read_b128 a[20:23], v186 offset:38912\n"
"v_mfma_f32_16x16x32_bf16 v[24:27], a[96:99], a[48:51], v[24:27]\n"
"buffer_load_dwordx4 a[88:91], v199, s[36:39], s25 offen offset:2048 sc1\n"
"v_mfma_f32_16x16x32_bf16 v[56:59], a[100:103], a[48:51], v[56:59]\n"
"ds_read_b128 a[24:27], v187 offset:36864\n"
"v_mfma_f32_16x16x32_bf16 v[60:63], a[100:103], a[52:55], v[60:63]\n"
"ds_read_b128 a[28:31], v187 offset:38912\n"
"v_mfma_f32_16x16x32_bf16 v[28:31], a[96:99], a[52:55], v[28:31]\n"
"buffer_load_dwordx4 v212, s[48:51], s24 offen offset:1024 lds\n"
"v_mfma_f32_16x16x32_bf16 v[32:35], a[96:99], a[56:59], v[32:35]\n"
"ds_read_b128 a[64:67], v193 offset:36864\n"
"v_mfma_f32_16x16x32_bf16 v[64:67], a[100:103], a[56:59], v[64:67]\n"
"v_mfma_f32_16x16x32_bf16 v[68:71], a[100:103], a[60:63], v[68:71]\n"
"v_mfma_f32_16x16x32_bf16 v[36:39], a[96:99], a[60:63], v[36:39]\n"
"v_mfma_f32_16x16x32_bf16 v[72:75], a[68:71], a[32:35], v[72:75]\n"
"buffer_load_dwordx4 a[92:95], v200, s[36:39], s25 offen offset:2048 sc1\n"
"v_mfma_f32_16x16x32_bf16 v[76:79], a[68:71], a[36:39], v[76:79]\n"
"s_waitcnt vmcnt(26)\n"
"s_waitcnt lgkmcnt(0)\n"
"v_mfma_f32_16x16x32_bf16 v[8:11], a[104:107], a[0:3], v[8:11]\n"
"ds_read_b128 a[32:35], v188 offset:36864\n"
"v_mfma_f32_16x16x32_bf16 v[40:43], a[108:111], a[0:3], v[40:43]\n"
"ds_read_b128 a[36:39], v188 offset:38912\n"
"v_mfma_f32_16x16x32_bf16 v[44:47], a[108:111], a[4:7], v[44:47]\n"
"buffer_load_dwordx4 v216, s[48:51], s24 offen offset:2048 lds\n"
"v_mfma_f32_16x16x32_bf16 v[12:15], a[104:107], a[4:7], v[12:15]\n"
"ds_read_b128 a[40:43], v189 offset:36864\n"
"v_mfma_f32_16x16x32_bf16 v[16:19], a[104:107], a[8:11], v[16:19]\n"
"ds_read_b128 a[44:47], v189 offset:38912\n"
"v_mfma_f32_16x16x32_bf16 v[48:51], a[108:111], a[8:11], v[48:51]\n"
"v_mfma_f32_16x16x32_bf16 v[52:55], a[108:111], a[12:15], v[52:55]\n"
"ds_read_b128 a[48:51], v190 offset:36864\n"
"v_mfma_f32_16x16x32_bf16 v[20:23], a[104:107], a[12:15], v[20:23]\n"
"ds_read_b128 a[52:55], v190 offset:38912\n"
"v_mfma_f32_16x16x32_bf16 v[24:27], a[104:107], a[16:19], v[24:27]\n"
"buffer_load_dwordx4 a[96:99], v199, s[36:39], s25 offen offset:3072 sc1\n"
"v_mfma_f32_16x16x32_bf16 v[56:59], a[108:111], a[16:19], v[56:59]\n"
"ds_read_b128 a[56:59], v191 offset:36864\n"
"v_mfma_f32_16x16x32_bf16 v[60:63], a[108:111], a[20:23], v[60:63]\n"
"ds_read_b128 a[60:63], v191 offset:38912\n"
"v_mfma_f32_16x16x32_bf16 v[28:31], a[104:107], a[20:23], v[28:31]\n"
"buffer_load_dwordx4 v220, s[48:51], s24 offen offset:3072 lds\n"
"s_add_u32 s24, s24, 128\n"
"v_mfma_f32_16x16x32_bf16 v[32:35], a[104:107], a[24:27], v[32:35]\n"
"ds_read_b128 a[68:71], v193 offset:37888\n"
"v_mfma_f32_16x16x32_bf16 v[64:67], a[108:111], a[24:27], v[64:67]\n"
"v_mfma_f32_16x16x32_bf16 v[68:71], a[108:111], a[28:31], v[68:71]\n"
"v_mfma_f32_16x16x32_bf16 v[36:39], a[104:107], a[28:31], v[36:39]\n"
"v_mfma_f32_16x16x32_bf16 v[72:75], a[64:67], a[0:3], v[72:75]\n"
"buffer_load_dwordx4 a[100:103], v200, s[36:39], s25 offen offset:3072 sc1\n"
"s_add_u32 s25, s25, 4096\n"
"v_mfma_f32_16x16x32_bf16 v[76:79], a[64:67], a[4:7], v[76:79]\n"
"s_waitcnt vmcnt(25)\n"
"s_waitcnt lgkmcnt(0)\n"
"v_mfma_f32_16x16x32_bf16 v[8:11], a[112:115], a[32:35], v[8:11]\n"
"ds_read_b128 a[0:3], v184 offset:53248\n"
"v_mfma_f32_16x16x32_bf16 v[40:43], a[116:119], a[32:35], v[40:43]\n"
"ds_read_b128 a[4:7], v184 offset:55296\n"
"v_mfma_f32_16x16x32_bf16 v[44:47], a[116:119], a[36:39], v[44:47]\n"
"s_add_u32 m0, s6, 16384\n"
"s_nop 0\n"
"buffer_load_dwordx4 v208, s[48:51], s24 offen offset:0 lds\n"
"v_mfma_f32_16x16x32_bf16 v[12:15], a[112:115], a[36:39], v[12:15]\n"
"ds_read_b128 a[8:11], v185 offset:53248\n"
"v_mfma_f32_16x16x32_bf16 v[16:19], a[112:115], a[40:43], v[16:19]\n"
"ds_read_b128 a[12:15], v185 offset:55296\n"
"v_mfma_f32_16x16x32_bf16 v[48:51], a[116:119], a[40:43], v[48:51]\n"
"v_mfma_f32_16x16x32_bf16 v[52:55], a[116:119], a[44:47], v[52:55]\n"
"ds_read_b128 a[16:19], v186 offset:53248\n"
"v_mfma_f32_16x16x32_bf16 v[20:23], a[112:115], a[44:47], v[20:23]\n"
"ds_read_b128 a[20:23], v186 offset:55296\n"
"v_mfma_f32_16x16x32_bf16 v[24:27], a[112:115], a[48:51], v[24:27]\n"
"buffer_load_dwordx4 a[104:107], v199, s[36:39], s25 offen offset:0 sc1\n"
"v_mfma_f32_16x16x32_bf16 v[56:59], a[116:119], a[48:51], v[56:59]\n"
"ds_read_b128 a[24:27], v187 offset:53248\n"
"v_mfma_f32_16x16x32_bf16 v[60:63], a[116:119], a[52:55], v[60:63]\n"
"ds_read_b128 a[28:31], v187 offset:55296\n"
"v_mfma_f32_16x16x32_bf16 v[28:31], a[112:115], a[52:55], v[28:31]\n"
"buffer_load_dwordx4 v212, s[48:51], s24 offen offset:1024 lds\n"
"v_mfma_f32_16x16x32_bf16 v[32:35], a[112:115], a[56:59], v[32:35]\n"
"ds_read_b128 a[64:67], v193 offset:38912\n"
"v_mfma_f32_16x16x32_bf16 v[64:67], a[116:119], a[56:59], v[64:67]\n"
"v_mfma_f32_16x16x32_bf16 v[68:71], a[116:119], a[60:63], v[68:71]\n"
"v_mfma_f32_16x16x32_bf16 v[36:39], a[112:115], a[60:63], v[36:39]\n"
"v_mfma_f32_16x16x32_bf16 v[72:75], a[68:71], a[32:35], v[72:75]\n"
"buffer_load_dwordx4 a[108:111], v200, s[36:39], s25 offen offset:0 sc1\n"
"v_mfma_f32_16x16x32_bf16 v[76:79], a[68:71], a[36:39], v[76:79]\n"
"s_waitcnt vmcnt(25)\n"
"s_waitcnt lgkmcnt(0)\n"
"v_mfma_f32_16x16x32_bf16 v[8:11], a[120:123], a[0:3], v[8:11]\n"
"ds_read_b128 a[32:35], v188 offset:53248\n"
"v_mfma_f32_16x16x32_bf16 v[40:43], a[124:127], a[0:3], v[40:43]\n"
"ds_read_b128 a[36:39], v188 offset:55296\n"
"v_mfma_f32_16x16x32_bf16 v[44:47], a[124:127], a[4:7], v[44:47]\n"
"buffer_load_dwordx4 v216, s[48:51], s24 offen offset:2048 lds\n"
"v_mfma_f32_16x16x32_bf16 v[12:15], a[120:123], a[4:7], v[12:15]\n"
"ds_read_b128 a[40:43], v189 offset:53248\n"
"v_mfma_f32_16x16x32_bf16 v[16:19], a[120:123], a[8:11], v[16:19]\n"
"ds_read_b128 a[44:47], v189 offset:55296\n"
"v_mfma_f32_16x16x32_bf16 v[48:51], a[124:127], a[8:11], v[48:51]\n"
"v_mfma_f32_16x16x32_bf16 v[52:55], a[124:127], a[12:15], v[52:55]\n"
"ds_read_b128 a[48:51], v190 offset:53248\n"
"v_mfma_f32_16x16x32_bf16 v[20:23], a[120:123], a[12:15], v[20:23]\n"
"ds_read_b128 a[52:55], v190 offset:55296\n"
"v_mfma_f32_16x16x32_bf16 v[24:27], a[120:123], a[16:19], v[24:27]\n"
"buffer_load_dwordx4 a[112:115], v199, s[36:39], s25 offen offset:1024 sc1\n"
"v_mfma_f32_16x16x32_bf16 v[56:59], a[124:127], a[16:19], v[56:59]\n"
"ds_read_b128 a[56:59], v191 offset:53248\n"
"v_mfma_f32_16x16x32_bf16 v[60:63], a[124:127], a[20:23], v[60:63]\n"
"ds_read_b128 a[60:63], v191 offset:55296\n"
"v_mfma_f32_16x16x32_bf16 v[28:31], a[120:123], a[20:23], v[28:31]\n"
"buffer_load_dwordx4 v220, s[48:51], s24 offen offset:3072 lds\n"
"s_add_u32 s24, s24, 128\n"
"v_mfma_f32_16x16x32_bf16 v[32:35], a[120:123], a[24:27], v[32:35]\n"
"ds_read_b128 a[68:71], v193 offset:39936\n"
"v_mfma_f32_16x16x32_bf16 v[64:67], a[124:127], a[24:27], v[64:67]\n"
"v_mfma_f32_16x16x32_bf16 v[68:71], a[124:127], a[28:31], v[68:71]\n"
"s_add_u32 m0, s7, 32768\n"
"s_nop 0\n"
"buffer_load_dwordx4 v194, s[36:39], s26 offen sc1 lds\n"
"s_add_u32 s26, s26, 4096\n"
"v_mfma_f32_16x16x32_bf16 v[36:39], a[120:123], a[28:31], v[36:39]\n"
"v_mfma_f32_16x16x32_bf16 v[72:75], a[64:67], a[0:3], v[72:75]\n"
"buffer_load_dwordx4 a[116:119], v200, s[36:39], s25 offen offset:1024 sc1\n"
"v_mfma_f32_16x16x32_bf16 v[76:79], a[64:67], a[4:7], v[76:79]\n"
"s_waitcnt vmcnt(26)\n"
"s_waitcnt vmcnt(35) lgkmcnt(0)\n"
"s_barrier\n"
"v_mfma_f32_16x16x32_bf16 v[8:11], a[128:131], a[32:35], v[8:11]\n"
"ds_read_b128 a[0:3], v176 offset:0\n"
"v_mfma_f32_16x16x32_bf16 v[40:43], a[132:135], a[32:35], v[40:43]\n"
"ds_read_b128 a[4:7], v176 offset:2048\n"
"v_mfma_f32_16x16x32_bf16 v[44:47], a[132:135], a[36:39], v[44:47]\n"
"s_add_u32 m0, s6, 36864\n"
"s_nop 0\n"
"buffer_load_dwordx4 v208, s[48:51], s24 offen offset:0 lds\n"
"v_mfma_f32_16x16x32_bf16 v[12:15], a[128:131], a[36:39], v[12:15]\n"
"ds_read_b128 a[8:11], v177 offset:0\n"
"v_mfma_f32_16x16x32_bf16 v[16:19], a[128:131], a[40:43], v[16:19]\n"
"ds_read_b128 a[12:15], v177 offset:2048\n"
"v_mfma_f32_16x16x32_bf16 v[48:51], a[132:135], a[40:43], v[48:51]\n"
"v_mfma_f32_16x16x32_bf16 v[52:55], a[132:135], a[44:47], v[52:55]\n"
"ds_read_b128 a[16:19], v178 offset:0\n"
"v_mfma_f32_16x16x32_bf16 v[20:23], a[128:131], a[44:47], v[20:23]\n"
"ds_read_b128 a[20:23], v178 offset:2048\n"
"v_mfma_f32_16x16x32_bf16 v[24:27], a[128:131], a[48:51], v[24:27]\n"
"buffer_load_dwordx4 a[120:123], v199, s[36:39], s25 offen offset:2048 sc1\n"
"v_mfma_f32_16x16x32_bf16 v[56:59], a[132:135], a[48:51], v[56:59]\n"
"ds_read_b128 a[24:27], v179 offset:0\n"
"v_mfma_f32_16x16x32_bf16 v[60:63], a[132:135], a[52:55], v[60:63]\n"
"ds_read_b128 a[28:31], v179 offset:2048\n"
"v_mfma_f32_16x16x32_bf16 v[28:31], a[128:131], a[52:55], v[28:31]\n"
"buffer_load_dwordx4 v212, s[48:51], s24 offen offset:1024 lds\n"
"v_mfma_f32_16x16x32_bf16 v[32:35], a[128:131], a[56:59], v[32:35]\n"
"ds_read_b128 a[64:67], v192 offset:0\n"
"v_mfma_f32_16x16x32_bf16 v[64:67], a[132:135], a[56:59], v[64:67]\n"
"v_mfma_f32_16x16x32_bf16 v[68:71], a[132:135], a[60:63], v[68:71]\n"
"v_mfma_f32_16x16x32_bf16 v[36:39], a[128:131], a[60:63], v[36:39]\n"
"v_mfma_f32_16x16x32_bf16 v[72:75], a[68:71], a[32:35], v[72:75]\n"
"buffer_load_dwordx4 a[124:127], v200, s[36:39], s25 offen offset:2048 sc1\n"
"v_mfma_f32_16x16x32_bf16 v[76:79], a[68:71], a[36:39], v[76:79]\n"
"s_waitcnt vmcnt(26)\n"
"s_waitcnt lgkmcnt(0)\n"
"v_mfma_f32_16x16x32_bf16 v[8:11], a[72:75], a[0:3], v[8:11]\n"
"ds_read_b128 a[32:35], v180 offset:0\n"
"v_mfma_f32_16x16x32_bf16 v[40:43], a[76:79], a[0:3], v[40:43]\n"
"ds_read_b128 a[36:39], v180 offset:2048\n"
"v_mfma_f32_16x16x32_bf16 v[44:47], a[76:79], a[4:7], v[44:47]\n"
"buffer_load_dwordx4 v216, s[48:51], s24 offen offset:2048 lds\n"
"v_mfma_f32_16x16x32_bf16 v[12:15], a[72:75], a[4:7], v[12:15]\n"
"ds_read_b128 a[40:43], v181 offset:0\n"
"v_mfma_f32_16x16x32_bf16 v[16:19], a[72:75], a[8:11], v[16:19]\n"
"ds_read_b128 a[44:47], v181 offset:2048\n"
"v_mfma_f32_16x16x32_bf16 v[48:51], a[76:79], a[8:11], v[48:51]\n"
"v_mfma_f32_16x16x32_bf16 v[52:55], a[76:79], a[12:15], v[52:55]\n"
"ds_read_b128 a[48:51], v182 offset:0\n"
"v_mfma_f32_16x16x32_bf16 v[20:23], a[72:75], a[12:15], v[20:23]\n"
"ds_read_b128 a[52:55], v182 offset:2048\n"
"v_mfma_f32_16x16x32_bf16 v[24:27], a[72:75], a[16:19], v[24:27]\n"
"buffer_load_dwordx4 a[128:131], v199, s[36:39], s25 offen offset:3072 sc1\n"
"v_mfma_f32_16x16x32_bf16 v[56:59], a[76:79], a[16:19], v[56:59]\n"
"ds_read_b128 a[56:59], v183 offset:0\n"
"v_mfma_f32_16x16x32_bf16 v[60:63], a[76:79], a[20:23], v[60:63]\n"
"ds_read_b128 a[60:63], v183 offset:2048\n"
"v_mfma_f32_16x16x32_bf16 v[28:31], a[72:75], a[20:23], v[28:31]\n"
"buffer_load_dwordx4 v220, s[48:51], s24 offen offset:3072 lds\n"
"s_add_u32 s24, s24, 128\n"
"v_mfma_f32_16x16x32_bf16 v[32:35], a[72:75], a[24:27], v[32:35]\n"
"ds_read_b128 a[68:71], v192 offset:1024\n"
"v_mfma_f32_16x16x32_bf16 v[64:67], a[76:79], a[24:27], v[64:67]\n"
"v_mfma_f32_16x16x32_bf16 v[68:71], a[76:79], a[28:31], v[68:71]\n"
"v_mfma_f32_16x16x32_bf16 v[36:39], a[72:75], a[28:31], v[36:39]\n"
"v_mfma_f32_16x16x32_bf16 v[72:75], a[64:67], a[0:3], v[72:75]\n"
"buffer_load_dwordx4 a[132:135], v200, s[36:39], s25 offen offset:3072 sc1\n"
"s_add_u32 s25, s25, 4096\n"
"v_mfma_f32_16x16x32_bf16 v[76:79], a[64:67], a[4:7], v[76:79]\n"
"s_waitcnt vmcnt(25)\n"
"s_waitcnt lgkmcnt(0)\n"
"v_mfma_f32_16x16x32_bf16 v[8:11], a[80:83], a[32:35], v[8:11]\n"
"ds_read_b128 a[0:3], v176 offset:16384\n"
"v_mfma_f32_16x16x32_bf16 v[40:43], a[84:87], a[32:35], v[40:43]\n"
"ds_read_b128 a[4:7], v176 offset:18432\n"
"v_mfma_f32_16x16x32_bf16 v[44:47], a[84:87], a[36:39], v[44:47]\n"
"s_add_u32 m0, s6, 53248\n"
"s_nop 0\n"
"buffer_load_dwordx4 v208, s[48:51], s24 offen offset:0 lds\n"
"v_mfma_f32_16x16x32_bf16 v[12:15], a[80:83], a[36:39], v[12:15]\n"
"ds_read_b128 a[8:11], v177 offset:16384\n"
"v_mfma_f32_16x16x32_bf16 v[16:19], a[80:83], a[40:43], v[16:19]\n"
"ds_read_b128 a[12:15], v177 offset:18432\n"
"v_mfma_f32_16x16x32_bf16 v[48:51], a[84:87], a[40:43], v[48:51]\n"
"v_mfma_f32_16x16x32_bf16 v[52:55], a[84:87], a[44:47], v[52:55]\n"
"ds_read_b128 a[16:19], v178 offset:16384\n"
"v_mfma_f32_16x16x32_bf16 v[20:23], a[80:83], a[44:47], v[20:23]\n"
"ds_read_b128 a[20:23], v178 offset:18432\n"
"v_mfma_f32_16x16x32_bf16 v[24:27], a[80:83], a[48:51], v[24:27]\n"
"buffer_load_dwordx4 a[72:75], v199, s[36:39], s25 offen offset:0 sc1\n"
"v_mfma_f32_16x16x32_bf16 v[56:59], a[84:87], a[48:51], v[56:59]\n"
"ds_read_b128 a[24:27], v179 offset:16384\n"
"v_mfma_f32_16x16x32_bf16 v[60:63], a[84:87], a[52:55], v[60:63]\n"
"ds_read_b128 a[28:31], v179 offset:18432\n"
"v_mfma_f32_16x16x32_bf16 v[28:31], a[80:83], a[52:55], v[28:31]\n"
"buffer_load_dwordx4 v212, s[48:51], s24 offen offset:1024 lds\n"
"v_mfma_f32_16x16x32_bf16 v[32:35], a[80:83], a[56:59], v[32:35]\n"
"ds_read_b128 a[64:67], v192 offset:2048\n"
"v_mfma_f32_16x16x32_bf16 v[64:67], a[84:87], a[56:59], v[64:67]\n"
"v_mfma_f32_16x16x32_bf16 v[68:71], a[84:87], a[60:63], v[68:71]\n"
"v_mfma_f32_16x16x32_bf16 v[36:39], a[80:83], a[60:63], v[36:39]\n"
"v_mfma_f32_16x16x32_bf16 v[72:75], a[68:71], a[32:35], v[72:75]\n"
"buffer_load_dwordx4 a[76:79], v200, s[36:39], s25 offen offset:0 sc1\n"
"v_mfma_f32_16x16x32_bf16 v[76:79], a[68:71], a[36:39], v[76:79]\n"
"s_waitcnt vmcnt(25)\n"
"s_waitcnt lgkmcnt(0)\n"
"v_mfma_f32_16x16x32_bf16 v[8:11], a[88:91], a[0:3], v[8:11]\n"
"ds_read_b128 a[32:35], v180 offset:16384\n"
"v_mfma_f32_16x16x32_bf16 v[40:43], a[92:95], a[0:3], v[40:43]\n"
"ds_read_b128 a[36:39], v180 offset:18432\n"
"v_mfma_f32_16x16x32_bf16 v[44:47], a[92:95], a[4:7], v[44:47]\n"
"buffer_load_dwordx4 v216, s[48:51], s24 offen offset:2048 lds\n"
"v_mfma_f32_16x16x32_bf16 v[12:15], a[88:91], a[4:7], v[12:15]\n"
"ds_read_b128 a[40:43], v181 offset:16384\n"
"v_mfma_f32_16x16x32_bf16 v[16:19], a[88:91], a[8:11], v[16:19]\n"
"ds_read_b128 a[44:47], v181 offset:18432\n"
"v_mfma_f32_16x16x32_bf16 v[48:51], a[92:95], a[8:11], v[48:51]\n"
"v_mfma_f32_16x16x32_bf16 v[52:55], a[92:95], a[12:15], v[52:55]\n"
"ds_read_b128 a[48:51], v182 offset:16384\n"
"v_mfma_f32_16x16x32_bf16 v[20:23], a[88:91], a[12:15], v[20:23]\n"
"ds_read_b128 a[52:55], v182 offset:18432\n"
"v_mfma_f32_16x16x32_bf16 v[24:27], a[88:91], a[16:19], v[24:27]\n"
"buffer_load_dwordx4 a[80:83], v199, s[36:39], s25 offen offset:1024 sc1\n"
"v_mfma_f32_16x16x32_bf16 v[56:59], a[92:95], a[16:19], v[56:59]\n"
"ds_read_b128 a[56:59], v183 offset:16384\n"
"v_mfma_f32_16x16x32_bf16 v[60:63], a[92:95], a[20:23], v[60:63]\n"
"ds_read_b128 a[60:63], v183 offset:18432\n"
"v_mfma_f32_16x16x32_bf16 v[28:31], a[88:91], a[20:23], v[28:31]\n"
"buffer_load_dwordx4 v220, s[48:51], s24 offen offset:3072 lds\n"
"s_add_u32 s24, s24, 128\n"
"v_mfma_f32_16x16x32_bf16 v[32:35], a[88:91], a[24:27], v[32:35]\n"
"ds_read_b128 a[68:71], v192 offset:3072\n"
"v_mfma_f32_16x16x32_bf16 v[64:67], a[92:95], a[24:27], v[64:67]\n"
"v_mfma_f32_16x16x32_bf16 v[68:71], a[92:95], a[28:31], v[68:71]\n"
"s_add_u32 m0, s7, 69632\n"
"s_nop 0\n"
"buffer_load_dwordx4 v194, s[36:39], s26 offen sc1 lds\n"
"s_add_u32 s26, s26, 4096\n"
"v_mfma_f32_16x16x32_bf16 v[36:39], a[88:91], a[28:31], v[36:39]\n"
"v_mfma_f32_16x16x32_bf16 v[72:75], a[64:67], a[0:3], v[72:75]\n"
"buffer_load_dwordx4 a[84:87], v200, s[36:39], s25 offen offset:1024 sc1\n"
"v_mfma_f32_16x16x32_bf16 v[76:79], a[64:67], a[4:7], v[76:79]\n"
"s_waitcnt vmcnt(26)\n"
"s_waitcnt vmcnt(35) lgkmcnt(0)\n"
"s_barrier\n"
"v_mfma_f32_16x16x32_bf16 v[8:11], a[96:99], a[32:35], v[8:11]\n"
"ds_read_b128 a[0:3], v176 offset:36864\n"
"v_mfma_f32_16x16x32_bf16 v[40:43], a[100:103], a[32:35], v[40:43]\n"
"ds_read_b128 a[4:7], v176 offset:38912\n"
"v_mfma_f32_16x16x32_bf16 v[44:47], a[100:103], a[36:39], v[44:47]\n"
"v_mfma_f32_16x16x32_bf16 v[12:15], a[96:99], a[36:39], v[12:15]\n"
"ds_read_b128 a[8:11], v177 offset:36864\n"
"v_mfma_f32_16x16x32_bf16 v[16:19], a[96:99], a[40:43], v[16:19]\n"
"ds_read_b128 a[12:15], v177 offset:38912\n"
"v_mfma_f32_16x16x32_bf16 v[48:51], a[100:103], a[40:43], v[48:51]\n"
"buffer_load_dwordx4 a[176:179], v236, s[36:39], s4 offen offset:0 sc1\n"
"v_mfma_f32_16x16x32_bf16 v[52:55], a[100:103], a[44:47], v[52:55]\n"
"ds_read_b128 a[16:19], v178 offset:36864\n"
"v_mfma_f32_16x16x32_bf16 v[20:23], a[96:99], a[44:47], v[20:23]\n"
"ds_read_b128 a[20:23], v178 offset:38912\n"
"v_mfma_f32_16x16x32_bf16 v[24:27], a[96:99], a[48:51], v[24:27]\n"
"buffer_load_dwordx4 a[88:91], v199, s[36:39], s25 offen offset:2048 sc1\n"
"v_mfma_f32_16x16x32_bf16 v[56:59], a[100:103], a[48:51], v[56:59]\n"
"ds_read_b128 a[24:27], v179 offset:36864\n"
"v_mfma_f32_16x16x32_bf16 v[60:63], a[100:103], a[52:55], v[60:63]\n"
"ds_read_b128 a[28:31], v179 offset:38912\n"
"v_mfma_f32_16x16x32_bf16 v[28:31], a[96:99], a[52:55], v[28:31]\n"
"v_mfma_f32_16x16x32_bf16 v[32:35], a[96:99], a[56:59], v[32:35]\n"
"ds_read_b128 a[64:67], v192 offset:36864\n"
"v_mfma_f32_16x16x32_bf16 v[64:67], a[100:103], a[56:59], v[64:67]\n"
"v_mfma_f32_16x16x32_bf16 v[68:71], a[100:103], a[60:63], v[68:71]\n"
"buffer_load_dwordx4 a[180:183], v236, s[36:39], s4 offen offset:1024 sc1\n"
"v_mfma_f32_16x16x32_bf16 v[36:39], a[96:99], a[60:63], v[36:39]\n"
"v_mfma_f32_16x16x32_bf16 v[72:75], a[68:71], a[32:35], v[72:75]\n"
"buffer_load_dwordx4 a[92:95], v200, s[36:39], s25 offen offset:2048 sc1\n"
"v_mfma_f32_16x16x32_bf16 v[76:79], a[68:71], a[36:39], v[76:79]\n"
"s_waitcnt vmcnt(26)\n"
"s_waitcnt lgkmcnt(0)\n"
"v_mfma_f32_16x16x32_bf16 v[8:11], a[104:107], a[0:3], v[8:11]\n"
"ds_read_b128 a[32:35], v180 offset:36864\n"
"s_lshl_b32 s76, s97, 12\n"
"v_mfma_f32_16x16x32_bf16 v[40:43], a[108:111], a[0:3], v[40:43]\n"
"ds_read_b128 a[36:39], v180 offset:38912\n"
"v_lshlrev_b32 v2, 4, v0\n"
"v_mfma_f32_16x16x32_bf16 v[44:47], a[108:111], a[4:7], v[44:47]\n"
"buffer_load_dwordx4 v[228:231], v2, s[52:55], s76 offen\n"
"v_mfma_f32_16x16x32_bf16 v[12:15], a[104:107], a[4:7], v[12:15]\n"
"ds_read_b128 a[40:43], v181 offset:36864\n"
"v_mfma_f32_16x16x32_bf16 v[16:19], a[104:107], a[8:11], v[16:19]\n"
"ds_read_b128 a[44:47], v181 offset:38912\n"
"v_mfma_f32_16x16x32_bf16 v[48:51], a[108:111], a[8:11], v[48:51]\n"
"v_mfma_f32_16x16x32_bf16 v[52:55], a[108:111], a[12:15], v[52:55]\n"
"ds_read_b128 a[48:51], v182 offset:36864\n"
"v_mfma_f32_16x16x32_bf16 v[20:23], a[104:107], a[12:15], v[20:23]\n"
"ds_read_b128 a[52:55], v182 offset:38912\n"
"v_mfma_f32_16x16x32_bf16 v[24:27], a[104:107], a[16:19], v[24:27]\n"
"buffer_load_dwordx4 a[96:99], v199, s[36:39], s25 offen offset:3072 sc1\n"
"v_mfma_f32_16x16x32_bf16 v[56:59], a[108:111], a[16:19], v[56:59]\n"
"ds_read_b128 a[56:59], v183 offset:36864\n"
"v_mfma_f32_16x16x32_bf16 v[60:63], a[108:111], a[20:23], v[60:63]\n"
"ds_read_b128 a[60:63], v183 offset:38912\n"
"v_mfma_f32_16x16x32_bf16 v[28:31], a[104:107], a[20:23], v[28:31]\n"
"buffer_load_dwordx2 v[232:233], v2, s[52:55], s76 offen offset:16\n"
"v_add_u32 v3, s65, v1\n"
"v_mfma_f32_16x16x32_bf16 v[32:35], a[104:107], a[24:27], v[32:35]\n"
"ds_read_b128 a[68:71], v192 offset:37888\n"
"v_subrev_u32 v3, 63, v3\n"
"v_mfma_f32_16x16x32_bf16 v[64:67], a[108:111], a[24:27], v[64:67]\n"
"v_lshlrev_b32 v2, 1, v3\n"
"v_mfma_f32_16x16x32_bf16 v[68:71], a[108:111], a[28:31], v[68:71]\n"
"buffer_load_dwordx4 a[184:187], v236, s[36:39], s4 offen offset:2048 sc1\n"
"v_mfma_f32_16x16x32_bf16 v[36:39], a[104:107], a[28:31], v[36:39]\n"
"v_cmp_gt_i32 vcc, 0, v3\n"
"v_mov_b32 v3, 2146435072\n"
"s_nop 0\n"
"v_cndmask_b32 v2, v2, v3, vcc\n"
"v_mfma_f32_16x16x32_bf16 v[72:75], a[64:67], a[0:3], v[72:75]\n"
"buffer_load_dwordx4 a[100:103], v200, s[36:39], s25 offen offset:3072 sc1\n"
"s_add_u32 s25, s25, 4096\n"
"v_mfma_f32_16x16x32_bf16 v[76:79], a[64:67], a[4:7], v[76:79]\n"
"s_waitcnt vmcnt(26)\n"
"s_waitcnt lgkmcnt(0)\n"
"v_mfma_f32_16x16x32_bf16 v[8:11], a[112:115], a[32:35], v[8:11]\n"
"ds_read_b128 a[0:3], v176 offset:53248\n"
"buffer_load_ushort v246, v2, s[52:55], s76 offen\n"
"v_mfma_f32_16x16x32_bf16 v[40:43], a[116:119], a[32:35], v[40:43]\n"
"ds_read_b128 a[4:7], v176 offset:55296\n"
"s_lshl_b32 s77, s97, 1\n"
"v_mfma_f32_16x16x32_bf16 v[44:47], a[116:119], a[36:39], v[44:47]\n"
"v_mov_b32 v2, s77\n"
"v_mfma_f32_16x16x32_bf16 v[12:15], a[112:115], a[36:39], v[12:15]\n"
"ds_read_b128 a[8:11], v177 offset:53248\n"
"v_mfma_f32_16x16x32_bf16 v[16:19], a[112:115], a[40:43], v[16:19]\n"
"ds_read_b128 a[12:15], v177 offset:55296\n"
"v_mfma_f32_16x16x32_bf16 v[48:51], a[116:119], a[40:43], v[48:51]\n"
"v_mfma_f32_16x16x32_bf16 v[52:55], a[116:119], a[44:47], v[52:55]\n"
"ds_read_b128 a[16:19], v178 offset:53248\n"
"v_mfma_f32_16x16x32_bf16 v[20:23], a[112:115], a[44:47], v[20:23]\n"
"ds_read_b128 a[20:23], v178 offset:55296\n"
"v_mfma_f32_16x16x32_bf16 v[24:27], a[112:115], a[48:51], v[24:27]\n"
"buffer_load_dwordx4 a[104:107], v199, s[36:39], s25 offen offset:0 sc1\n"
"v_mfma_f32_16x16x32_bf16 v[56:59], a[116:119], a[48:51], v[56:59]\n"
"ds_read_b128 a[24:27], v179 offset:53248\n"
"global_load_ushort v247, v2, s[14:15]\n"
"v_mfma_f32_16x16x32_bf16 v[60:63], a[116:119], a[52:55], v[60:63]\n"
"ds_read_b128 a[28:31], v179 offset:55296\n"
"s_lshl_b32 s71, s97, 12\n"
"v_mfma_f32_16x16x32_bf16 v[28:31], a[112:115], a[52:55], v[28:31]\n"
"s_add_u32 s71, s71, 1048576\n"
"v_mfma_f32_16x16x32_bf16 v[32:35], a[112:115], a[56:59], v[32:35]\n"
"ds_read_b128 a[64:67], v192 offset:38912\n"
"v_mfma_f32_16x16x32_bf16 v[64:67], a[116:119], a[56:59], v[64:67]\n"
"v_mfma_f32_16x16x32_bf16 v[68:71], a[116:119], a[60:63], v[68:71]\n"
"buffer_load_dwordx4 a[188:191], v236, s[36:39], s4 offen offset:3072 sc1\n"
"s_add_u32 s4, s4, 4096\n"
"v_mfma_f32_16x16x32_bf16 v[36:39], a[112:115], a[60:63], v[36:39]\n"
"v_mfma_f32_16x16x32_bf16 v[72:75], a[68:71], a[32:35], v[72:75]\n"
"buffer_load_dwordx4 a[108:111], v200, s[36:39], s25 offen offset:0 sc1\n"
"v_mfma_f32_16x16x32_bf16 v[76:79], a[68:71], a[36:39], v[76:79]\n"
"s_waitcnt vmcnt(27)\n"
"s_waitcnt lgkmcnt(0)\n"
"v_mfma_f32_16x16x32_bf16 v[8:11], a[120:123], a[0:3], v[8:11]\n"
"ds_read_b128 a[32:35], v180 offset:53248\n"
"buffer_load_dwordx4 v[248:251], v171, s[36:39], s71 offen sc1\n"
"v_mfma_f32_16x16x32_bf16 v[40:43], a[124:127], a[0:3], v[40:43]\n"
"ds_read_b128 a[36:39], v180 offset:55296\n"
"v_lshrrev_b32 v2, 2, v1\n"
"v_mfma_f32_16x16x32_bf16 v[44:47], a[124:127], a[4:7], v[44:47]\n"
"v_lshlrev_b32 v2, 7, v2\n"
"v_lshlrev_b32 v3, 4, v1\n"
"v_mfma_f32_16x16x32_bf16 v[12:15], a[120:123], a[4:7], v[12:15]\n"
"ds_read_b128 a[40:43], v181 offset:53248\n"
"s_mul_i32 s74, s65, 18\n"
"v_mfma_f32_16x16x32_bf16 v[16:19], a[120:123], a[8:11], v[16:19]\n"
"ds_read_b128 a[44:47], v181 offset:55296\n"
"s_mul_i32 s71, s99, 288\n"
"v_mfma_f32_16x16x32_bf16 v[48:51], a[124:127], a[8:11], v[48:51]\n"
"s_add_u32 s71, s71, s74\n"
"s_lshl_b32 s71, s71, 10\n"
"v_mfma_f32_16x16x32_bf16 v[52:55], a[124:127], a[12:15], v[52:55]\n"
"ds_read_b128 a[48:51], v182 offset:53248\n"
"v_lshl_add_u32 v241, v1, 4, s71\n"
"v_mfma_f32_16x16x32_bf16 v[20:23], a[120:123], a[12:15], v[20:23]\n"
"ds_read_b128 a[52:55], v182 offset:55296\n"
"s_mul_i32 s71, s47, 73728\n"
"v_mfma_f32_16x16x32_bf16 v[24:27], a[120:123], a[16:19], v[24:27]\n"
"buffer_load_dwordx4 a[112:115], v199, s[36:39], s25 offen offset:1024 sc1\n"
"v_add_u32 v244, s71, v241\n"
"v_mfma_f32_16x16x32_bf16 v[56:59], a[124:127], a[16:19], v[56:59]\n"
"ds_read_b128 a[56:59], v183 offset:53248\n"
"s_mul_i32 s71, s46, 73728\n"
"v_mfma_f32_16x16x32_bf16 v[60:63], a[124:127], a[20:23], v[60:63]\n"
"ds_read_b128 a[60:63], v183 offset:55296\n"
"v_add_u32 v243, s71, v241\n"
"v_mfma_f32_16x16x32_bf16 v[28:31], a[120:123], a[20:23], v[28:31]\n"
"s_mul_i32 s71, s45, 73728\n"
"v_add_u32 v242, s71, v241\n"
"v_mfma_f32_16x16x32_bf16 v[32:35], a[120:123], a[24:27], v[32:35]\n"
"ds_read_b128 a[68:71], v192 offset:39936\n"
"s_mul_i32 s71, s44, 73728\n"
"v_mfma_f32_16x16x32_bf16 v[64:67], a[124:127], a[24:27], v[64:67]\n"
"v_add_u32 v241, s71, v241\n"
"v_add_u32 v164, 49152, v164\n"
"v_mfma_f32_16x16x32_bf16 v[68:71], a[124:127], a[28:31], v[68:71]\n"
"buffer_load_dwordx4 a[192:195], v236, s[36:39], s4 offen offset:0 sc1\n"
"v_add_u32 v170, 49152, v170\n"
"v_mfma_f32_16x16x32_bf16 v[36:39], a[120:123], a[28:31], v[36:39]\n"
"v_add_u32 v165, 32128, v165\n"
"v_add_u32 v168, 32128, v168\n"
"v_mfma_f32_16x16x32_bf16 v[72:75], a[64:67], a[0:3], v[72:75]\n"
"buffer_load_dwordx4 a[116:119], v200, s[36:39], s25 offen offset:1024 sc1\n"
"v_add_u32 v169, 32128, v169\n"
"v_mfma_f32_16x16x32_bf16 v[76:79], a[64:67], a[4:7], v[76:79]\n"
"s_lshr_b32 s72, s67, 1\n"
"s_mul_i32 s72, s72, 9\n"
"s_waitcnt vmcnt(27)\n"
"s_waitcnt vmcnt(36) lgkmcnt(0)\n"
"s_barrier\n"
"v_mfma_f32_16x16x32_bf16 v[8:11], a[128:131], a[32:35], v[8:11]\n"
"ds_read_b128 a[0:3], v184 offset:0\n"
"s_and_b32 s73, s67, 1\n"
"v_mfma_f32_16x16x32_bf16 v[40:43], a[132:135], a[32:35], v[40:43]\n"
"ds_read_b128 a[4:7], v184 offset:2048\n"
"s_lshl_b32 s74, s73, 2\n"
"v_mfma_f32_16x16x32_bf16 v[44:47], a[132:135], a[36:39], v[44:47]\n"
"s_add_u32 s74, s74, s72\n"
"s_add_u32 s74, s74, s65\n"
"v_mfma_f32_16x16x32_bf16 v[12:15], a[128:131], a[36:39], v[12:15]\n"
"ds_read_b128 a[8:11], v185 offset:0\n"
"s_lshl_b32 s74, s74, 10\n"
"v_mfma_f32_16x16x32_bf16 v[16:19], a[128:131], a[40:43], v[16:19]\n"
"ds_read_b128 a[12:15], v185 offset:2048\n"
"s_mul_i32 s75, s66, 589824\n"
"v_mfma_f32_16x16x32_bf16 v[48:51], a[132:135], a[40:43], v[48:51]\n"
"buffer_load_dwordx4 a[196:199], v236, s[36:39], s4 offen offset:1024 sc1\n"
"s_add_u32 s75, s75, 20971520\n"
"v_mfma_f32_16x16x32_bf16 v[52:55], a[132:135], a[44:47], v[52:55]\n"
"ds_read_b128 a[16:19], v186 offset:0\n"
"s_add_u32 s78, s74, s75\n"
"v_mfma_f32_16x16x32_bf16 v[20:23], a[128:131], a[44:47], v[20:23]\n"
"ds_read_b128 a[20:23], v186 offset:2048\n"
"s_add_u32 s72, s72, 8\n"
"v_mfma_f32_16x16x32_bf16 v[24:27], a[128:131], a[48:51], v[24:27]\n"
"buffer_load_dwordx4 a[120:123], v199, s[36:39], s25 offen offset:2048 sc1\n"
"s_lshl_b32 s72, s72, 10\n"
"v_mfma_f32_16x16x32_bf16 v[56:59], a[132:135], a[48:51], v[56:59]\n"
"ds_read_b128 a[24:27], v187 offset:0\n"
"s_add_u32 s72, s72, s75\n"
"v_mfma_f32_16x16x32_bf16 v[60:63], a[132:135], a[52:55], v[60:63]\n"
"ds_read_b128 a[28:31], v187 offset:2048\n"
"s_lshl_b32 s73, s73, 3\n"
"v_mfma_f32_16x16x32_bf16 v[28:31], a[128:131], a[52:55], v[28:31]\n"
"s_add_u32 s72, s72, s73\n"
"s_mul_i32 s73, s65, 147456\n"
"v_mfma_f32_16x16x32_bf16 v[32:35], a[128:131], a[56:59], v[32:35]\n"
"ds_read_b128 a[64:67], v193 offset:0\n"
"s_add_u32 s79, s72, s73\n"
"v_mfma_f32_16x16x32_bf16 v[64:67], a[132:135], a[56:59], v[64:67]\n"
"s_lshl_b32 s71, s66, 4\n"
"s_add_u32 s71, s71, s67\n"
"v_mfma_f32_16x16x32_bf16 v[68:71], a[132:135], a[60:63], v[68:71]\n"
"buffer_load_dwordx4 a[200:203], v236, s[36:39], s4 offen offset:2048 sc1\n"
"s_lshl_b32 s71, s71, 2\n"
"v_mfma_f32_16x16x32_bf16 v[36:39], a[128:131], a[60:63], v[36:39]\n"
"s_add_u32 s71, s71, s65\n"
"s_lshl_b32 s71, s71, 7\n"
"v_mfma_f32_16x16x32_bf16 v[72:75], a[68:71], a[32:35], v[72:75]\n"
"buffer_load_dwordx4 a[124:127], v200, s[36:39], s25 offen offset:2048 sc1\n"
"s_add_u32 s27, s71, 917504\n"
"v_mfma_f32_16x16x32_bf16 v[76:79], a[68:71], a[36:39], v[76:79]\n"
"s_lshr_b32 s75, s99, 1\n"
"s_lshl_b32 s75, s75, 2\n"
"s_waitcnt vmcnt(27)\n"
"s_waitcnt lgkmcnt(0)\n"
"v_mfma_f32_16x16x32_bf16 v[8:11], a[72:75], a[0:3], v[8:11]\n"
"ds_read_b128 a[32:35], v188 offset:0\n"
"s_add_u32 s75, s75, s65\n"
"v_mfma_f32_16x16x32_bf16 v[40:43], a[76:79], a[0:3], v[40:43]\n"
"ds_read_b128 a[36:39], v188 offset:2048\n"
"s_lshl_b32 s75, s75, 11\n"
"v_mfma_f32_16x16x32_bf16 v[44:47], a[76:79], a[4:7], v[44:47]\n"
"buffer_load_dwordx4 a[204:207], v236, s[36:39], s4 offen offset:3072 sc1\n"
"s_add_u32 s4, s4, 4096\n"
"v_mfma_f32_16x16x32_bf16 v[12:15], a[72:75], a[4:7], v[12:15]\n"
"ds_read_b128 a[40:43], v189 offset:0\n"
"s_add_u32 s12, s75, 917504\n"
"v_mfma_f32_16x16x32_bf16 v[16:19], a[72:75], a[8:11], v[16:19]\n"
"ds_read_b128 a[44:47], v189 offset:2048\n"
"v_mfma_f32_16x16x32_bf16 v[48:51], a[76:79], a[8:11], v[48:51]\n"
"v_mfma_f32_16x16x32_bf16 v[52:55], a[76:79], a[12:15], v[52:55]\n"
"ds_read_b128 a[48:51], v190 offset:0\n"
"buffer_load_dwordx4 a[208:211], v236, s[36:39], s4 offen offset:0 sc1\n"
"v_mfma_f32_16x16x32_bf16 v[20:23], a[72:75], a[12:15], v[20:23]\n"
"ds_read_b128 a[52:55], v190 offset:2048\n"
"v_mfma_f32_16x16x32_bf16 v[24:27], a[72:75], a[16:19], v[24:27]\n"
"buffer_load_dwordx4 a[128:131], v199, s[36:39], s25 offen offset:3072 sc1\n"
"v_mfma_f32_16x16x32_bf16 v[56:59], a[76:79], a[16:19], v[56:59]\n"
"ds_read_b128 a[56:59], v191 offset:0\n"
"v_mfma_f32_16x16x32_bf16 v[60:63], a[76:79], a[20:23], v[60:63]\n"
"ds_read_b128 a[60:63], v191 offset:2048\n"
"buffer_load_dwordx4 a[212:215], v236, s[36:39], s4 offen offset:1024 sc1\n"
"v_mfma_f32_16x16x32_bf16 v[28:31], a[72:75], a[20:23], v[28:31]\n"
"v_mfma_f32_16x16x32_bf16 v[32:35], a[72:75], a[24:27], v[32:35]\n"
"ds_read_b128 a[68:71], v193 offset:1024\n"
"v_mfma_f32_16x16x32_bf16 v[64:67], a[76:79], a[24:27], v[64:67]\n"
"v_mfma_f32_16x16x32_bf16 v[68:71], a[76:79], a[28:31], v[68:71]\n"
"buffer_load_dwordx4 a[216:219], v236, s[36:39], s4 offen offset:2048 sc1\n"
"v_mfma_f32_16x16x32_bf16 v[36:39], a[72:75], a[28:31], v[36:39]\n"
"v_mfma_f32_16x16x32_bf16 v[72:75], a[64:67], a[0:3], v[72:75]\n"
"buffer_load_dwordx4 a[132:135], v200, s[36:39], s25 offen offset:3072 sc1\n"
"s_add_u32 s25, s25, 4096\n"
"v_mfma_f32_16x16x32_bf16 v[76:79], a[64:67], a[4:7], v[76:79]\n"
"s_waitcnt vmcnt(28)\n"
"s_waitcnt lgkmcnt(0)\n"
"v_mfma_f32_16x16x32_bf16 v[8:11], a[80:83], a[32:35], v[8:11]\n"
"ds_read_b128 a[0:3], v184 offset:16384\n"
"v_mfma_f32_16x16x32_bf16 v[40:43], a[84:87], a[32:35], v[40:43]\n"
"ds_read_b128 a[4:7], v184 offset:18432\n"
"v_mfma_f32_16x16x32_bf16 v[44:47], a[84:87], a[36:39], v[44:47]\n"
"buffer_load_dwordx4 a[220:223], v236, s[36:39], s4 offen offset:3072 sc1\n"
"s_add_u32 s4, s4, 4096\n"
"v_mfma_f32_16x16x32_bf16 v[12:15], a[80:83], a[36:39], v[12:15]\n"
"ds_read_b128 a[8:11], v185 offset:16384\n"
"v_mfma_f32_16x16x32_bf16 v[16:19], a[80:83], a[40:43], v[16:19]\n"
"ds_read_b128 a[12:15], v185 offset:18432\n"
"v_mfma_f32_16x16x32_bf16 v[48:51], a[84:87], a[40:43], v[48:51]\n"
"v_mfma_f32_16x16x32_bf16 v[52:55], a[84:87], a[44:47], v[52:55]\n"
"ds_read_b128 a[16:19], v186 offset:16384\n"
"buffer_load_dwordx4 a[224:227], v236, s[36:39], s4 offen offset:0 sc1\n"
"v_mfma_f32_16x16x32_bf16 v[20:23], a[80:83], a[44:47], v[20:23]\n"
"ds_read_b128 a[20:23], v186 offset:18432\n"
"v_mfma_f32_16x16x32_bf16 v[24:27], a[80:83], a[48:51], v[24:27]\n"
"v_mfma_f32_16x16x32_bf16 v[56:59], a[84:87], a[48:51], v[56:59]\n"
"ds_read_b128 a[24:27], v187 offset:16384\n"
"v_mfma_f32_16x16x32_bf16 v[60:63], a[84:87], a[52:55], v[60:63]\n"
"ds_read_b128 a[28:31], v187 offset:18432\n"
"buffer_load_dwordx4 a[228:231], v236, s[36:39], s4 offen offset:1024 sc1\n"
"v_mfma_f32_16x16x32_bf16 v[28:31], a[80:83], a[52:55], v[28:31]\n"
"v_mfma_f32_16x16x32_bf16 v[32:35], a[80:83], a[56:59], v[32:35]\n"
"ds_read_b128 a[64:67], v193 offset:2048\n"
"v_mfma_f32_16x16x32_bf16 v[64:67], a[84:87], a[56:59], v[64:67]\n"
"v_mfma_f32_16x16x32_bf16 v[68:71], a[84:87], a[60:63], v[68:71]\n"
"buffer_load_dwordx4 a[232:235], v236, s[36:39], s4 offen offset:2048 sc1\n"
"v_mfma_f32_16x16x32_bf16 v[36:39], a[80:83], a[60:63], v[36:39]\n"
"v_mfma_f32_16x16x32_bf16 v[72:75], a[68:71], a[32:35], v[72:75]\n"
"v_mfma_f32_16x16x32_bf16 v[76:79], a[68:71], a[36:39], v[76:79]\n"
"s_waitcnt vmcnt(28)\n"
"s_waitcnt lgkmcnt(0)\n"
"v_mfma_f32_16x16x32_bf16 v[8:11], a[88:91], a[0:3], v[8:11]\n"
"ds_read_b128 a[32:35], v188 offset:16384\n"
"v_mfma_f32_16x16x32_bf16 v[40:43], a[92:95], a[0:3], v[40:43]\n"
"ds_read_b128 a[36:39], v188 offset:18432\n"
"v_mfma_f32_16x16x32_bf16 v[44:47], a[92:95], a[4:7], v[44:47]\n"
"buffer_load_dwordx4 a[236:239], v236, s[36:39], s4 offen offset:3072 sc1\n"
"s_add_u32 s4, s4, 4096\n"
"v_mfma_f32_16x16x32_bf16 v[12:15], a[88:91], a[4:7], v[12:15]\n"
"ds_read_b128 a[40:43], v189 offset:16384\n"
"v_mfma_f32_16x16x32_bf16 v[16:19], a[88:91], a[8:11], v[16:19]\n"
"ds_read_b128 a[44:47], v189 offset:18432\n"
"v_mfma_f32_16x16x32_bf16 v[48:51], a[92:95], a[8:11], v[48:51]\n"
"v_mfma_f32_16x16x32_bf16 v[52:55], a[92:95], a[12:15], v[52:55]\n"
"ds_read_b128 a[48:51], v190 offset:16384\n"
"buffer_load_dwordx4 a[240:243], v236, s[36:39], s4 offen offset:0 sc1\n"
"v_mfma_f32_16x16x32_bf16 v[20:23], a[88:91], a[12:15], v[20:23]\n"
"ds_read_b128 a[52:55], v190 offset:18432\n"
"v_mfma_f32_16x16x32_bf16 v[24:27], a[88:91], a[16:19], v[24:27]\n"
"v_mfma_f32_16x16x32_bf16 v[56:59], a[92:95], a[16:19], v[56:59]\n"
"ds_read_b128 a[56:59], v191 offset:16384\n"
"v_mfma_f32_16x16x32_bf16 v[60:63], a[92:95], a[20:23], v[60:63]\n"
"ds_read_b128 a[60:63], v191 offset:18432\n"
"buffer_load_dwordx4 a[244:247], v236, s[36:39], s4 offen offset:1024 sc1\n"
"v_mfma_f32_16x16x32_bf16 v[28:31], a[88:91], a[20:23], v[28:31]\n"
"v_mfma_f32_16x16x32_bf16 v[32:35], a[88:91], a[24:27], v[32:35]\n"
"ds_read_b128 a[68:71], v193 offset:3072\n"
"v_mfma_f32_16x16x32_bf16 v[64:67], a[92:95], a[24:27], v[64:67]\n"
"v_mfma_f32_16x16x32_bf16 v[68:71], a[92:95], a[28:31], v[68:71]\n"
"buffer_load_dwordx4 a[248:251], v236, s[36:39], s4 offen offset:2048 sc1\n"
"v_mfma_f32_16x16x32_bf16 v[36:39], a[88:91], a[28:31], v[36:39]\n"
"v_mfma_f32_16x16x32_bf16 v[72:75], a[64:67], a[0:3], v[72:75]\n"
"v_mfma_f32_16x16x32_bf16 v[76:79], a[64:67], a[4:7], v[76:79]\n"
"s_waitcnt vmcnt(27)\n"
"s_waitcnt vmcnt(37) lgkmcnt(0)\n"
"s_barrier\n"
"v_mfma_f32_16x16x32_bf16 v[8:11], a[96:99], a[32:35], v[8:11]\n"
"ds_read_b128 a[0:3], v184 offset:36864\n"
"v_mfma_f32_16x16x32_bf16 v[40:43], a[100:103], a[32:35], v[40:43]\n"
"ds_read_b128 a[4:7], v184 offset:38912\n"
"v_mfma_f32_16x16x32_bf16 v[44:47], a[100:103], a[36:39], v[44:47]\n"
"buffer_load_dwordx4 a[252:255], v236, s[36:39], s4 offen offset:3072 sc1\n"
"s_add_u32 s4, s4, 4096\n"
"v_mfma_f32_16x16x32_bf16 v[12:15], a[96:99], a[36:39], v[12:15]\n"
"ds_read_b128 a[8:11], v185 offset:36864\n"
"v_mfma_f32_16x16x32_bf16 v[16:19], a[96:99], a[40:43], v[16:19]\n"
"ds_read_b128 a[12:15], v185 offset:38912\n"
"v_mfma_f32_16x16x32_bf16 v[48:51], a[100:103], a[40:43], v[48:51]\n"
"v_mfma_f32_16x16x32_bf16 v[52:55], a[100:103], a[44:47], v[52:55]\n"
"ds_read_b128 a[16:19], v186 offset:36864\n"
"buffer_load_dwordx4 a[136:139], v236, s[36:39], s4 offen offset:0 sc1\n"
"v_mfma_f32_16x16x32_bf16 v[20:23], a[96:99], a[44:47], v[20:23]\n"
"ds_read_b128 a[20:23], v186 offset:38912\n"
"v_mfma_f32_16x16x32_bf16 v[24:27], a[96:99], a[48:51], v[24:27]\n"
"v_mfma_f32_16x16x32_bf16 v[56:59], a[100:103], a[48:51], v[56:59]\n"
"ds_read_b128 a[24:27], v187 offset:36864\n"
"v_mfma_f32_16x16x32_bf16 v[60:63], a[100:103], a[52:55], v[60:63]\n"
"ds_read_b128 a[28:31], v187 offset:38912\n"
"buffer_load_dwordx4 a[140:143], v236, s[36:39], s4 offen offset:1024 sc1\n"
"v_mfma_f32_16x16x32_bf16 v[28:31], a[96:99], a[52:55], v[28:31]\n"
"v_mfma_f32_16x16x32_bf16 v[32:35], a[96:99], a[56:59], v[32:35]\n"
"ds_read_b128 a[64:67], v193 offset:36864\n"
"v_mfma_f32_16x16x32_bf16 v[64:67], a[100:103], a[56:59], v[64:67]\n"
"v_mfma_f32_16x16x32_bf16 v[68:71], a[100:103], a[60:63], v[68:71]\n"
"buffer_load_dwordx4 a[144:147], v236, s[36:39], s4 offen offset:2048 sc1\n"
"v_mfma_f32_16x16x32_bf16 v[36:39], a[96:99], a[60:63], v[36:39]\n"
"v_mfma_f32_16x16x32_bf16 v[72:75], a[68:71], a[32:35], v[72:75]\n"
"v_mfma_f32_16x16x32_bf16 v[76:79], a[68:71], a[36:39], v[76:79]\n"
"s_waitcnt vmcnt(26)\n"
"s_waitcnt lgkmcnt(0)\n"
"v_mfma_f32_16x16x32_bf16 v[8:11], a[104:107], a[0:3], v[8:11]\n"
"ds_read_b128 a[32:35], v188 offset:36864\n"
"v_mfma_f32_16x16x32_bf16 v[40:43], a[108:111], a[0:3], v[40:43]\n"
"ds_read_b128 a[36:39], v188 offset:38912\n"
"v_mfma_f32_16x16x32_bf16 v[44:47], a[108:111], a[4:7], v[44:47]\n"
"buffer_load_dwordx4 a[148:151], v236, s[36:39], s4 offen offset:3072 sc1\n"
"s_add_u32 s4, s4, 4096\n"
"v_mfma_f32_16x16x32_bf16 v[12:15], a[104:107], a[4:7], v[12:15]\n"
"ds_read_b128 a[40:43], v189 offset:36864\n"
"v_mfma_f32_16x16x32_bf16 v[16:19], a[104:107], a[8:11], v[16:19]\n"
"ds_read_b128 a[44:47], v189 offset:38912\n"
"v_mfma_f32_16x16x32_bf16 v[48:51], a[108:111], a[8:11], v[48:51]\n"
"v_mfma_f32_16x16x32_bf16 v[52:55], a[108:111], a[12:15], v[52:55]\n"
"ds_read_b128 a[48:51], v190 offset:36864\n"
"buffer_load_dwordx4 a[152:155], v236, s[36:39], s4 offen offset:0 sc1\n"
"v_mfma_f32_16x16x32_bf16 v[20:23], a[104:107], a[12:15], v[20:23]\n"
"ds_read_b128 a[52:55], v190 offset:38912\n"
"v_mfma_f32_16x16x32_bf16 v[24:27], a[104:107], a[16:19], v[24:27]\n"
"v_mfma_f32_16x16x32_bf16 v[56:59], a[108:111], a[16:19], v[56:59]\n"
"ds_read_b128 a[56:59], v191 offset:36864\n"
"v_mfma_f32_16x16x32_bf16 v[60:63], a[108:111], a[20:23], v[60:63]\n"
"ds_read_b128 a[60:63], v191 offset:38912\n"
"buffer_load_dwordx4 a[156:159], v236, s[36:39], s4 offen offset:1024 sc1\n"
"v_mfma_f32_16x16x32_bf16 v[28:31], a[104:107], a[20:23], v[28:31]\n"
"v_mfma_f32_16x16x32_bf16 v[32:35], a[104:107], a[24:27], v[32:35]\n"
"ds_read_b128 a[68:71], v193 offset:37888\n"
"v_mfma_f32_16x16x32_bf16 v[64:67], a[108:111], a[24:27], v[64:67]\n"
"v_mfma_f32_16x16x32_bf16 v[68:71], a[108:111], a[28:31], v[68:71]\n"
"buffer_load_dwordx4 a[160:163], v236, s[36:39], s4 offen offset:2048 sc1\n"
"v_mfma_f32_16x16x32_bf16 v[36:39], a[104:107], a[28:31], v[36:39]\n"
"v_mfma_f32_16x16x32_bf16 v[72:75], a[64:67], a[0:3], v[72:75]\n"
"v_mfma_f32_16x16x32_bf16 v[76:79], a[64:67], a[4:7], v[76:79]\n"
"s_waitcnt vmcnt(26)\n"
"s_waitcnt lgkmcnt(0)\n"
"v_mfma_f32_16x16x32_bf16 v[8:11], a[112:115], a[32:35], v[8:11]\n"
"ds_read_b128 a[0:3], v184 offset:53248\n"
"v_mfma_f32_16x16x32_bf16 v[40:43], a[116:119], a[32:35], v[40:43]\n"
"ds_read_b128 a[4:7], v184 offset:55296\n"
"v_mfma_f32_16x16x32_bf16 v[44:47], a[116:119], a[36:39], v[44:47]\n"
"buffer_load_dwordx4 a[164:167], v236, s[36:39], s4 offen offset:3072 sc1\n"
"s_add_u32 s4, s4, 4096\n"
"v_mfma_f32_16x16x32_bf16 v[12:15], a[112:115], a[36:39], v[12:15]\n"
"ds_read_b128 a[8:11], v185 offset:53248\n"
"v_mfma_f32_16x16x32_bf16 v[16:19], a[112:115], a[40:43], v[16:19]\n"
"ds_read_b128 a[12:15], v185 offset:55296\n"
"v_mfma_f32_16x16x32_bf16 v[48:51], a[116:119], a[40:43], v[48:51]\n"
"v_mfma_f32_16x16x32_bf16 v[52:55], a[116:119], a[44:47], v[52:55]\n"
"ds_read_b128 a[16:19], v186 offset:53248\n"
"buffer_load_dwordx4 a[168:171], v236, s[36:39], s4 offen offset:0 sc1\n"
"v_mfma_f32_16x16x32_bf16 v[20:23], a[112:115], a[44:47], v[20:23]\n"
"ds_read_b128 a[20:23], v186 offset:55296\n"
"v_mfma_f32_16x16x32_bf16 v[24:27], a[112:115], a[48:51], v[24:27]\n"
"v_mfma_f32_16x16x32_bf16 v[56:59], a[116:119], a[48:51], v[56:59]\n"
"ds_read_b128 a[24:27], v187 offset:53248\n"
"v_mfma_f32_16x16x32_bf16 v[60:63], a[116:119], a[52:55], v[60:63]\n"
"ds_read_b128 a[28:31], v187 offset:55296\n"
"buffer_load_dwordx4 a[172:175], v236, s[36:39], s4 offen offset:1024 sc1\n"
"v_mfma_f32_16x16x32_bf16 v[28:31], a[112:115], a[52:55], v[28:31]\n"
"v_mfma_f32_16x16x32_bf16 v[32:35], a[112:115], a[56:59], v[32:35]\n"
"ds_read_b128 a[64:67], v193 offset:38912\n"
"v_mfma_f32_16x16x32_bf16 v[64:67], a[116:119], a[56:59], v[64:67]\n"
"v_mfma_f32_16x16x32_bf16 v[68:71], a[116:119], a[60:63], v[68:71]\n"
"buffer_load_dwordx4 v[152:155], v236, s[36:39], s4 offen offset:2048 sc1\n"
"v_mfma_f32_16x16x32_bf16 v[36:39], a[112:115], a[60:63], v[36:39]\n"
"v_mfma_f32_16x16x32_bf16 v[72:75], a[68:71], a[32:35], v[72:75]\n"
"v_mfma_f32_16x16x32_bf16 v[76:79], a[68:71], a[36:39], v[76:79]\n"
"s_waitcnt vmcnt(26)\n"
"s_waitcnt lgkmcnt(0)\n"
"v_mfma_f32_16x16x32_bf16 v[8:11], a[120:123], a[0:3], v[8:11]\n"
"ds_read_b128 a[32:35], v188 offset:53248\n"
"v_mfma_f32_16x16x32_bf16 v[40:43], a[124:127], a[0:3], v[40:43]\n"
"ds_read_b128 a[36:39], v188 offset:55296\n"
"v_mfma_f32_16x16x32_bf16 v[44:47], a[124:127], a[4:7], v[44:47]\n"
"buffer_load_dwordx4 v[156:159], v236, s[36:39], s4 offen offset:3072 sc1\n"
"s_add_u32 s4, s4, 4096\n"
"v_mfma_f32_16x16x32_bf16 v[12:15], a[120:123], a[4:7], v[12:15]\n"
"ds_read_b128 a[40:43], v189 offset:53248\n"
"v_mfma_f32_16x16x32_bf16 v[16:19], a[120:123], a[8:11], v[16:19]\n"
"ds_read_b128 a[44:47], v189 offset:55296\n"
"v_mfma_f32_16x16x32_bf16 v[48:51], a[124:127], a[8:11], v[48:51]\n"
"v_mfma_f32_16x16x32_bf16 v[52:55], a[124:127], a[12:15], v[52:55]\n"
"ds_read_b128 a[48:51], v190 offset:53248\n"
"buffer_load_dwordx4 v[160:163], v236, s[36:39], s4 offen offset:0 sc1\n"
"v_mfma_f32_16x16x32_bf16 v[20:23], a[120:123], a[12:15], v[20:23]\n"
"ds_read_b128 a[52:55], v190 offset:55296\n"
"v_mfma_f32_16x16x32_bf16 v[24:27], a[120:123], a[16:19], v[24:27]\n"
"v_mfma_f32_16x16x32_bf16 v[56:59], a[124:127], a[16:19], v[56:59]\n"
"ds_read_b128 a[56:59], v191 offset:53248\n"
"v_mfma_f32_16x16x32_bf16 v[60:63], a[124:127], a[20:23], v[60:63]\n"
"ds_read_b128 a[60:63], v191 offset:55296\n"
"buffer_load_dwordx4 v[204:207], v236, s[36:39], s4 offen offset:1024 sc1\n"
"v_mfma_f32_16x16x32_bf16 v[28:31], a[120:123], a[20:23], v[28:31]\n"
"v_mfma_f32_16x16x32_bf16 v[32:35], a[120:123], a[24:27], v[32:35]\n"
"ds_read_b128 a[68:71], v193 offset:39936\n"
"v_mfma_f32_16x16x32_bf16 v[64:67], a[124:127], a[24:27], v[64:67]\n"
"v_mfma_f32_16x16x32_bf16 v[68:71], a[124:127], a[28:31], v[68:71]\n"
"buffer_load_dwordx4 v[208:211], v236, s[36:39], s4 offen offset:2048 sc1\n"
"v_mfma_f32_16x16x32_bf16 v[36:39], a[120:123], a[28:31], v[36:39]\n"
"v_mfma_f32_16x16x32_bf16 v[72:75], a[64:67], a[0:3], v[72:75]\n"
"v_mfma_f32_16x16x32_bf16 v[76:79], a[64:67], a[4:7], v[76:79]\n"
"s_waitcnt vmcnt(24)\n"
"s_waitcnt lgkmcnt(0)\n"
"v_mfma_f32_16x16x32_bf16 v[8:11], a[128:131], a[32:35], v[8:11]\n"
"v_mfma_f32_16x16x32_bf16 v[40:43], a[132:135], a[32:35], v[40:43]\n"
"v_mfma_f32_16x16x32_bf16 v[44:47], a[132:135], a[36:39], v[44:47]\n"
"buffer_load_dwordx4 v[212:215], v236, s[36:39], s4 offen offset:3072 sc1\n"
"s_add_u32 s4, s4, 4096\n"
"v_mfma_f32_16x16x32_bf16 v[12:15], a[128:131], a[36:39], v[12:15]\n"
"v_mfma_f32_16x16x32_bf16 v[16:19], a[128:131], a[40:43], v[16:19]\n"
"v_mfma_f32_16x16x32_bf16 v[48:51], a[132:135], a[40:43], v[48:51]\n"
"v_mfma_f32_16x16x32_bf16 v[52:55], a[132:135], a[44:47], v[52:55]\n"
"buffer_load_dwordx4 v[216:219], v236, s[36:39], s4 offen offset:0 sc1\n"
"v_mfma_f32_16x16x32_bf16 v[20:23], a[128:131], a[44:47], v[20:23]\n"
"v_mfma_f32_16x16x32_bf16 v[24:27], a[128:131], a[48:51], v[24:27]\n"
"v_mfma_f32_16x16x32_bf16 v[56:59], a[132:135], a[48:51], v[56:59]\n"
"v_mfma_f32_16x16x32_bf16 v[60:63], a[132:135], a[52:55], v[60:63]\n"
"buffer_load_dwordx4 v[220:223], v236, s[36:39], s4 offen offset:1024 sc1\n"
"v_mfma_f32_16x16x32_bf16 v[28:31], a[128:131], a[52:55], v[28:31]\n"
"v_mfma_f32_16x16x32_bf16 v[32:35], a[128:131], a[56:59], v[32:35]\n"
"v_mfma_f32_16x16x32_bf16 v[64:67], a[132:135], a[56:59], v[64:67]\n"
"v_mfma_f32_16x16x32_bf16 v[68:71], a[132:135], a[60:63], v[68:71]\n"
"buffer_load_dwordx4 v[224:227], v236, s[36:39], s4 offen offset:2048 sc1\n"
"v_mfma_f32_16x16x32_bf16 v[36:39], a[128:131], a[60:63], v[36:39]\n"
"v_mfma_f32_16x16x32_bf16 v[72:75], a[68:71], a[32:35], v[72:75]\n"
"v_mfma_f32_16x16x32_bf16 v[76:79], a[68:71], a[36:39], v[76:79]\n"
"v_pk_mul_f32 v[8:9], v[8:9], v[80:81]\n"
"v_pk_mul_f32 v[10:11], v[10:11], v[82:83]\n"
"v_pk_mul_f32 v[40:41], v[40:41], v[112:113]\n"
"v_pk_mul_f32 v[42:43], v[42:43], v[114:115]\n"
"v_cvt_pk_bf16_f32 v80, v8, v9\n"
"v_cvt_pk_bf16_f32 v81, v10, v11\n"
"v_cvt_pk_bf16_f32 v82, v40, v41\n"
"v_cvt_pk_bf16_f32 v83, v42, v43\n"
"s_mul_i32 s71, s44, 147456\n"
"s_add_u32 s71, s71, s78\n"
"buffer_store_dwordx4 v[80:83], v3, s[36:39], s71 offen sc1\n"
"v_pk_mul_f32 v[12:13], v[12:13], v[84:85]\n"
"v_pk_mul_f32 v[14:15], v[14:15], v[86:87]\n"
"v_pk_mul_f32 v[44:45], v[44:45], v[116:117]\n"
"v_pk_mul_f32 v[46:47], v[46:47], v[118:119]\n"
"v_cvt_pk_bf16_f32 v84, v12, v13\n"
"v_cvt_pk_bf16_f32 v85, v14, v15\n"
"v_cvt_pk_bf16_f32 v86, v44, v45\n"
"v_cvt_pk_bf16_f32 v87, v46, v47\n"
"s_add_u32 s71, s71, 73728\n"
"buffer_store_dwordx4 v[84:87], v3, s[36:39], s71 offen sc1\n"
"v_pk_mul_f32 v[16:17], v[16:17], v[88:89]\n"
"v_pk_mul_f32 v[18:19], v[18:19], v[90:91]\n"
"v_pk_mul_f32 v[48:49], v[48:49], v[120:121]\n"
"v_pk_mul_f32 v[50:51], v[50:51], v[122:123]\n"
"v_cvt_pk_bf16_f32 v88, v16, v17\n"
"v_cvt_pk_bf16_f32 v89, v18, v19\n"
"v_cvt_pk_bf16_f32 v90, v48, v49\n"
"v_cvt_pk_bf16_f32 v91, v50, v51\n"
"s_mul_i32 s71, s45, 147456\n"
"s_add_u32 s71, s71, s78\n"
"buffer_store_dwordx4 v[88:91], v3, s[36:39], s71 offen sc1\n"
"v_pk_mul_f32 v[20:21], v[20:21], v[92:93]\n"
"v_pk_mul_f32 v[22:23], v[22:23], v[94:95]\n"
"v_pk_mul_f32 v[52:53], v[52:53], v[124:125]\n"
"v_pk_mul_f32 v[54:55], v[54:55], v[126:127]\n"
"v_cvt_pk_bf16_f32 v92, v20, v21\n"
"v_cvt_pk_bf16_f32 v93, v22, v23\n"
"v_cvt_pk_bf16_f32 v94, v52, v53\n"
"v_cvt_pk_bf16_f32 v95, v54, v55\n"
"s_add_u32 s71, s71, 73728\n"
"buffer_store_dwordx4 v[92:95], v3, s[36:39], s71 offen sc1\n"
"v_pk_mul_f32 v[24:25], v[24:25], v[96:97]\n"
"v_pk_mul_f32 v[26:27], v[26:27], v[98:99]\n"
"v_pk_mul_f32 v[56:57], v[56:57], v[128:129]\n"
"v_pk_mul_f32 v[58:59], v[58:59], v[130:131]\n"
"v_cvt_pk_bf16_f32 v96, v24, v25\n"
"v_cvt_pk_bf16_f32 v97, v26, v27\n"
"v_cvt_pk_bf16_f32 v98, v56, v57\n"
"v_cvt_pk_bf16_f32 v99, v58, v59\n"
"s_mul_i32 s71, s46, 147456\n"
"s_add_u32 s71, s71, s78\n"
"buffer_store_dwordx4 v[96:99], v3, s[36:39], s71 offen sc1\n"
"v_pk_mul_f32 v[28:29], v[28:29], v[100:101]\n"
"v_pk_mul_f32 v[30:31], v[30:31], v[102:103]\n"
"v_pk_mul_f32 v[60:61], v[60:61], v[132:133]\n"
"v_pk_mul_f32 v[62:63], v[62:63], v[134:135]\n"
"v_cvt_pk_bf16_f32 v100, v28, v29\n"
"v_cvt_pk_bf16_f32 v101, v30, v31\n"
"v_cvt_pk_bf16_f32 v102, v60, v61\n"
"v_cvt_pk_bf16_f32 v103, v62, v63\n"
"s_add_u32 s71, s71, 73728\n"
"buffer_store_dwordx4 v[100:103], v3, s[36:39], s71 offen sc1\n"
"v_pk_mul_f32 v[32:33], v[32:33], v[104:105]\n"
"v_pk_mul_f32 v[34:35], v[34:35], v[106:107]\n"
"v_pk_mul_f32 v[64:65], v[64:65], v[136:137]\n"
"v_pk_mul_f32 v[66:67], v[66:67], v[138:139]\n"
"v_cvt_pk_bf16_f32 v104, v32, v33\n"
"v_cvt_pk_bf16_f32 v105, v34, v35\n"
"v_cvt_pk_bf16_f32 v106, v64, v65\n"
"v_cvt_pk_bf16_f32 v107, v66, v67\n"
"s_mul_i32 s71, s47, 147456\n"
"s_add_u32 s71, s71, s78\n"
"buffer_store_dwordx4 v[104:107], v3, s[36:39], s71 offen sc1\n"
"v_pk_mul_f32 v[36:37], v[36:37], v[108:109]\n"
"v_pk_mul_f32 v[38:39], v[38:39], v[110:111]\n"
"v_pk_mul_f32 v[68:69], v[68:69], v[140:141]\n"
"v_pk_mul_f32 v[70:71], v[70:71], v[142:143]\n"
"v_cvt_pk_bf16_f32 v108, v36, v37\n"
"v_cvt_pk_bf16_f32 v109, v38, v39\n"
"v_cvt_pk_bf16_f32 v110, v68, v69\n"
"v_cvt_pk_bf16_f32 v111, v70, v71\n"
"s_add_u32 s71, s71, 73728\n"
"buffer_store_dwordx4 v[108:111], v3, s[36:39], s71 offen sc1\n"
"v_pk_mul_f32 v[72:73], v[72:73], v[144:145]\n"
"v_pk_mul_f32 v[74:75], v[74:75], v[146:147]\n"
"v_cvt_pk_bf16_f32 v144, v72, v73\n"
"v_cvt_pk_bf16_f32 v145, v74, v75\n"
"buffer_store_dwordx2 v[144:145], v3, s[36:39], s79 offen sc1\n"
"s_add_u32 s79, s79, 73728\n"
"v_pk_mul_f32 v[76:77], v[76:77], v[148:149]\n"
"v_pk_mul_f32 v[78:79], v[78:79], v[150:151]\n"
"v_cvt_pk_bf16_f32 v148, v76, v77\n"
"v_cvt_pk_bf16_f32 v149, v78, v79\n"
"buffer_store_dwordx2 v[148:149], v3, s[36:39], s79 offen sc1\n"
"v_mov_b32 v48, 0\n"
"v_mov_b32 v49, 0\n"
"v_mov_b32 v50, 0\n"
"v_mov_b32 v51, 0\n"
"ds_write_b128 v169, v[48:51] offset:0\n"
"s_sub_u32 s72, 63, s65\n"
"v_cmp_eq_u32 vcc, s72, v1\n"
"s_lshl_b32 s19, s97, 12\n"
"s_add_u32 s19, s19, 4194304\n"
"s_waitcnt vmcnt(51)\n"
"v_add_u32 v52, 86144, v171\n"
"ds_write_b128 v168, v[248:251] offset:0\n"
"v_lshlrev_b32 v246, 16, v246\n"
"v_lshlrev_b32 v247, 16, v247\n"
"v_cndmask_b32 v247, 0, v247, vcc\n"
"ds_write_b128 v52, v[230:233] offset:12864\n"
"v_alignbit_b32 v60, v229, v228, 16\n"
"v_alignbit_b32 v61, v230, v229, 16\n"
"v_alignbit_b32 v62, v231, v230, 16\n"
"v_alignbit_b32 v63, v232, v231, 16\n"
"v_add_f32 v246, v246, v247\n"
"ds_write_b128 v52, v[60:63] offset:0\n"
"v_alignbit_b32 v64, v230, v229, 16\n"
"v_alignbit_b32 v65, v231, v230, 16\n"
"v_alignbit_b32 v66, v232, v231, 16\n"
"v_alignbit_b32 v67, v233, v232, 16\n"
"v_cvt_pk_bf16_f32 v246, v246, v246\n"
"ds_write_b128 v52, v[64:67] offset:8576\n"
"v_mov_b32 v68, v229\n"
"v_mov_b32 v69, v230\n"
"v_mov_b32 v70, v231\n"
"v_mov_b32 v71, v232\n"
"ds_write_b16 v170, v246 offset:0\n"
"ds_write_b128 v52, v[68:71] offset:4288\n"
"s_waitcnt lgkmcnt(0)\n"
"s_barrier\n"
"ds_read_b128 a[80:83], v165 offset:4064\n"
"ds_read_b64 a[8:9], v164 offset:64\n"
"ds_read_b64 a[10:11], v164 offset:72\n"
"ds_read_b64 a[12:13], v164 offset:96\n"
"ds_read_b64 a[14:15], v164 offset:104\n"
"ds_read_b64 a[16:17], v164 offset:128\n"
"ds_read_b64 a[18:19], v164 offset:136\n"
"ds_read_b64 a[20:21], v164 offset:160\n"
"ds_read_b64 a[22:23], v164 offset:168\n"
"ds_read_b128 a[84:87], v165 offset:4000\n"
"ds_read_b64 a[0:1], v164 offset:0\n"
"ds_read_b64 a[2:3], v164 offset:8\n"
"ds_read_b64 a[4:5], v164 offset:32\n"
"ds_read_b64 a[6:7], v164 offset:40\n"
"s_waitcnt lgkmcnt(7)\n"
"v_mfma_f32_16x16x32_bf16 v[8:11], a[8:11], a[80:83], 0\n"
"ds_read_b128 a[88:91], v165 offset:6624\n"
"ds_read_b128 a[92:95], v165 offset:6560\n"
"v_mfma_f32_16x16x32_bf16 v[12:15], a[12:15], a[80:83], 0\n"
"ds_read_b128 a[96:99], v165 offset:3904\n"
"ds_read_b64 a[24:25], v164 offset:192\n"
"v_mfma_f32_16x16x32_bf16 v[16:19], a[16:19], a[80:83], 0\n"
"ds_read_b64 a[26:27], v164 offset:200\n"
"ds_read_b64 a[28:29], v164 offset:224\n"
"s_waitcnt lgkmcnt(6)\n"
"v_mfma_f32_16x16x32_bf16 v[20:23], a[20:23], a[80:83], 0\n"
"ds_read_b64 a[30:31], v164 offset:232\n"
"ds_read_b64 a[32:33], v164 offset:256\n"
"v_mfma_f32_16x16x32_bf16 v[8:11], a[0:3], a[84:87], v[8:11]\n"
"ds_read_b64 a[34:35], v164 offset:264\n"
"ds_read_b64 a[36:37], v164 offset:288\n"
"v_mfma_f32_16x16x32_bf16 v[12:15], a[4:7], a[84:87], v[12:15]\n"
"ds_read_b64 a[38:39], v164 offset:296\n"
"ds_read_b128 a[100:103], v165 offset:3840\n"
"v_mfma_f32_16x16x32_bf16 v[16:19], a[8:11], a[84:87], v[16:19]\n"
"ds_read_b128 a[104:107], v165 offset:6464\n"
"ds_read_b128 a[108:111], v165 offset:6400\n"
"v_mfma_f32_16x16x32_bf16 v[20:23], a[12:15], a[84:87], v[20:23]\n"
"s_waitcnt lgkmcnt(13)\n"
"v_mfma_f32_16x16x32_bf16 v[24:27], a[8:11], a[88:91], 0\n"
"v_mfma_f32_16x16x32_bf16 v[28:31], a[12:15], a[88:91], 0\n"
"v_mfma_f32_16x16x32_bf16 v[32:35], a[16:19], a[88:91], 0\n"
"v_mfma_f32_16x16x32_bf16 v[36:39], a[20:23], a[88:91], 0\n"
"ds_read_b128 a[88:91], v165 offset:4064\n"
"s_waitcnt lgkmcnt(13)\n"
"v_mfma_f32_16x16x32_bf16 v[24:27], a[0:3], a[92:95], v[24:27]\n"
"ds_read_b64 a[48:49], v164 offset:2112\n"
"v_mfma_f32_16x16x32_bf16 v[28:31], a[4:7], a[92:95], v[28:31]\n"
"v_mfma_f32_16x16x32_bf16 v[32:35], a[8:11], a[92:95], v[32:35]\n"
"v_mfma_f32_16x16x32_bf16 v[36:39], a[12:15], a[92:95], v[36:39]\n"
"s_waitcnt lgkmcnt(7)\n"
"v_mfma_f32_16x16x32_bf16 v[8:11], a[24:27], a[96:99], v[8:11]\n"
"ds_read_b64 a[50:51], v164 offset:2120\n"
"ds_read_b64 a[52:53], v164 offset:2144\n"
"v_mfma_f32_16x16x32_bf16 v[12:15], a[28:31], a[96:99], v[12:15]\n"
"ds_read_b64 a[54:55], v164 offset:2152\n"
"ds_read_b64 a[56:57], v164 offset:2176\n"
"v_mfma_f32_16x16x32_bf16 v[16:19], a[32:35], a[96:99], v[16:19]\n"
"ds_read_b64 a[58:59], v164 offset:2184\n"
"ds_read_b64 a[60:61], v164 offset:2208\n"
"s_waitcnt lgkmcnt(10)\n"
"v_mfma_f32_16x16x32_bf16 v[20:23], a[36:39], a[96:99], v[20:23]\n"
"ds_read_b64 a[62:63], v164 offset:2216\n"
"ds_read_b128 a[92:95], v165 offset:4000\n"
"v_mfma_f32_16x16x32_bf16 v[8:11], a[16:19], a[100:103], v[8:11]\n"
"ds_read_b64 a[40:41], v164 offset:2048\n"
"ds_read_b64 a[42:43], v164 offset:2056\n"
"v_mfma_f32_16x16x32_bf16 v[12:15], a[20:23], a[100:103], v[12:15]\n"
"v_mfma_f32_16x16x32_bf16 v[16:19], a[24:27], a[100:103], v[16:19]\n"
"v_mfma_f32_16x16x32_bf16 v[20:23], a[28:31], a[100:103], v[20:23]\n"
"s_waitcnt lgkmcnt(13)\n"
"v_mfma_f32_16x16x32_bf16 v[24:27], a[24:27], a[104:107], v[24:27]\n"
"ds_read_b64 a[44:45], v164 offset:2080\n"
"v_mfma_f32_16x16x32_bf16 v[28:31], a[28:31], a[104:107], v[28:31]\n"
"v_mfma_f32_16x16x32_bf16 v[32:35], a[32:35], a[104:107], v[32:35]\n"
"v_mfma_f32_16x16x32_bf16 v[36:39], a[36:39], a[104:107], v[36:39]\n"
"s_waitcnt lgkmcnt(13)\n"
"v_mfma_f32_16x16x32_bf16 v[24:27], a[16:19], a[108:111], v[24:27]\n"
"ds_read_b64 a[46:47], v164 offset:2088\n"
"v_mfma_f32_16x16x32_bf16 v[28:31], a[20:23], a[108:111], v[28:31]\n"
"v_mfma_f32_16x16x32_bf16 v[32:35], a[24:27], a[108:111], v[32:35]\n"
"v_mfma_f32_16x16x32_bf16 v[36:39], a[28:31], a[108:111], v[36:39]\n"
"s_waitcnt lgkmcnt(7)\n"
"v_mfma_f32_16x16x32_bf16 v[24:27], a[48:51], a[88:91], v[24:27]\n"
"ds_read_b128 a[104:107], v165 offset:3904\n"
"ds_read_b64 a[64:65], v164 offset:2240\n"
"v_mfma_f32_16x16x32_bf16 v[28:31], a[52:55], a[88:91], v[28:31]\n"
"ds_read_b64 a[66:67], v164 offset:2248\n"
"ds_read_b64 a[68:69], v164 offset:2272\n"
"v_mfma_f32_16x16x32_bf16 v[32:35], a[56:59], a[88:91], v[32:35]\n"
"ds_read_b64 a[70:71], v164 offset:2280\n"
"ds_read_b64 a[72:73], v164 offset:2304\n"
"s_waitcnt lgkmcnt(6)\n"
"v_mfma_f32_16x16x32_bf16 v[36:39], a[60:63], a[88:91], v[36:39]\n"
"ds_read_b64 a[74:75], v164 offset:2312\n"
"ds_read_b64 a[76:77], v164 offset:2336\n"
"v_mfma_f32_16x16x32_bf16 v[24:27], a[40:43], a[92:95], v[24:27]\n"
"ds_read_b64 a[78:79], v164 offset:2344\n"
"ds_read_b128 a[108:111], v165 offset:3840\n"
"v_mfma_f32_16x16x32_bf16 v[28:31], a[44:47], a[92:95], v[28:31]\n"
"ds_read_b128 a[80:83], v165 offset:2784\n"
"ds_read_b64 a[8:9], v164 offset:1088\n"
"v_mfma_f32_16x16x32_bf16 v[32:35], a[48:51], a[92:95], v[32:35]\n"
"ds_read_b64 a[10:11], v164 offset:1096\n"
"ds_read_b64 a[12:13], v164 offset:1120\n"
"v_mfma_f32_16x16x32_bf16 v[36:39], a[52:55], a[92:95], v[36:39]\n"
"s_waitcnt lgkmcnt(7)\n"
"v_mfma_f32_16x16x32_bf16 v[24:27], a[64:67], a[104:107], v[24:27]\n"
"ds_read_b64 a[14:15], v164 offset:1128\n"
"ds_read_b64 a[16:17], v164 offset:1152\n"
"v_mfma_f32_16x16x32_bf16 v[28:31], a[68:71], a[104:107], v[28:31]\n"
"ds_read_b64 a[18:19], v164 offset:1160\n"
"ds_read_b64 a[20:21], v164 offset:1184\n"
"v_mfma_f32_16x16x32_bf16 v[32:35], a[72:75], a[104:107], v[32:35]\n"
"ds_read_b64 a[22:23], v164 offset:1192\n"
"ds_read_b128 a[84:87], v165 offset:2720\n"
"s_waitcnt lgkmcnt(10)\n"
"v_mfma_f32_16x16x32_bf16 v[36:39], a[76:79], a[104:107], v[36:39]\n"
"ds_read_b64 a[0:1], v164 offset:1024\n"
"ds_read_b64 a[2:3], v164 offset:1032\n"
"v_mfma_f32_16x16x32_bf16 v[24:27], a[56:59], a[108:111], v[24:27]\n"
"ds_read_b64 a[4:5], v164 offset:1056\n"
"ds_read_b64 a[6:7], v164 offset:1064\n"
"v_mfma_f32_16x16x32_bf16 v[28:31], a[60:63], a[108:111], v[28:31]\n"
"s_waitcnt vmcnt(0)\n"
"v_mfma_f32_16x16x32_bf16 v[32:35], a[64:67], a[108:111], v[32:35]\n"
"v_mov_b32 v4, s68\n"
"v_mov_b32 v5, s69\n"
"v_min_u32 v6, 1, v1\n"
"v_lshlrev_b32 v6, 30, v6\n"
"buffer_store_dwordx2 v[4:5], v6, s[36:39], s27 offen sc1\n"
"v_mfma_f32_16x16x32_bf16 v[36:39], a[68:71], a[108:111], v[36:39]\n"
"s_waitcnt lgkmcnt(7)\n"
"v_mfma_f32_16x16x32_bf16 v[8:11], a[8:11], a[80:83], v[8:11]\n"
"ds_read_b128 a[88:91], v165 offset:5344\n"
"ds_read_b128 a[92:95], v165 offset:5280\n"
"v_mfma_f32_16x16x32_bf16 v[12:15], a[12:15], a[80:83], v[12:15]\n"
"ds_read_b128 a[96:99], v165 offset:2624\n"
"ds_read_b64 a[24:25], v164 offset:1216\n"
"v_mfma_f32_16x16x32_bf16 v[16:19], a[16:19], a[80:83], v[16:19]\n"
"ds_read_b64 a[26:27], v164 offset:1224\n"
"ds_read_b64 a[28:29], v164 offset:1248\n"
"s_waitcnt lgkmcnt(6)\n"
"v_mfma_f32_16x16x32_bf16 v[20:23], a[20:23], a[80:83], v[20:23]\n"
"ds_read_b64 a[30:31], v164 offset:1256\n"
"ds_read_b64 a[32:33], v164 offset:1280\n"
"v_mfma_f32_16x16x32_bf16 v[8:11], a[0:3], a[84:87], v[8:11]\n"
"ds_read_b64 a[34:35], v164 offset:1288\n"
"ds_read_b64 a[36:37], v164 offset:1312\n"
"v_mfma_f32_16x16x32_bf16 v[12:15], a[4:7], a[84:87], v[12:15]\n"
"ds_read_b64 a[38:39], v164 offset:1320\n"
"ds_read_b128 a[100:103], v165 offset:2560\n"
"v_mfma_f32_16x16x32_bf16 v[16:19], a[8:11], a[84:87], v[16:19]\n"
"ds_read_b128 a[104:107], v165 offset:5184\n"
"ds_read_b128 a[108:111], v165 offset:5120\n"
"v_mfma_f32_16x16x32_bf16 v[20:23], a[12:15], a[84:87], v[20:23]\n"
"buffer_load_dwordx4 v[176:179], v236, s[36:39], s4 offen offset:3072 sc1\n"
"s_add_u32 s4, s4, 4096\n"
"s_waitcnt lgkmcnt(13)\n"
"v_mfma_f32_16x16x32_bf16 v[24:27], a[8:11], a[88:91], v[24:27]\n"
"v_mfma_f32_16x16x32_bf16 v[28:31], a[12:15], a[88:91], v[28:31]\n"
"v_mfma_f32_16x16x32_bf16 v[32:35], a[16:19], a[88:91], v[32:35]\n"
"buffer_load_dwordx4 v[180:183], v236, s[36:39], s4 offen offset:0 sc1\n"
"v_mfma_f32_16x16x32_bf16 v[36:39], a[20:23], a[88:91], v[36:39]\n"
"ds_read_b128 a[88:91], v165 offset:2784\n"
"s_waitcnt lgkmcnt(13)\n"
"v_mfma_f32_16x16x32_bf16 v[24:27], a[0:3], a[92:95], v[24:27]\n"
"ds_read_b64 a[48:49], v164 offset:3136\n"
"v_mfma_f32_16x16x32_bf16 v[28:31], a[4:7], a[92:95], v[28:31]\n"
"buffer_load_dwordx4 v[184:187], v236, s[36:39], s4 offen offset:1024 sc1\n"
"v_mfma_f32_16x16x32_bf16 v[32:35], a[8:11], a[92:95], v[32:35]\n"
"v_mfma_f32_16x16x32_bf16 v[36:39], a[12:15], a[92:95], v[36:39]\n"
"s_waitcnt lgkmcnt(7)\n"
"v_mfma_f32_16x16x32_bf16 v[8:11], a[24:27], a[96:99], v[8:11]\n"
"ds_read_b64 a[50:51], v164 offset:3144\n"
"ds_read_b64 a[52:53], v164 offset:3168\n"
"v_mfma_f32_16x16x32_bf16 v[12:15], a[28:31], a[96:99], v[12:15]\n"
"ds_read_b64 a[54:55], v164 offset:3176\n"
"ds_read_b64 a[56:57], v164 offset:3200\n"
"v_mfma_f32_16x16x32_bf16 v[16:19], a[32:35], a[96:99], v[16:19]\n"
"ds_read_b64 a[58:59], v164 offset:3208\n"
"ds_read_b64 a[60:61], v164 offset:3232\n"
"s_waitcnt lgkmcnt(10)\n"
"v_mfma_f32_16x16x32_bf16 v[20:23], a[36:39], a[96:99], v[20:23]\n"
"ds_read_b64 a[62:63], v164 offset:3240\n"
"ds_read_b128 a[92:95], v165 offset:2720\n"
"v_mfma_f32_16x16x32_bf16 v[8:11], a[16:19], a[100:103], v[8:11]\n"
"ds_read_b64 a[40:41], v164 offset:3072\n"
"ds_read_b64 a[42:43], v164 offset:3080\n"
"v_mfma_f32_16x16x32_bf16 v[12:15], a[20:23], a[100:103], v[12:15]\n"
"buffer_load_dwordx4 v[188:191], v236, s[36:39], s4 offen offset:2048 sc1\n"
"v_mfma_f32_16x16x32_bf16 v[16:19], a[24:27], a[100:103], v[16:19]\n"
"v_mfma_f32_16x16x32_bf16 v[20:23], a[28:31], a[100:103], v[20:23]\n"
"s_waitcnt lgkmcnt(13)\n"
"v_mfma_f32_16x16x32_bf16 v[24:27], a[24:27], a[104:107], v[24:27]\n"
"ds_read_b64 a[44:45], v164 offset:3104\n"
"v_mfma_f32_16x16x32_bf16 v[28:31], a[28:31], a[104:107], v[28:31]\n"
"buffer_load_dwordx4 v[192:195], v236, s[36:39], s4 offen offset:3072 sc1\n"
"s_add_u32 s4, s4, 4096\n"
"v_mfma_f32_16x16x32_bf16 v[32:35], a[32:35], a[104:107], v[32:35]\n"
"v_mfma_f32_16x16x32_bf16 v[36:39], a[36:39], a[104:107], v[36:39]\n"
"s_waitcnt lgkmcnt(13)\n"
"v_mfma_f32_16x16x32_bf16 v[24:27], a[16:19], a[108:111], v[24:27]\n"
"ds_read_b64 a[46:47], v164 offset:3112\n"
"buffer_load_dwordx4 v[196:199], v236, s[36:39], s4 offen offset:0 sc1\n"
"v_mfma_f32_16x16x32_bf16 v[28:31], a[20:23], a[108:111], v[28:31]\n"
"v_mfma_f32_16x16x32_bf16 v[32:35], a[24:27], a[108:111], v[32:35]\n"
"v_mfma_f32_16x16x32_bf16 v[36:39], a[28:31], a[108:111], v[36:39]\n"
"buffer_load_dwordx4 v[200:203], v236, s[36:39], s4 offen offset:1024 sc1\n"
"s_waitcnt lgkmcnt(7)\n"
"v_mfma_f32_16x16x32_bf16 v[24:27], a[48:51], a[88:91], v[24:27]\n"
"ds_read_b128 a[104:107], v165 offset:2624\n"
"ds_read_b64 a[64:65], v164 offset:3264\n"
"v_mfma_f32_16x16x32_bf16 v[28:31], a[52:55], a[88:91], v[28:31]\n"
"ds_read_b64 a[66:67], v164 offset:3272\n"
"ds_read_b64 a[68:69], v164 offset:3296\n"
"v_mfma_f32_16x16x32_bf16 v[32:35], a[56:59], a[88:91], v[32:35]\n"
"ds_read_b64 a[70:71], v164 offset:3304\n"
"ds_read_b64 a[72:73], v164 offset:3328\n"
"s_waitcnt lgkmcnt(6)\n"
"v_mfma_f32_16x16x32_bf16 v[36:39], a[60:63], a[88:91], v[36:39]\n"
"ds_read_b64 a[74:75], v164 offset:3336\n"
"ds_read_b64 a[76:77], v164 offset:3360\n"
"v_mfma_f32_16x16x32_bf16 v[24:27], a[40:43], a[92:95], v[24:27]\n"
"ds_read_b64 a[78:79], v164 offset:3368\n"
"ds_read_b128 a[108:111], v165 offset:2560\n"
"v_mfma_f32_16x16x32_bf16 v[28:31], a[44:47], a[92:95], v[28:31]\n"
"buffer_load_dwordx4 v[112:115], v236, s[36:39], s4 offen offset:2048 sc1\n"
"v_mfma_f32_16x16x32_bf16 v[32:35], a[48:51], a[92:95], v[32:35]\n"
"v_mfma_f32_16x16x32_bf16 v[36:39], a[52:55], a[92:95], v[36:39]\n"
"s_waitcnt lgkmcnt(3)\n"
"v_mfma_f32_16x16x32_bf16 v[24:27], a[64:67], a[104:107], v[24:27]\n"
"buffer_load_dwordx4 v[116:119], v236, s[36:39], s4 offen offset:3072 sc1\n"
"s_add_u32 s4, s4, 4096\n"
"v_mfma_f32_16x16x32_bf16 v[28:31], a[68:71], a[104:107], v[28:31]\n"
"v_mfma_f32_16x16x32_bf16 v[32:35], a[72:75], a[104:107], v[32:35]\n"
"s_waitcnt lgkmcnt(0)\n"
"v_mfma_f32_16x16x32_bf16 v[36:39], a[76:79], a[104:107], v[36:39]\n"
"buffer_load_dwordx4 v[120:123], v236, s[36:39], s4 offen offset:0 sc1\n"
"v_mfma_f32_16x16x32_bf16 v[24:27], a[56:59], a[108:111], v[24:27]\n"
"v_mfma_f32_16x16x32_bf16 v[28:31], a[60:63], a[108:111], v[28:31]\n"
"v_mfma_f32_16x16x32_bf16 v[32:35], a[64:67], a[108:111], v[32:35]\n"
"buffer_load_dwordx4 v[124:127], v236, s[36:39], s4 offen offset:1024 sc1\n"
"v_mfma_f32_16x16x32_bf16 v[36:39], a[68:71], a[108:111], v[36:39]\n"
"buffer_load_dwordx4 v[128:131], v236, s[36:39], s4 offen offset:2048 sc1\n"
"buffer_load_dwordx4 v[132:135], v236, s[36:39], s4 offen offset:3072 sc1\n"
"s_add_u32 s4, s4, 4096\n"
"buffer_load_dwordx4 v[136:139], v236, s[36:39], s4 offen offset:0 sc1\n"
"buffer_load_dwordx4 v[140:143], v236, s[36:39], s4 offen offset:1024 sc1\n"
"buffer_load_dwordx4 v[144:147], v236, s[36:39], s4 offen offset:2048 sc1\n"
"buffer_load_dwordx4 v[148:151], v236, s[36:39], s4 offen offset:3072 sc1\n"
"s_add_u32 s4, s4, 4096\n"
"buffer_load_dwordx4 v[80:83], v236, s[36:39], s4 offen offset:0 sc1\n"
"buffer_load_dwordx4 v[84:87], v236, s[36:39], s4 offen offset:1024 sc1\n"
"buffer_load_dwordx4 v[88:91], v236, s[36:39], s4 offen offset:2048 sc1\n"
"buffer_load_dwordx4 v[92:95], v236, s[36:39], s4 offen offset:3072 sc1\n"
"s_add_u32 s4, s4, 4096\n"
"buffer_load_dwordx4 v[96:99], v236, s[36:39], s4 offen offset:0 sc1\n"
"buffer_load_dwordx4 v[100:103], v236, s[36:39], s4 offen offset:1024 sc1\n"
"buffer_load_dwordx4 v[104:107], v236, s[36:39], s4 offen offset:2048 sc1\n"
"buffer_load_dwordx4 v[108:111], v236, s[36:39], s4 offen offset:3072 sc1\n"
"s_add_u32 s4, s4, 4096\n"
"buffer_load_dwordx2 v[72:73], v2, s[36:39], s12 offen sc1\n"
"s_mov_b32 s5, 20971520\n"
"ds_write_b128 v166, v[8:11] offset:0\n"
"ds_write_b128 v166, v[12:15] offset:64\n"
"ds_write_b128 v166, v[16:19] offset:128\n"
"ds_write_b128 v166, v[20:23] offset:192\n"
"ds_write_b128 v166, v[24:27] offset:4352\n"
"ds_write_b128 v166, v[28:31] offset:4416\n"
"ds_write_b128 v166, v[32:35] offset:4480\n"
"ds_write_b128 v166, v[36:39] offset:4544\n"
"s_add_u32 m0, s41, 4096\n"
"s_nop 0\n"
"buffer_load_dwordx4 v236, s[36:39], s40 offen offset:0 sc1 lds\n"
"s_add_u32 m0, s41, 4096\n"
"s_nop 0\n"
"buffer_load_dwordx4 v236, s[36:39], s40 offen offset:1024 sc1 lds\n"
"s_add_u32 m0, s41, 4096\n"
"s_nop 0\n"
"buffer_load_dwordx4 v236, s[36:39], s40 offen offset:2048 sc1 lds\n"
"s_add_u32 m0, s41, 4096\n"
"s_nop 0\n"
"buffer_load_dwordx4 v236, s[36:39], s40 offen offset:3072 sc1 lds\n"
"s_add_u32 s40, s40, 4096\n"
"s_add_u32 m0, s41, 8192\n"
"s_nop 0\n"
"buffer_load_dwordx4 v236, s[36:39], s40 offen offset:0 sc1 lds\n"
"s_add_u32 m0, s41, 8192\n"
"s_nop 0\n"
"buffer_load_dwordx4 v236, s[36:39], s40 offen offset:1024 sc1 lds\n"
"s_add_u32 m0, s41, 8192\n"
"s_nop 0\n"
"buffer_load_dwordx4 v236, s[36:39], s40 offen offset:2048 sc1 lds\n"
"s_add_u32 m0, s41, 8192\n"
"s_nop 0\n"
"buffer_load_dwordx4 v236, s[36:39], s40 offen offset:3072 sc1 lds\n"
"s_add_u32 s40, s40, 4096\n"
"s_add_u32 m0, s41, 12288\n"
"s_nop 0\n"
"buffer_load_dwordx4 v236, s[36:39], s40 offen offset:0 sc1 lds\n"
"s_add_u32 m0, s42, 3072\n"
"s_nop 0\n"
"buffer_load_dwordx4 v236, s[36:39], s40 offen offset:1024 sc1 lds\n"
"s_add_u32 m0, s42, 3072\n"
"s_nop 0\n"
"buffer_load_dwordx4 v236, s[36:39], s40 offen offset:2048 sc1 lds\n"
"s_add_u32 m0, s42, 3072\n"
"s_nop 0\n"
"buffer_load_dwordx4 v236, s[36:39], s40 offen offset:3072 sc1 lds\n"
"s_add_u32 s40, s40, 4096\n"
"s_add_u32 m0, s42, 7168\n"
"s_nop 0\n"
"buffer_load_dwordx4 v236, s[36:39], s40 offen offset:0 sc1 lds\n"
"s_add_u32 m0, s42, 7168\n"
"s_nop 0\n"
"buffer_load_dwordx4 v236, s[36:39], s40 offen offset:1024 sc1 lds\n"
"s_add_u32 m0, s42, 7168\n"
"s_nop 0\n"
"buffer_load_dwordx4 v236, s[36:39], s40 offen offset:2048 sc1 lds\n"
"s_add_u32 m0, s42, 7168\n"
"s_nop 0\n"
"buffer_load_dwordx4 v236, s[36:39], s40 offen offset:3072 sc1 lds\n"
"s_add_u32 s40, s40, 4096\n"
"s_waitcnt vmcnt(16)\n"
"poll6c_%=:\n"
"v_xor_b32 v75, s68, v72\n"
"v_xor_b32 v76, s69, v73\n"
"v_or_b32 v74, v75, v76\n"
"v_cmp_ne_u32 vcc, 0, v74\n"
"s_cbranch_vccz poll6d_%=\n"
"s_sub_u32 s70, s70, 1\n"
"s_cbranch_scc1 poll6t_%=\n"
"s_sleep 2\n"
"buffer_load_dwordx2 v[72:73], v2, s[36:39], s12 offen sc1\n"
"s_waitcnt vmcnt(0)\n"
"s_branch poll6c_%=\n"
"poll6t_%=:\n"
"s_mov_b32 s70, 0\n"
"poll6d_%=:\n"
"buffer_load_dwordx4 a[0:3], v241, s[36:39], s5 offen offset:0 sc1\n"
"buffer_load_dwordx4 a[4:7], v242, s[36:39], s5 offen offset:0 sc1\n"
"buffer_load_dwordx4 a[8:11], v243, s[36:39], s5 offen offset:0 sc1\n"
"buffer_load_dwordx4 a[12:15], v244, s[36:39], s5 offen offset:0 sc1\n"
"buffer_load_dwordx4 a[16:19], v241, s[36:39], s5 offen offset:1024 sc1\n"
"buffer_load_dwordx4 a[20:23], v242, s[36:39], s5 offen offset:1024 sc1\n"
"buffer_load_dwordx4 a[24:27], v243, s[36:39], s5 offen offset:1024 sc1\n"
"buffer_load_dwordx4 a[28:31], v244, s[36:39], s5 offen offset:1024 sc1\n"
"buffer_load_dwordx4 a[32:35], v241, s[36:39], s5 offen offset:2048 sc1\n"
"buffer_load_dwordx4 a[36:39], v242, s[36:39], s5 offen offset:2048 sc1\n"
"s_waitcnt lgkmcnt(0)\n"
"s_barrier\n"
"ds_read_b128 v[8:11], v167 offset:0\n"
"ds_read_b128 v[12:15], v167 offset:16\n"
"ds_read_b128 v[16:19], v167 offset:8704\n"
"ds_read_b128 v[20:23], v167 offset:8720\n"
"ds_read_b128 v[24:27], v167 offset:17408\n"
"ds_read_b128 v[28:31], v167 offset:17424\n"
"ds_read_b128 v[32:35], v167 offset:26112\n"
"ds_read_b128 v[36:39], v167 offset:26128\n"
"s_waitcnt lgkmcnt(0)\n"
"v_pk_add_f32 v[8:9], v[8:9], v[16:17]\n"
"v_pk_add_f32 v[24:25], v[24:25], v[32:33]\n"
"buffer_load_dwordx4 a[40:43], v243, s[36:39], s5 offen offset:2048 sc1\n"
"v_pk_add_f32 v[10:11], v[10:11], v[18:19]\n"
"v_pk_add_f32 v[26:27], v[26:27], v[34:35]\n"
"buffer_load_dwordx4 a[44:47], v244, s[36:39], s5 offen offset:2048 sc1\n"
"v_pk_add_f32 v[12:13], v[12:13], v[20:21]\n"
"v_pk_add_f32 v[28:29], v[28:29], v[36:37]\n"
"buffer_load_dwordx4 a[48:51], v241, s[36:39], s5 offen offset:3072 sc1\n"
"v_pk_add_f32 v[14:15], v[14:15], v[22:23]\n"
"v_pk_add_f32 v[30:31], v[30:31], v[38:39]\n"
"buffer_load_dwordx4 a[52:55], v242, s[36:39], s5 offen offset:3072 sc1\n"
"v_pk_add_f32 v[8:9], v[8:9], v[24:25]\n"
"v_pk_add_f32 v[10:11], v[10:11], v[26:27]\n"
"v_pk_add_f32 v[12:13], v[12:13], v[28:29]\n"
"v_pk_add_f32 v[14:15], v[14:15], v[30:31]\n"
"v_cvt_pk_bf16_f32 v16, v8, v9\n"
"v_cvt_pk_bf16_f32 v17, v10, v11\n"
"v_cvt_pk_bf16_f32 v18, v12, v13\n"
"v_cvt_pk_bf16_f32 v19, v14, v15\n"
"buffer_store_dwordx4 v[16:19], v171, s[36:39], s19 offen sc1\n"
"buffer_load_dwordx4 a[56:59], v243, s[36:39], s5 offen offset:3072 sc1\n"
"buffer_load_dwordx4 a[60:63], v244, s[36:39], s5 offen offset:3072 sc1\n"
"s_add_u32 s5, s5, 4096\n"
"buffer_load_dwordx4 a[64:67], v241, s[36:39], s5 offen offset:0 sc1\n"
"buffer_load_dwordx4 a[68:71], v242, s[36:39], s5 offen offset:0 sc1\n"
"buffer_load_dwordx4 a[72:75], v243, s[36:39], s5 offen offset:0 sc1\n"
"buffer_load_dwordx4 a[76:79], v244, s[36:39], s5 offen offset:0 sc1\n"
"buffer_load_dwordx4 a[80:83], v241, s[36:39], s5 offen offset:1024 sc1\n"
"buffer_load_dwordx4 a[84:87], v242, s[36:39], s5 offen offset:1024 sc1\n"
"buffer_load_dwordx4 a[88:91], v243, s[36:39], s5 offen offset:1024 sc1\n"
"buffer_load_dwordx4 a[92:95], v244, s[36:39], s5 offen offset:1024 sc1\n"
"s_waitcnt vmcnt(21)\n"
"s_waitcnt vmcnt(63)\n"
"v_mfma_f32_16x16x32_bf16 v[32:35], a[176:179], a[4:7], 0\n"
"v_mfma_f32_16x16x32_bf16 v[56:59], a[176:179], a[8:11], 0\n"
"buffer_load_dwordx4 a[96:99], v241, s[36:39], s5 offen offset:2048 sc1\n"
"v_mfma_f32_16x16x32_bf16 v[60:63], a[180:183], a[8:11], 0\n"
"v_mfma_f32_16x16x32_bf16 v[36:39], a[180:183], a[4:7], 0\n"
"v_mfma_f32_16x16x32_bf16 v[40:43], a[184:187], a[4:7], 0\n"
"v_mfma_f32_16x16x32_bf16 v[64:67], a[184:187], a[8:11], 0\n"
"buffer_load_dwordx4 a[100:103], v242, s[36:39], s5 offen offset:2048 sc1\n"
"v_mfma_f32_16x16x32_bf16 v[68:71], a[188:191], a[8:11], 0\n"
"v_mfma_f32_16x16x32_bf16 v[44:47], a[188:191], a[4:7], 0\n"
"v_mfma_f32_16x16x32_bf16 v[48:51], a[192:195], a[4:7], 0\n"
"v_mfma_f32_16x16x32_bf16 v[72:75], a[192:195], a[8:11], 0\n"
"buffer_load_dwordx4 a[104:107], v243, s[36:39], s5 offen offset:2048 sc1\n"
"v_mfma_f32_16x16x32_bf16 a[112:115], a[176:179], a[12:15], 0\n"
"v_mfma_f32_16x16x32_bf16 v[8:11], a[176:179], a[0:3], 0\n"
"v_mfma_f32_16x16x32_bf16 v[12:15], a[180:183], a[0:3], 0\n"
"v_mfma_f32_16x16x32_bf16 a[116:119], a[180:183], a[12:15], 0\n"
"buffer_load_dwordx4 a[108:111], v244, s[36:39], s5 offen offset:2048 sc1\n"
"v_mfma_f32_16x16x32_bf16 a[120:123], a[184:187], a[12:15], 0\n"
"v_mfma_f32_16x16x32_bf16 v[16:19], a[184:187], a[0:3], 0\n"
"v_mfma_f32_16x16x32_bf16 v[20:23], a[188:191], a[0:3], 0\n"
"v_mfma_f32_16x16x32_bf16 a[124:127], a[188:191], a[12:15], 0\n"
"v_mfma_f32_16x16x32_bf16 a[128:131], a[192:195], a[12:15], 0\n"
"v_mfma_f32_16x16x32_bf16 v[24:27], a[192:195], a[0:3], 0\n"
"s_waitcnt vmcnt(21)\n"
"s_waitcnt vmcnt(63)\n"
"v_mfma_f32_16x16x32_bf16 v[32:35], a[196:199], a[20:23], v[32:35]\n"
"v_mfma_f32_16x16x32_bf16 v[56:59], a[196:199], a[24:27], v[56:59]\n"
"buffer_load_dwordx4 a[0:3], v241, s[36:39], s5 offen offset:3072 sc1\n"
"v_mfma_f32_16x16x32_bf16 v[60:63], a[200:203], a[24:27], v[60:63]\n"
"v_mfma_f32_16x16x32_bf16 v[36:39], a[200:203], a[20:23], v[36:39]\n"
"v_mfma_f32_16x16x32_bf16 v[40:43], a[204:207], a[20:23], v[40:43]\n"
"v_mfma_f32_16x16x32_bf16 v[64:67], a[204:207], a[24:27], v[64:67]\n"
"buffer_load_dwordx4 a[4:7], v242, s[36:39], s5 offen offset:3072 sc1\n"
"v_mfma_f32_16x16x32_bf16 v[68:71], a[208:211], a[24:27], v[68:71]\n"
"v_mfma_f32_16x16x32_bf16 v[44:47], a[208:211], a[20:23], v[44:47]\n"
"v_mfma_f32_16x16x32_bf16 v[52:55], a[192:195], a[20:23], 0\n"
"v_mfma_f32_16x16x32_bf16 v[76:79], a[192:195], a[24:27], 0\n"
"buffer_load_dwordx4 a[8:11], v243, s[36:39], s5 offen offset:3072 sc1\n"
"v_mfma_f32_16x16x32_bf16 a[112:115], a[196:199], a[28:31], a[112:115]\n"
"v_mfma_f32_16x16x32_bf16 v[8:11], a[196:199], a[16:19], v[8:11]\n"
"v_mfma_f32_16x16x32_bf16 v[12:15], a[200:203], a[16:19], v[12:15]\n"
"v_mfma_f32_16x16x32_bf16 a[116:119], a[200:203], a[28:31], a[116:119]\n"
"buffer_load_dwordx4 a[12:15], v244, s[36:39], s5 offen offset:3072 sc1\n"
"s_add_u32 s5, s5, 4096\n"
"v_mfma_f32_16x16x32_bf16 a[120:123], a[204:207], a[28:31], a[120:123]\n"
"v_mfma_f32_16x16x32_bf16 v[16:19], a[204:207], a[16:19], v[16:19]\n"
"v_mfma_f32_16x16x32_bf16 v[20:23], a[208:211], a[16:19], v[20:23]\n"
"v_mfma_f32_16x16x32_bf16 a[124:127], a[208:211], a[28:31], a[124:127]\n"
"v_mfma_f32_16x16x32_bf16 a[132:135], a[192:195], a[28:31], 0\n"
"v_mfma_f32_16x16x32_bf16 v[28:31], a[192:195], a[16:19], 0\n"
"s_waitcnt vmcnt(21)\n"
"s_waitcnt vmcnt(63)\n"
"v_mfma_f32_16x16x32_bf16 v[32:35], a[212:215], a[36:39], v[32:35]\n"
"v_mfma_f32_16x16x32_bf16 v[56:59], a[212:215], a[40:43], v[56:59]\n"
"buffer_load_dwordx4 a[16:19], v241, s[36:39], s5 offen offset:0 sc1\n"
"v_mfma_f32_16x16x32_bf16 v[60:63], a[216:219], a[40:43], v[60:63]\n"
"v_mfma_f32_16x16x32_bf16 v[36:39], a[216:219], a[36:39], v[36:39]\n"
"v_mfma_f32_16x16x32_bf16 v[40:43], a[220:223], a[36:39], v[40:43]\n"
"v_mfma_f32_16x16x32_bf16 v[64:67], a[220:223], a[40:43], v[64:67]\n"
"buffer_load_dwordx4 a[20:23], v242, s[36:39], s5 offen offset:0 sc1\n"
"v_mfma_f32_16x16x32_bf16 v[68:71], a[224:227], a[40:43], v[68:71]\n"
"v_mfma_f32_16x16x32_bf16 v[44:47], a[224:227], a[36:39], v[44:47]\n"
"v_mfma_f32_16x16x32_bf16 v[48:51], a[228:231], a[36:39], v[48:51]\n"
"v_mfma_f32_16x16x32_bf16 v[72:75], a[228:231], a[40:43], v[72:75]\n"
"buffer_load_dwordx4 a[24:27], v243, s[36:39], s5 offen offset:0 sc1\n"
"v_mfma_f32_16x16x32_bf16 a[112:115], a[212:215], a[44:47], a[112:115]\n"
"v_mfma_f32_16x16x32_bf16 v[8:11], a[212:215], a[32:35], v[8:11]\n"
"v_mfma_f32_16x16x32_bf16 v[12:15], a[216:219], a[32:35], v[12:15]\n"
"v_mfma_f32_16x16x32_bf16 a[116:119], a[216:219], a[44:47], a[116:119]\n"
"buffer_load_dwordx4 a[28:31], v244, s[36:39], s5 offen offset:0 sc1\n"
"v_mfma_f32_16x16x32_bf16 a[120:123], a[220:223], a[44:47], a[120:123]\n"
"v_mfma_f32_16x16x32_bf16 v[16:19], a[220:223], a[32:35], v[16:19]\n"
"v_mfma_f32_16x16x32_bf16 v[20:23], a[224:227], a[32:35], v[20:23]\n"
"v_mfma_f32_16x16x32_bf16 a[124:127], a[224:227], a[44:47], a[124:127]\n"
"v_mfma_f32_16x16x32_bf16 a[128:131], a[228:231], a[44:47], a[128:131]\n"
"v_mfma_f32_16x16x32_bf16 v[24:27], a[228:231], a[32:35], v[24:27]\n"
"s_waitcnt vmcnt(20)\n"
"s_waitcnt vmcnt(63)\n"
"v_mfma_f32_16x16x32_bf16 v[32:35], a[232:235], a[52:55], v[32:35]\n"
"s_waitcnt vmcnt(22)\n"
"s_lshl_b32 s71, s97, 2\n"
"v_mfma_f32_16x16x32_bf16 v[56:59], a[232:235], a[56:59], v[56:59]\n"
"buffer_load_dwordx4 a[32:35], v241, s[36:39], s5 offen offset:1024 sc1\n"
"s_add_u32 s71, s71, s65\n"
"v_mfma_f32_16x16x32_bf16 v[60:63], a[236:239], a[56:59], v[60:63]\n"
"s_lshl_b32 s71, s71, 3\n"
"s_add_u32 s71, s71, 53248\n"
"v_mfma_f32_16x16x32_bf16 v[36:39], a[236:239], a[52:55], v[36:39]\n"
"v_mov_b32 v4, s68\n"
"v_mov_b32 v5, s69\n"
"v_mfma_f32_16x16x32_bf16 v[40:43], a[240:243], a[52:55], v[40:43]\n"
"v_min_u32 v6, 1, v1\n"
"v_mfma_f32_16x16x32_bf16 v[64:67], a[240:243], a[56:59], v[64:67]\n"
"buffer_load_dwordx4 a[36:39], v242, s[36:39], s5 offen offset:1024 sc1\n"
"v_lshlrev_b32 v6, 30, v6\n"
"v_mfma_f32_16x16x32_bf16 v[68:71], a[244:247], a[56:59], v[68:71]\n"
"buffer_store_dwordx2 v[4:5], v6, s[36:39], s71 offen sc1\n"
"s_mul_i32 s75, s65, 9216\n"
"v_mfma_f32_16x16x32_bf16 v[44:47], a[244:247], a[52:55], v[44:47]\n"
"v_lshl_add_u32 v238, v1, 4, s75\n"
"s_mul_i32 s75, s65, 7168\n"
"v_mfma_f32_16x16x32_bf16 v[52:55], a[228:231], a[52:55], v[52:55]\n"
"s_add_u32 s75, s75, 132096\n"
"v_lshl_add_u32 v239, v1, 4, s75\n"
"v_mfma_f32_16x16x32_bf16 v[76:79], a[228:231], a[56:59], v[76:79]\n"
"buffer_load_dwordx4 a[40:43], v243, s[36:39], s5 offen offset:1024 sc1\n"
"v_mfma_f32_16x16x32_bf16 a[112:115], a[232:235], a[60:63], a[112:115]\n"
"v_mfma_f32_16x16x32_bf16 v[8:11], a[232:235], a[48:51], v[8:11]\n"
"v_mfma_f32_16x16x32_bf16 v[12:15], a[236:239], a[48:51], v[12:15]\n"
"v_mfma_f32_16x16x32_bf16 a[116:119], a[236:239], a[60:63], a[116:119]\n"
"buffer_load_dwordx4 a[44:47], v244, s[36:39], s5 offen offset:1024 sc1\n"
"v_mfma_f32_16x16x32_bf16 a[120:123], a[240:243], a[60:63], a[120:123]\n"
"v_mfma_f32_16x16x32_bf16 v[16:19], a[240:243], a[48:51], v[16:19]\n"
"v_mfma_f32_16x16x32_bf16 v[20:23], a[244:247], a[48:51], v[20:23]\n"
"v_mfma_f32_16x16x32_bf16 a[124:127], a[244:247], a[60:63], a[124:127]\n"
"v_mfma_f32_16x16x32_bf16 a[132:135], a[228:231], a[60:63], a[132:135]\n"
"v_mfma_f32_16x16x32_bf16 v[28:31], a[228:231], a[48:51], v[28:31]\n"
"s_waitcnt vmcnt(21)\n"
"s_waitcnt vmcnt(63)\n"
"v_mfma_f32_16x16x32_bf16 v[32:35], a[248:251], a[68:71], v[32:35]\n"
"v_mfma_f32_16x16x32_bf16 v[56:59], a[248:251], a[72:75], v[56:59]\n"
"buffer_load_dwordx4 a[48:51], v241, s[36:39], s5 offen offset:2048 sc1\n"
"v_mfma_f32_16x16x32_bf16 v[60:63], a[252:255], a[72:75], v[60:63]\n"
"v_mfma_f32_16x16x32_bf16 v[36:39], a[252:255], a[68:71], v[36:39]\n"
"v_mfma_f32_16x16x32_bf16 v[40:43], a[136:139], a[68:71], v[40:43]\n"
"v_mfma_f32_16x16x32_bf16 v[64:67], a[136:139], a[72:75], v[64:67]\n"
"buffer_load_dwordx4 a[52:55], v242, s[36:39], s5 offen offset:2048 sc1\n"
"v_mfma_f32_16x16x32_bf16 v[68:71], a[140:143], a[72:75], v[68:71]\n"
"v_mfma_f32_16x16x32_bf16 v[44:47], a[140:143], a[68:71], v[44:47]\n"
"v_mfma_f32_16x16x32_bf16 v[48:51], a[144:147], a[68:71], v[48:51]\n"
"v_mfma_f32_16x16x32_bf16 v[72:75], a[144:147], a[72:75], v[72:75]\n"
"buffer_load_dwordx4 a[56:59], v243, s[36:39], s5 offen offset:2048 sc1\n"
"v_mfma_f32_16x16x32_bf16 a[112:115], a[248:251], a[76:79], a[112:115]\n"
"v_mfma_f32_16x16x32_bf16 v[8:11], a[248:251], a[64:67], v[8:11]\n"
"v_mfma_f32_16x16x32_bf16 v[12:15], a[252:255], a[64:67], v[12:15]\n"
"v_mfma_f32_16x16x32_bf16 a[116:119], a[252:255], a[76:79], a[116:119]\n"
"buffer_load_dwordx4 a[60:63], v244, s[36:39], s5 offen offset:2048 sc1\n"
"v_mfma_f32_16x16x32_bf16 a[120:123], a[136:139], a[76:79], a[120:123]\n"
"v_mfma_f32_16x16x32_bf16 v[16:19], a[136:139], a[64:67], v[16:19]\n"
"v_mfma_f32_16x16x32_bf16 v[20:23], a[140:143], a[64:67], v[20:23]\n"
"v_mfma_f32_16x16x32_bf16 a[124:127], a[140:143], a[76:79], a[124:127]\n"
"buffer_load_dwordx4 a[240:243], v236, s[36:39], s40 offen sc1\n"
"v_mfma_f32_16x16x32_bf16 a[128:131], a[144:147], a[76:79], a[128:131]\n"
"v_mfma_f32_16x16x32_bf16 v[24:27], a[144:147], a[64:67], v[24:27]\n"
"s_waitcnt vmcnt(22)\n"
"s_waitcnt vmcnt(63)\n"
"v_mfma_f32_16x16x32_bf16 v[32:35], a[148:151], a[84:87], v[32:35]\n"
"s_lshl_b32 s71, s99, 6\n"
"s_lshl_b32 s72, s65, 4\n"
"v_mfma_f32_16x16x32_bf16 v[56:59], a[148:151], a[88:91], v[56:59]\n"
"buffer_load_dwordx4 a[64:67], v241, s[36:39], s5 offen offset:3072 sc1\n"
"s_add_u32 s71, s71, s72\n"
"v_mfma_f32_16x16x32_bf16 v[60:63], a[152:155], a[88:91], v[60:63]\n"
"v_and_b32 v246, 15, v1\n"
"v_mfma_f32_16x16x32_bf16 v[36:39], a[152:155], a[84:87], v[36:39]\n"
"v_add_u32 v246, s71, v246\n"
"v_mfma_f32_16x16x32_bf16 v[40:43], a[156:159], a[84:87], v[40:43]\n"
"v_lshlrev_b32 v247, 4, v246\n"
"s_mov_b32 s72, 81920\n"
"v_mfma_f32_16x16x32_bf16 v[64:67], a[156:159], a[88:91], v[64:67]\n"
"buffer_load_dwordx4 a[68:71], v242, s[36:39], s5 offen offset:3072 sc1\n"
"buffer_load_dwordx4 v[232:235], v247, s[36:39], s72 offen sc1\n"
"v_mfma_f32_16x16x32_bf16 v[68:71], a[160:163], a[88:91], v[68:71]\n"
"s_mul_i32 s18, s23, 144\n"
"v_mul_u32_u24 v246, 1152, v246\n"
"v_mfma_f32_16x16x32_bf16 v[44:47], a[160:163], a[84:87], v[44:47]\n"
"v_lshrrev_b32 v247, 4, v1\n"
"v_mfma_f32_16x16x32_bf16 v[52:55], a[144:147], a[84:87], v[52:55]\n"
"v_lshl_add_u32 v245, v247, 3, v246\n"
"v_lshl_add_u32 v246, v247, 4, v246\n"
"v_mfma_f32_16x16x32_bf16 v[76:79], a[144:147], a[88:91], v[76:79]\n"
"buffer_load_dwordx4 a[72:75], v243, s[36:39], s5 offen offset:3072 sc1\n"
"v_lshrrev_b32 v247, 5, v1\n"
"v_mfma_f32_16x16x32_bf16 a[112:115], a[148:151], a[92:95], a[112:115]\n"
"v_lshl_or_b32 v245, v247, 30, v245\n"
"buffer_load_dwordx4 v[164:167], v246, s[28:31], s18 offen\n"
"v_mfma_f32_16x16x32_bf16 v[8:11], a[148:151], a[80:83], v[8:11]\n"
"v_mfma_f32_16x16x32_bf16 v[12:15], a[152:155], a[80:83], v[12:15]\n"
"v_mfma_f32_16x16x32_bf16 a[116:119], a[152:155], a[92:95], a[116:119]\n"
"buffer_load_dwordx4 a[76:79], v244, s[36:39], s5 offen offset:3072 sc1\n"
"s_add_u32 s5, s5, 4096\n"
"v_mfma_f32_16x16x32_bf16 a[120:123], a[156:159], a[92:95], a[120:123]\n"
"buffer_load_dwordx4 v[168:171], v246, s[28:31], s18 offen offset:64\n"
"v_mfma_f32_16x16x32_bf16 v[16:19], a[156:159], a[80:83], v[16:19]\n"
"v_mfma_f32_16x16x32_bf16 v[20:23], a[160:163], a[80:83], v[20:23]\n"
"v_mfma_f32_16x16x32_bf16 a[124:127], a[160:163], a[92:95], a[124:127]\n"
"buffer_load_dwordx2 v[172:173], v245, s[28:31], s18 offen offset:128\n"
"v_mfma_f32_16x16x32_bf16 a[132:135], a[144:147], a[92:95], a[132:135]\n"
"v_mfma_f32_16x16x32_bf16 v[28:31], a[144:147], a[80:83], v[28:31]\n"
"s_waitcnt vmcnt(26)\n"
"s_waitcnt vmcnt(63)\n"
"v_mfma_f32_16x16x32_bf16 v[32:35], a[164:167], a[100:103], v[32:35]\n"
"v_mfma_f32_16x16x32_bf16 v[56:59], a[164:167], a[104:107], v[56:59]\n"
"buffer_load_dwordx4 a[80:83], v241, s[36:39], s5 offen offset:0 sc1\n"
"v_mfma_f32_16x16x32_bf16 v[60:63], a[168:171], a[104:107], v[60:63]\n"
"v_mfma_f32_16x16x32_bf16 v[36:39], a[168:171], a[100:103], v[36:39]\n"
"v_mfma_f32_16x16x32_bf16 v[40:43], a[172:175], a[100:103], v[40:43]\n"
"v_mfma_f32_16x16x32_bf16 v[64:67], a[172:175], a[104:107], v[64:67]\n"
"buffer_load_dwordx4 a[84:87], v242, s[36:39], s5 offen offset:0 sc1\n"
"v_mfma_f32_16x16x32_bf16 v[68:71], v[152:155], a[104:107], v[68:71]\n"
"v_mfma_f32_16x16x32_bf16 v[44:47], v[152:155], a[100:103], v[44:47]\n"
"v_mfma_f32_16x16x32_bf16 v[48:51], v[156:159], a[100:103], v[48:51]\n"
"v_mfma_f32_16x16x32_bf16 v[72:75], v[156:159], a[104:107], v[72:75]\n"
"buffer_load_dwordx4 a[88:91], v243, s[36:39], s5 offen offset:0 sc1\n"
"v_mfma_f32_16x16x32_bf16 a[112:115], a[164:167], a[108:111], a[112:115]\n"
"v_mfma_f32_16x16x32_bf16 v[8:11], a[164:167], a[96:99], v[8:11]\n"
"v_mfma_f32_16x16x32_bf16 v[12:15], a[168:171], a[96:99], v[12:15]\n"
"v_mfma_f32_16x16x32_bf16 a[116:119], a[168:171], a[108:111], a[116:119]\n"
"buffer_load_dwordx4 a[92:95], v244, s[36:39], s5 offen offset:0 sc1\n"
"v_mfma_f32_16x16x32_bf16 a[120:123], a[172:175], a[108:111], a[120:123]\n"
"v_mfma_f32_16x16x32_bf16 v[16:19], a[172:175], a[96:99], v[16:19]\n"
"v_mfma_f32_16x16x32_bf16 v[20:23], v[152:155], a[96:99], v[20:23]\n"
"v_mfma_f32_16x16x32_bf16 a[124:127], v[152:155], a[108:111], a[124:127]\n"
"v_mfma_f32_16x16x32_bf16 a[128:131], v[156:159], a[108:111], a[128:131]\n"
"v_mfma_f32_16x16x32_bf16 v[24:27], v[156:159], a[96:99], v[24:27]\n"
"s_waitcnt vmcnt(26)\n"
"s_waitcnt vmcnt(63)\n"
"v_mfma_f32_16x16x32_bf16 v[32:35], v[160:163], a[4:7], v[32:35]\n"
"v_mfma_f32_16x16x32_bf16 v[56:59], v[160:163], a[8:11], v[56:59]\n"
"buffer_load_dwordx4 a[96:99], v241, s[36:39], s5 offen offset:1024 sc1\n"
"v_mfma_f32_16x16x32_bf16 v[60:63], v[204:207], a[8:11], v[60:63]\n"
"v_mfma_f32_16x16x32_bf16 v[36:39], v[204:207], a[4:7], v[36:39]\n"
"v_mfma_f32_16x16x32_bf16 v[40:43], v[208:211], a[4:7], v[40:43]\n"
"v_mfma_f32_16x16x32_bf16 v[64:67], v[208:211], a[8:11], v[64:67]\n"
"buffer_load_dwordx4 a[100:103], v242, s[36:39], s5 offen offset:1024 sc1\n"
"v_mfma_f32_16x16x32_bf16 v[68:71], v[212:215], a[8:11], v[68:71]\n"
"v_mfma_f32_16x16x32_bf16 v[44:47], v[212:215], a[4:7], v[44:47]\n"
"v_mfma_f32_16x16x32_bf16 v[52:55], v[156:159], a[4:7], v[52:55]\n"
"v_mfma_f32_16x16x32_bf16 v[76:79], v[156:159], a[8:11], v[76:79]\n"
"buffer_load_dwordx4 a[104:107], v243, s[36:39], s5 offen offset:1024 sc1\n"
"v_mfma_f32_16x16x32_bf16 a[112:115], v[160:163], a[12:15], a[112:115]\n"
"v_mfma_f32_16x16x32_bf16 v[8:11], v[160:163], a[0:3], v[8:11]\n"
"v_mfma_f32_16x16x32_bf16 v[12:15], v[204:207], a[0:3], v[12:15]\n"
"v_mfma_f32_16x16x32_bf16 a[116:119], v[204:207], a[12:15], a[116:119]\n"
"buffer_load_dwordx4 a[108:111], v244, s[36:39], s5 offen offset:1024 sc1\n"
"v_mfma_f32_16x16x32_bf16 a[120:123], v[208:211], a[12:15], a[120:123]\n"
"v_mfma_f32_16x16x32_bf16 v[16:19], v[208:211], a[0:3], v[16:19]\n"
"v_mfma_f32_16x16x32_bf16 v[20:23], v[212:215], a[0:3], v[20:23]\n"
"v_mfma_f32_16x16x32_bf16 a[124:127], v[212:215], a[12:15], a[124:127]\n"
"v_mfma_f32_16x16x32_bf16 a[132:135], v[156:159], a[12:15], a[132:135]\n"
"v_mfma_f32_16x16x32_bf16 v[28:31], v[156:159], a[0:3], v[28:31]\n"
"s_waitcnt vmcnt(26)\n"
"s_waitcnt vmcnt(63)\n"
"v_mfma_f32_16x16x32_bf16 v[32:35], v[216:219], a[20:23], v[32:35]\n"
"v_mfma_f32_16x16x32_bf16 v[56:59], v[216:219], a[24:27], v[56:59]\n"
"buffer_load_dwordx4 a[0:3], v241, s[36:39], s5 offen offset:2048 sc1\n"
"v_mfma_f32_16x16x32_bf16 v[60:63], v[220:223], a[24:27], v[60:63]\n"
"v_mfma_f32_16x16x32_bf16 v[36:39], v[220:223], a[20:23], v[36:39]\n"
"v_mfma_f32_16x16x32_bf16 v[40:43], v[224:227], a[20:23], v[40:43]\n"
"v_mfma_f32_16x16x32_bf16 v[64:67], v[224:227], a[24:27], v[64:67]\n"
"buffer_load_dwordx4 a[4:7], v242, s[36:39], s5 offen offset:2048 sc1\n"
"v_mfma_f32_16x16x32_bf16 v[68:71], v[176:179], a[24:27], v[68:71]\n"
"v_mfma_f32_16x16x32_bf16 v[44:47], v[176:179], a[20:23], v[44:47]\n"
"v_mfma_f32_16x16x32_bf16 v[48:51], v[180:183], a[20:23], v[48:51]\n"
"v_mfma_f32_16x16x32_bf16 v[72:75], v[180:183], a[24:27], v[72:75]\n"
"buffer_load_dwordx4 a[8:11], v243, s[36:39], s5 offen offset:2048 sc1\n"
"v_mfma_f32_16x16x32_bf16 a[112:115], v[216:219], a[28:31], a[112:115]\n"
"v_mfma_f32_16x16x32_bf16 v[8:11], v[216:219], a[16:19], v[8:11]\n"
"v_mfma_f32_16x16x32_bf16 v[12:15], v[220:223], a[16:19], v[12:15]\n"
"v_mfma_f32_16x16x32_bf16 a[116:119], v[220:223], a[28:31], a[116:119]\n"
"buffer_load_dwordx4 a[12:15], v244, s[36:39], s5 offen offset:2048 sc1\n"
"v_mfma_f32_16x16x32_bf16 a[120:123], v[224:227], a[28:31], a[120:123]\n"
"v_mfma_f32_16x16x32_bf16 v[16:19], v[224:227], a[16:19], v[16:19]\n"
"v_mfma_f32_16x16x32_bf16 v[20:23], v[176:179], a[16:19], v[20:23]\n"
"v_mfma_f32_16x16x32_bf16 a[124:127], v[176:179], a[28:31], a[124:127]\n"
"v_mfma_f32_16x16x32_bf16 a[128:131], v[180:183], a[28:31], a[128:131]\n"
"v_mfma_f32_16x16x32_bf16 v[24:27], v[180:183], a[16:19], v[24:27]\n"
"s_waitcnt vmcnt(25)\n"
"s_waitcnt vmcnt(63)\n"
"v_mfma_f32_16x16x32_bf16 v[32:35], v[184:187], a[36:39], v[32:35]\n"
"s_mul_i32 s72, s23, 2304\n"
"s_add_u32 s17, s72, 53248\n"
"v_mfma_f32_16x16x32_bf16 v[56:59], v[184:187], a[40:43], v[56:59]\n"
"buffer_load_dwordx4 a[16:19], v241, s[36:39], s5 offen offset:3072 sc1\n"
"v_lshlrev_b32 v2, 4, v1\n"
"v_mfma_f32_16x16x32_bf16 v[60:63], v[188:191], a[40:43], v[60:63]\n"
"v_min_u32 v3, 15, v1\n"
"v_mfma_f32_16x16x32_bf16 v[36:39], v[188:191], a[36:39], v[36:39]\n"
"v_lshlrev_b32 v3, 4, v3\n"
"buffer_load_dwordx4 v[248:251], v2, s[36:39], s17 offen sc1\n"
"v_mfma_f32_16x16x32_bf16 v[40:43], v[192:195], a[36:39], v[40:43]\n"
"v_mfma_f32_16x16x32_bf16 v[64:67], v[192:195], a[40:43], v[64:67]\n"
"buffer_load_dwordx4 a[20:23], v242, s[36:39], s5 offen offset:3072 sc1\n"
"v_mfma_f32_16x16x32_bf16 v[68:71], v[196:199], a[40:43], v[68:71]\n"
"buffer_load_dwordx4 v[252:255], v2, s[36:39], s17 offen offset:1024 sc1\n"
"v_mfma_f32_16x16x32_bf16 v[44:47], v[196:199], a[36:39], v[44:47]\n"
"v_mfma_f32_16x16x32_bf16 v[52:55], v[180:183], a[36:39], v[52:55]\n"
"v_mfma_f32_16x16x32_bf16 v[76:79], v[180:183], a[40:43], v[76:79]\n"
"buffer_load_dwordx4 a[24:27], v243, s[36:39], s5 offen offset:3072 sc1\n"
"buffer_load_dwordx4 v[4:7], v3, s[36:39], s17 offen offset:2048 sc1\n"
"v_mfma_f32_16x16x32_bf16 a[112:115], v[184:187], a[44:47], a[112:115]\n"
"v_mfma_f32_16x16x32_bf16 v[8:11], v[184:187], a[32:35], v[8:11]\n"
"v_mfma_f32_16x16x32_bf16 v[12:15], v[188:191], a[32:35], v[12:15]\n"
"v_mfma_f32_16x16x32_bf16 a[116:119], v[188:191], a[44:47], a[116:119]\n"
"buffer_load_dwordx4 a[28:31], v244, s[36:39], s5 offen offset:3072 sc1\n"
"s_add_u32 s5, s5, 4096\n"
"v_mfma_f32_16x16x32_bf16 a[120:123], v[192:195], a[44:47], a[120:123]\n"
"v_mfma_f32_16x16x32_bf16 v[16:19], v[192:195], a[32:35], v[16:19]\n"
"v_mfma_f32_16x16x32_bf16 v[20:23], v[196:199], a[32:35], v[20:23]\n"
"v_mfma_f32_16x16x32_bf16 a[124:127], v[196:199], a[44:47], a[124:127]\n"
"v_mfma_f32_16x16x32_bf16 a[132:135], v[180:183], a[44:47], a[132:135]\n"
"v_mfma_f32_16x16x32_bf16 v[28:31], v[180:183], a[32:35], v[28:31]\n"
"s_waitcnt vmcnt(28)\n"
"s_waitcnt vmcnt(63)\n"
"v_mfma_f32_16x16x32_bf16 v[32:35], v[200:203], a[52:55], v[32:35]\n"
"v_mfma_f32_16x16x32_bf16 v[56:59], v[200:203], a[56:59], v[56:59]\n"
"buffer_load_dwordx4 a[32:35], v241, s[36:39], s5 offen offset:0 sc1\n"
"v_mfma_f32_16x16x32_bf16 v[60:63], v[112:115], a[56:59], v[60:63]\n"
"v_mfma_f32_16x16x32_bf16 v[36:39], v[112:115], a[52:55], v[36:39]\n"
"v_mfma_f32_16x16x32_bf16 v[40:43], v[116:119], a[52:55], v[40:43]\n"
"v_mfma_f32_16x16x32_bf16 v[64:67], v[116:119], a[56:59], v[64:67]\n"
"buffer_load_dwordx4 a[36:39], v242, s[36:39], s5 offen offset:0 sc1\n"
"v_mfma_f32_16x16x32_bf16 v[68:71], v[120:123], a[56:59], v[68:71]\n"
"v_mfma_f32_16x16x32_bf16 v[44:47], v[120:123], a[52:55], v[44:47]\n"
"v_mfma_f32_16x16x32_bf16 v[48:51], v[124:127], a[52:55], v[48:51]\n"
"v_mfma_f32_16x16x32_bf16 v[72:75], v[124:127], a[56:59], v[72:75]\n"
"buffer_load_dwordx4 a[40:43], v243, s[36:39], s5 offen offset:0 sc1\n"
"v_mfma_f32_16x16x32_bf16 a[112:115], v[200:203], a[60:63], a[112:115]\n"
"v_mfma_f32_16x16x32_bf16 v[8:11], v[200:203], a[48:51], v[8:11]\n"
"v_mfma_f32_16x16x32_bf16 v[12:15], v[112:115], a[48:51], v[12:15]\n"
"v_mfma_f32_16x16x32_bf16 a[116:119], v[112:115], a[60:63], a[116:119]\n"
"buffer_load_dwordx4 a[44:47], v244, s[36:39], s5 offen offset:0 sc1\n"
"v_mfma_f32_16x16x32_bf16 a[120:123], v[116:119], a[60:63], a[120:123]\n"
"v_mfma_f32_16x16x32_bf16 v[16:19], v[116:119], a[48:51], v[16:19]\n"
"v_mfma_f32_16x16x32_bf16 v[20:23], v[120:123], a[48:51], v[20:23]\n"
"v_mfma_f32_16x16x32_bf16 a[124:127], v[120:123], a[60:63], a[124:127]\n"
"v_mfma_f32_16x16x32_bf16 a[128:131], v[124:127], a[60:63], a[128:131]\n"
"v_mfma_f32_16x16x32_bf16 v[24:27], v[124:127], a[48:51], v[24:27]\n"
"s_waitcnt vmcnt(25)\n"
"s_waitcnt vmcnt(63)\n"
"v_mfma_f32_16x16x32_bf16 v[32:35], v[128:131], a[68:71], v[32:35]\n"
"v_mfma_f32_16x16x32_bf16 v[56:59], v[128:131], a[72:75], v[56:59]\n"
"buffer_load_dwordx4 a[48:51], v241, s[36:39], s5 offen offset:1024 sc1\n"
"v_mfma_f32_16x16x32_bf16 v[60:63], v[132:135], a[72:75], v[60:63]\n"
"v_mfma_f32_16x16x32_bf16 v[36:39], v[132:135], a[68:71], v[36:39]\n"
"v_mfma_f32_16x16x32_bf16 v[40:43], v[136:139], a[68:71], v[40:43]\n"
"v_mfma_f32_16x16x32_bf16 v[64:67], v[136:139], a[72:75], v[64:67]\n"
"buffer_load_dwordx4 a[52:55], v242, s[36:39], s5 offen offset:1024 sc1\n"
"v_mfma_f32_16x16x32_bf16 v[68:71], v[140:143], a[72:75], v[68:71]\n"
"v_mfma_f32_16x16x32_bf16 v[44:47], v[140:143], a[68:71], v[44:47]\n"
"v_mfma_f32_16x16x32_bf16 v[52:55], v[124:127], a[68:71], v[52:55]\n"
"v_mfma_f32_16x16x32_bf16 v[76:79], v[124:127], a[72:75], v[76:79]\n"
"buffer_load_dwordx4 a[56:59], v243, s[36:39], s5 offen offset:1024 sc1\n"
"v_mfma_f32_16x16x32_bf16 a[112:115], v[128:131], a[76:79], a[112:115]\n"
"v_mfma_f32_16x16x32_bf16 v[8:11], v[128:131], a[64:67], v[8:11]\n"
"v_mfma_f32_16x16x32_bf16 v[12:15], v[132:135], a[64:67], v[12:15]\n"
"v_mfma_f32_16x16x32_bf16 a[116:119], v[132:135], a[76:79], a[116:119]\n"
"buffer_load_dwordx4 a[60:63], v244, s[36:39], s5 offen offset:1024 sc1\n"
"v_mfma_f32_16x16x32_bf16 a[120:123], v[136:139], a[76:79], a[120:123]\n"
"v_mfma_f32_16x16x32_bf16 v[16:19], v[136:139], a[64:67], v[16:19]\n"
"v_mfma_f32_16x16x32_bf16 v[20:23], v[140:143], a[64:67], v[20:23]\n"
"v_mfma_f32_16x16x32_bf16 a[124:127], v[140:143], a[76:79], a[124:127]\n"
"v_mfma_f32_16x16x32_bf16 a[132:135], v[124:127], a[76:79], a[132:135]\n"
"v_mfma_f32_16x16x32_bf16 v[28:31], v[124:127], a[64:67], v[28:31]\n"
"s_waitcnt vmcnt(9)\n"
"poll7c_%=:\n"
"v_xor_b32 v153, s68, v248\n"
"v_xor_b32 v154, s69, v249\n"
"v_or_b32 v152, v153, v154\n"
"v_xor_b32 v153, s68, v250\n"
"v_xor_b32 v154, s69, v251\n"
"v_or3_b32 v152, v152, v153, v154\n"
"v_xor_b32 v153, s68, v252\n"
"v_xor_b32 v154, s69, v253\n"
"v_or3_b32 v152, v152, v153, v154\n"
"v_xor_b32 v153, s68, v254\n"
"v_xor_b32 v154, s69, v255\n"
"v_or3_b32 v152, v152, v153, v154\n"
"v_xor_b32 v153, s68, v4\n"
"v_xor_b32 v154, s69, v5\n"
"v_or3_b32 v152, v152, v153, v154\n"
"v_xor_b32 v153, s68, v6\n"
"v_xor_b32 v154, s69, v7\n"
"v_or3_b32 v152, v152, v153, v154\n"
"v_cmp_ne_u32 vcc, 0, v152\n"
"s_cbranch_vccz poll7d_%=\n"
"s_sub_u32 s70, s70, 1\n"
"s_cbranch_scc1 poll7t_%=\n"
"s_sleep 2\n"
"buffer_load_dwordx4 v[248:251], v2, s[36:39], s17 offen sc1\n"
"buffer_load_dwordx4 v[252:255], v2, s[36:39], s17 offen offset:1024 sc1\n"
"buffer_load_dwordx4 v[4:7], v3, s[36:39], s17 offen offset:2048 sc1\n"
"s_waitcnt vmcnt(0)\n"
"s_branch poll7c_%=\n"
"poll7t_%=:\n"
"s_mov_b32 s70, 0\n"
"poll7d_%=:\n"
"s_waitcnt vmcnt(23)\n"
"s_waitcnt vmcnt(63)\n"
"v_mfma_f32_16x16x32_bf16 v[32:35], v[144:147], a[84:87], v[32:35]\n"
"s_mul_i32 s71, s23, 72\n"
"s_lshl_b32 s71, s71, 12\n"
"v_mfma_f32_16x16x32_bf16 v[56:59], v[144:147], a[88:91], v[56:59]\n"
"s_lshl_b32 s72, s99, 7\n"
"s_add_u32 s71, s71, s72\n"
"v_mfma_f32_16x16x32_bf16 v[60:63], v[148:151], a[88:91], v[60:63]\n"
"s_add_u32 s71, s71, 4194304\n"
"v_lshrrev_b32 v2, 3, v1\n"
"v_mfma_f32_16x16x32_bf16 v[36:39], v[148:151], a[84:87], v[36:39]\n"
"v_lshlrev_b32 v2, 12, v2\n"
"v_and_b32 v3, 7, v1\n"
"v_mfma_f32_16x16x32_bf16 v[40:43], v[80:83], a[84:87], v[40:43]\n"
"v_lshl_add_u32 v2, v3, 4, v2\n"
"s_lshl_b32 s72, s65, 15\n"
"v_mfma_f32_16x16x32_bf16 v[64:67], v[80:83], a[88:91], v[64:67]\n"
"s_add_u32 s72, s72, s71\n"
"s_lshl_b32 s73, s65, 10\n"
"v_mfma_f32_16x16x32_bf16 v[68:71], v[84:87], a[88:91], v[68:71]\n"
"s_add_u32 m0, s73, 110592\n"
"s_nop 0\n"
"buffer_load_dwordx4 v2, s[36:39], s72 offen sc1 lds\n"
"v_mfma_f32_16x16x32_bf16 v[44:47], v[84:87], a[84:87], v[44:47]\n"
"s_add_u32 s72, s72, 131072\n"
"v_mfma_f32_16x16x32_bf16 v[48:51], v[88:91], a[84:87], v[48:51]\n"
"v_mfma_f32_16x16x32_bf16 v[72:75], v[88:91], a[88:91], v[72:75]\n"
"s_add_u32 m0, s73, 114688\n"
"s_nop 0\n"
"buffer_load_dwordx4 v2, s[36:39], s72 offen sc1 lds\n"
"v_mfma_f32_16x16x32_bf16 a[112:115], v[144:147], a[92:95], a[112:115]\n"
"s_add_u32 s72, s71, 262144\n"
"v_mfma_f32_16x16x32_bf16 v[8:11], v[144:147], a[80:83], v[8:11]\n"
"v_mfma_f32_16x16x32_bf16 v[12:15], v[148:151], a[80:83], v[12:15]\n"
"s_mul_i32 s73, s65, 1024\n"
"s_add_u32 m0, s73, 118784\n"
"s_min_u32 s73, s65, 1\n"
"s_lshl_b32 s73, s73, 30\n"
"v_or_b32 v2, s73, v2\n"
"buffer_load_dwordx4 v2, s[36:39], s72 offen sc1 lds\n"
"v_mfma_f32_16x16x32_bf16 a[116:119], v[148:151], a[92:95], a[116:119]\n"
"s_waitcnt vmcnt(30)\n"
"v_lshlrev_b32 v176, 16, v164\n"
"v_mfma_f32_16x16x32_bf16 a[120:123], v[80:83], a[92:95], a[120:123]\n"
"v_and_b32 v177, 0xffff0000, v164\n"
"v_lshlrev_b32 v178, 16, v165\n"
"v_mfma_f32_16x16x32_bf16 v[16:19], v[80:83], a[80:83], v[16:19]\n"
"v_and_b32 v179, 0xffff0000, v165\n"
"v_lshlrev_b32 v180, 16, v166\n"
"v_mfma_f32_16x16x32_bf16 v[20:23], v[84:87], a[80:83], v[20:23]\n"
"v_and_b32 v181, 0xffff0000, v166\n"
"v_lshlrev_b32 v182, 16, v167\n"
"v_mfma_f32_16x16x32_bf16 a[124:127], v[84:87], a[92:95], a[124:127]\n"
"v_and_b32 v183, 0xffff0000, v167\n"
"v_lshlrev_b32 v184, 16, v168\n"
"v_mfma_f32_16x16x32_bf16 a[128:131], v[88:91], a[92:95], a[128:131]\n"
"v_and_b32 v185, 0xffff0000, v168\n"
"v_lshlrev_b32 v186, 16, v169\n"
"v_mfma_f32_16x16x32_bf16 v[24:27], v[88:91], a[80:83], v[24:27]\n"
"v_and_b32 v187, 0xffff0000, v169\n"
"v_lshlrev_b32 v188, 16, v170\n"
"s_waitcnt vmcnt(22)\n"
"s_waitcnt vmcnt(63)\n"
"v_mfma_f32_16x16x32_bf16 v[32:35], v[92:95], a[100:103], v[32:35]\n"
"ds_read_b128 a[176:179], v238 offset:0\n"
"v_and_b32 v189, 0xffff0000, v170\n"
"v_mfma_f32_16x16x32_bf16 v[56:59], v[92:95], a[104:107], v[56:59]\n"
"v_lshlrev_b32 v190, 16, v171\n"
"v_and_b32 v191, 0xffff0000, v171\n"
"v_mfma_f32_16x16x32_bf16 v[60:63], v[96:99], a[104:107], v[60:63]\n"
"ds_read_b128 a[180:183], v238 offset:1024\n"
"v_lshlrev_b32 v192, 16, v172\n"
"v_mfma_f32_16x16x32_bf16 v[36:39], v[96:99], a[100:103], v[36:39]\n"
"v_and_b32 v193, 0xffff0000, v172\n"
"v_lshlrev_b32 v194, 16, v173\n"
"v_mfma_f32_16x16x32_bf16 v[40:43], v[100:103], a[100:103], v[40:43]\n"
"ds_read_b128 a[184:187], v238 offset:2048\n"
"v_and_b32 v195, 0xffff0000, v173\n"
"v_mfma_f32_16x16x32_bf16 v[64:67], v[100:103], a[104:107], v[64:67]\n"
"v_sub_f32 v204, v176, v232\n"
"v_sub_f32 v205, v177, v232\n"
"v_mfma_f32_16x16x32_bf16 v[68:71], v[104:107], a[104:107], v[68:71]\n"
"ds_read_b128 a[188:191], v238 offset:3072\n"
"v_sub_f32 v206, v178, v232\n"
"v_mfma_f32_16x16x32_bf16 v[44:47], v[104:107], a[100:103], v[44:47]\n"
"v_sub_f32 v207, v179, v232\n"
"v_sub_f32 v208, v180, v232\n"
"v_mfma_f32_16x16x32_bf16 v[52:55], v[88:91], a[100:103], v[52:55]\n"
"v_sub_f32 v209, v181, v232\n"
"v_sub_f32 v210, v182, v232\n"
"v_mfma_f32_16x16x32_bf16 v[76:79], v[88:91], a[104:107], v[76:79]\n"
"v_sub_f32 v211, v183, v232\n"
"v_sub_f32 v212, v184, v232\n"
"v_mfma_f32_16x16x32_bf16 a[112:115], v[92:95], a[108:111], a[112:115]\n"
"v_sub_f32 v213, v185, v232\n"
"v_sub_f32 v214, v186, v232\n"
"v_mfma_f32_16x16x32_bf16 v[8:11], v[92:95], a[96:99], v[8:11]\n"
"v_sub_f32 v215, v187, v232\n"
"v_sub_f32 v216, v188, v232\n"
"v_mfma_f32_16x16x32_bf16 v[12:15], v[96:99], a[96:99], v[12:15]\n"
"v_sub_f32 v217, v189, v232\n"
"v_sub_f32 v218, v190, v232\n"
"v_mfma_f32_16x16x32_bf16 a[116:119], v[96:99], a[108:111], a[116:119]\n"
"v_sub_f32 v219, v191, v232\n"
"v_sub_f32 v220, v192, v232\n"
"v_mfma_f32_16x16x32_bf16 a[120:123], v[100:103], a[108:111], a[120:123]\n"
"v_sub_f32 v221, v193, v232\n"
"v_sub_f32 v222, v194, v232\n"
"v_mfma_f32_16x16x32_bf16 v[16:19], v[100:103], a[96:99], v[16:19]\n"
"v_sub_f32 v223, v195, v232\n"
"v_mul_f32 v204, v204, v234\n"
"v_mfma_f32_16x16x32_bf16 v[20:23], v[104:107], a[96:99], v[20:23]\n"
"v_mul_f32 v205, v205, v234\n"
"v_mul_f32 v206, v206, v234\n"
"v_mfma_f32_16x16x32_bf16 a[124:127], v[104:107], a[108:111], a[124:127]\n"
"v_mul_f32 v207, v207, v234\n"
"v_mul_f32 v208, v208, v234\n"
"v_mfma_f32_16x16x32_bf16 a[132:135], v[88:91], a[108:111], a[132:135]\n"
"v_mul_f32 v209, v209, v234\n"
"v_mul_f32 v210, v210, v234\n"
"v_mfma_f32_16x16x32_bf16 v[28:31], v[88:91], a[96:99], v[28:31]\n"
"v_mul_f32 v211, v211, v234\n"
"v_mul_f32 v212, v212, v234\n"
"s_waitcnt vmcnt(18) lgkmcnt(0)\n"
"v_mfma_f32_16x16x32_bf16 v[32:35], v[108:111], a[4:7], v[32:35]\n"
"ds_read_b128 a[192:195], v238 offset:4096\n"
"v_mul_f32 v213, v213, v234\n"
"v_mfma_f32_16x16x32_bf16 v[56:59], v[108:111], a[8:11], v[56:59]\n"
"v_mul_f32 v214, v214, v234\n"
"v_mul_f32 v215, v215, v234\n"
"v_mfma_f32_16x16x32_bf16 v[60:63], a[176:179], a[8:11], v[60:63]\n"
"ds_read_b128 a[196:199], v238 offset:5120\n"
"v_mul_f32 v216, v216, v234\n"
"v_mfma_f32_16x16x32_bf16 v[36:39], a[176:179], a[4:7], v[36:39]\n"
"v_mul_f32 v217, v217, v234\n"
"v_mul_f32 v218, v218, v234\n"
"v_mfma_f32_16x16x32_bf16 v[40:43], a[180:183], a[4:7], v[40:43]\n"
"ds_read_b128 a[200:203], v238 offset:6144\n"
"v_mul_f32 v219, v219, v234\n"
"v_mfma_f32_16x16x32_bf16 v[64:67], a[180:183], a[8:11], v[64:67]\n"
"v_mul_f32 v220, v220, v234\n"
"v_mul_f32 v221, v221, v234\n"
"v_mfma_f32_16x16x32_bf16 v[68:71], a[184:187], a[8:11], v[68:71]\n"
"ds_read_b128 a[204:207], v238 offset:7168\n"
"v_mul_f32 v222, v222, v234\n"
"v_mfma_f32_16x16x32_bf16 v[44:47], a[184:187], a[4:7], v[44:47]\n"
"v_mul_f32 v223, v223, v234\n"
"v_lshrrev_b32 v124, 4, v1\n"
"v_mfma_f32_16x16x32_bf16 v[48:51], a[188:191], a[4:7], v[48:51]\n"
"v_bfe_u32 v125, v1, 2, 2\n"
"v_and_b32 v126, 3, v1\n"
"v_mfma_f32_16x16x32_bf16 v[72:75], a[188:191], a[8:11], v[72:75]\n"
"s_lshl_b32 s71, s65, 5\n"
"s_add_u32 s71, s71, 110592\n"
"v_mfma_f32_16x16x32_bf16 a[112:115], v[108:111], a[12:15], a[112:115]\n"
"v_lshl_add_u32 v126, v126, 3, s71\n"
"v_mfma_f32_16x16x32_bf16 v[8:11], v[108:111], a[0:3], v[8:11]\n"
"v_lshl_add_u32 v126, v125, 7, v126\n"
"v_mfma_f32_16x16x32_bf16 v[12:15], a[176:179], a[0:3], v[12:15]\n"
"v_lshl_add_u32 v127, v124, 10, v126\n"
"v_lshl_add_u32 v128, v124, 9, v126\n"
"v_mfma_f32_16x16x32_bf16 a[116:119], a[176:179], a[12:15], a[116:119]\n"
"s_mul_i32 s71, s65, 15360\n"
"s_add_u32 s71, s71, 36864\n"
"v_mfma_f32_16x16x32_bf16 a[120:123], a[180:183], a[12:15], a[120:123]\n"
"v_lshl_add_u32 v129, v1, 4, s71\n"
"s_mul_i32 s71, s45, 15360\n"
"v_mfma_f32_16x16x32_bf16 v[16:19], a[180:183], a[0:3], v[16:19]\n"
"s_add_u32 s71, s71, 36864\n"
"v_lshl_add_u32 v130, v1, 4, s71\n"
"v_mfma_f32_16x16x32_bf16 v[20:23], a[184:187], a[0:3], v[20:23]\n"
"s_mul_i32 s71, s46, 15360\n"
"s_add_u32 s71, s71, 41984\n"
"v_mfma_f32_16x16x32_bf16 a[124:127], a[184:187], a[12:15], a[124:127]\n"
"v_lshl_add_u32 v131, v1, 4, s71\n"
"s_mul_i32 s71, s47, 15360\n"
"v_mfma_f32_16x16x32_bf16 a[128:131], a[188:191], a[12:15], a[128:131]\n"
"s_add_u32 s71, s71, 47104\n"
"v_lshl_add_u32 v132, v1, 4, s71\n"
"v_mfma_f32_16x16x32_bf16 v[24:27], a[188:191], a[0:3], v[24:27]\n"
"s_waitcnt vmcnt(0)\n"
"s_barrier\n"
"s_waitcnt vmcnt(11) lgkmcnt(0)\n"
"v_mfma_f32_16x16x32_bf16 v[32:35], a[192:195], a[20:23], v[32:35]\n"
"ds_read_b128 a[208:211], v238 offset:8192\n"
"ds_read_b64_tr_b16 v[112:113], v127 offset:0\n"
"v_mfma_f32_16x16x32_bf16 v[56:59], a[192:195], a[24:27], v[56:59]\n"
"ds_read_b64_tr_b16 v[114:115], v127 offset:512\n"
"ds_read_b64_tr_b16 v[116:117], v127 offset:4096\n"
"v_mfma_f32_16x16x32_bf16 v[60:63], a[196:199], a[24:27], v[60:63]\n"
"ds_read_b128 a[212:215], v239 offset:0\n"
"ds_read_b64_tr_b16 v[118:119], v127 offset:4608\n"
"v_mfma_f32_16x16x32_bf16 v[36:39], a[196:199], a[20:23], v[36:39]\n"
"ds_read_b64_tr_b16 v[120:121], v128 offset:8192\n"
"s_lshl_b32 s72, s64, 2\n"
"v_mfma_f32_16x16x32_bf16 v[40:43], a[200:203], a[20:23], v[40:43]\n"
"ds_read_b128 a[216:219], v239 offset:1024\n"
"s_add_u32 s72, s72, s65\n"
"v_mfma_f32_16x16x32_bf16 v[64:67], a[200:203], a[24:27], v[64:67]\n"
"s_cmp_eq_u32 s72, 0\n"
"s_cselect_b32 s72, 0, 0x40000000\n"
"v_mfma_f32_16x16x32_bf16 v[68:71], a[204:207], a[24:27], v[68:71]\n"
"ds_read_b128 a[220:223], v239 offset:2048\n"
"s_mov_b32 s71, 0\n"
"v_mfma_f32_16x16x32_bf16 v[44:47], a[204:207], a[20:23], v[44:47]\n"
"v_mov_b32 v4, s68\n"
"v_mov_b32 v5, s69\n"
"v_mfma_f32_16x16x32_bf16 v[52:55], a[188:191], a[20:23], v[52:55]\n"
"ds_read_b128 a[224:227], v239 offset:3072\n"
"v_min_u32 v6, 1, v1\n"
"v_mfma_f32_16x16x32_bf16 v[76:79], a[188:191], a[24:27], v[76:79]\n"
"v_lshlrev_b32 v6, 30, v6\n"
"v_mfma_f32_16x16x32_bf16 a[112:115], a[192:195], a[28:31], a[112:115]\n"
"v_or_b32 v6, s72, v6\n"
"buffer_store_dwordx2 v[4:5], v6, s[36:39], s71 offen sc1\n"
"v_mfma_f32_16x16x32_bf16 v[8:11], a[192:195], a[16:19], v[8:11]\n"
"v_mfma_f32_16x16x32_bf16 v[12:15], a[196:199], a[16:19], v[12:15]\n"
"v_mfma_f32_16x16x32_bf16 a[116:119], a[196:199], a[28:31], a[116:119]\n"
"v_mfma_f32_16x16x32_bf16 a[120:123], a[200:203], a[28:31], a[120:123]\n"
"v_mfma_f32_16x16x32_bf16 v[16:19], a[200:203], a[16:19], v[16:19]\n"
"v_mfma_f32_16x16x32_bf16 v[20:23], a[204:207], a[16:19], v[20:23]\n"
"v_mfma_f32_16x16x32_bf16 a[124:127], a[204:207], a[28:31], a[124:127]\n"
"v_mfma_f32_16x16x32_bf16 a[132:135], a[188:191], a[28:31], a[132:135]\n"
"v_mfma_f32_16x16x32_bf16 v[28:31], a[188:191], a[16:19], v[28:31]\n"
"s_waitcnt vmcnt(8) lgkmcnt(0)\n"
"v_mfma_f32_16x16x32_bf16 v[32:35], a[208:211], a[36:39], v[32:35]\n"
"ds_read_b128 a[228:231], v239 offset:4096\n"
"v_lshlrev_b32 v134, 16, v112\n"
"v_mfma_f32_16x16x32_bf16 v[56:59], a[208:211], a[40:43], v[56:59]\n"
"v_and_b32 v135, 0xffff0000, v112\n"
"v_lshlrev_b32 v136, 16, v113\n"
"v_mfma_f32_16x16x32_bf16 v[60:63], a[212:215], a[40:43], v[60:63]\n"
"ds_read_b128 a[232:235], v239 offset:5120\n"
"v_and_b32 v137, 0xffff0000, v113\n"
"v_mfma_f32_16x16x32_bf16 v[36:39], a[212:215], a[36:39], v[36:39]\n"
"v_fmac_f32 v176, v204, v134\n"
"v_fmac_f32 v177, v205, v135\n"
"v_mfma_f32_16x16x32_bf16 v[40:43], a[216:219], a[36:39], v[40:43]\n"
"ds_read_b128 a[236:239], v239 offset:6144\n"
"v_fmac_f32 v178, v206, v136\n"
"v_mfma_f32_16x16x32_bf16 v[64:67], a[216:219], a[40:43], v[64:67]\n"
"v_fmac_f32 v179, v207, v137\n"
"v_lshlrev_b32 v134, 16, v114\n"
"v_mfma_f32_16x16x32_bf16 v[68:71], a[220:223], a[40:43], v[68:71]\n"
"v_and_b32 v135, 0xffff0000, v114\n"
"v_lshlrev_b32 v136, 16, v115\n"
"v_mfma_f32_16x16x32_bf16 v[44:47], a[220:223], a[36:39], v[44:47]\n"
"v_and_b32 v137, 0xffff0000, v115\n"
"v_fmac_f32 v180, v208, v134\n"
"v_mfma_f32_16x16x32_bf16 v[48:51], a[224:227], a[36:39], v[48:51]\n"
"v_fmac_f32 v181, v209, v135\n"
"v_fmac_f32 v182, v210, v136\n"
"v_mfma_f32_16x16x32_bf16 v[72:75], a[224:227], a[40:43], v[72:75]\n"
"v_fmac_f32 v183, v211, v137\n"
"v_lshlrev_b32 v134, 16, v116\n"
"v_mfma_f32_16x16x32_bf16 a[112:115], a[208:211], a[44:47], a[112:115]\n"
"v_and_b32 v135, 0xffff0000, v116\n"
"v_lshlrev_b32 v136, 16, v117\n"
"v_mfma_f32_16x16x32_bf16 v[8:11], a[208:211], a[32:35], v[8:11]\n"
"v_and_b32 v137, 0xffff0000, v117\n"
"v_fmac_f32 v184, v212, v134\n"
"v_mfma_f32_16x16x32_bf16 v[12:15], a[212:215], a[32:35], v[12:15]\n"
"v_fmac_f32 v185, v213, v135\n"
"v_fmac_f32 v186, v214, v136\n"
"v_mfma_f32_16x16x32_bf16 a[116:119], a[212:215], a[44:47], a[116:119]\n"
"v_fmac_f32 v187, v215, v137\n"
"v_lshlrev_b32 v134, 16, v118\n"
"v_mfma_f32_16x16x32_bf16 a[120:123], a[216:219], a[44:47], a[120:123]\n"
"v_and_b32 v135, 0xffff0000, v118\n"
"v_lshlrev_b32 v136, 16, v119\n"
"v_mfma_f32_16x16x32_bf16 v[16:19], a[216:219], a[32:35], v[16:19]\n"
"v_and_b32 v137, 0xffff0000, v119\n"
"v_fmac_f32 v188, v216, v134\n"
"v_mfma_f32_16x16x32_bf16 v[20:23], a[220:223], a[32:35], v[20:23]\n"
"v_fmac_f32 v189, v217, v135\n"
"v_fmac_f32 v190, v218, v136\n"
"v_mfma_f32_16x16x32_bf16 a[124:127], a[220:223], a[44:47], a[124:127]\n"
"v_fmac_f32 v191, v219, v137\n"
"v_lshlrev_b32 v134, 16, v120\n"
"v_mfma_f32_16x16x32_bf16 a[128:131], a[224:227], a[44:47], a[128:131]\n"
"v_and_b32 v135, 0xffff0000, v120\n"
"v_lshlrev_b32 v136, 16, v121\n"
"v_mfma_f32_16x16x32_bf16 v[24:27], a[224:227], a[32:35], v[24:27]\n"
"v_and_b32 v137, 0xffff0000, v121\n"
"v_fmac_f32 v192, v220, v134\n"
"s_waitcnt vmcnt(4) lgkmcnt(0)\n"
"s_waitcnt vmcnt(39)\n"
"v_mfma_f32_16x16x32_bf16 v[32:35], a[228:231], a[52:55], v[32:35]\n"
"v_fmac_f32 v193, v221, v135\n"
"v_fmac_f32 v194, v222, v136\n"
"v_mfma_f32_16x16x32_bf16 v[56:59], a[228:231], a[56:59], v[56:59]\n"
"v_fmac_f32 v195, v223, v137\n"
"v_mfma_f32_16x16x32_bf16 v[60:63], a[232:235], a[56:59], v[60:63]\n"
"v_mfma_f32_16x16x32_bf16 v[36:39], a[232:235], a[52:55], v[36:39]\n"
"v_mfma_f32_16x16x32_bf16 v[40:43], a[236:239], a[52:55], v[40:43]\n"
"v_mfma_f32_16x16x32_bf16 v[64:67], a[236:239], a[56:59], v[64:67]\n"
"v_mfma_f32_16x16x32_bf16 v[68:71], a[240:243], a[56:59], v[68:71]\n"
"v_mfma_f32_16x16x32_bf16 v[44:47], a[240:243], a[52:55], v[44:47]\n"
"v_mfma_f32_16x16x32_bf16 v[52:55], a[224:227], a[52:55], v[52:55]\n"
"v_mfma_f32_16x16x32_bf16 v[76:79], a[224:227], a[56:59], v[76:79]\n"
"v_mfma_f32_16x16x32_bf16 a[112:115], a[228:231], a[60:63], a[112:115]\n"
"v_mfma_f32_16x16x32_bf16 v[8:11], a[228:231], a[48:51], v[8:11]\n"
"v_mfma_f32_16x16x32_bf16 v[12:15], a[232:235], a[48:51], v[12:15]\n"
"v_mfma_f32_16x16x32_bf16 a[116:119], a[232:235], a[60:63], a[116:119]\n"
"v_mfma_f32_16x16x32_bf16 a[120:123], a[236:239], a[60:63], a[120:123]\n"
"v_mfma_f32_16x16x32_bf16 v[16:19], a[236:239], a[48:51], v[16:19]\n"
"v_mfma_f32_16x16x32_bf16 v[20:23], a[240:243], a[48:51], v[20:23]\n"
"v_mfma_f32_16x16x32_bf16 a[124:127], a[240:243], a[60:63], a[124:127]\n"
"v_mfma_f32_16x16x32_bf16 a[132:135], a[224:227], a[60:63], a[132:135]\n"
"v_mfma_f32_16x16x32_bf16 v[28:31], a[224:227], a[48:51], v[28:31]\n"
"ds_write_b128 v130, v[32:35] offset:0\n"
"ds_write_b128 v130, v[36:39] offset:1024\n"
"ds_write_b128 v130, v[40:43] offset:2048\n"
"ds_write_b128 v130, v[44:47] offset:3072\n"
"ds_write_b128 v131, v[56:59] offset:0\n"
"ds_write_b128 v131, v[60:63] offset:1024\n"
"ds_write_b128 v131, v[64:67] offset:2048\n"
"ds_write_b128 v131, v[68:71] offset:3072\n"
"ds_write_b128 v132, a[112:115] offset:0\n"
"ds_write_b128 v132, a[116:119] offset:1024\n"
"ds_write_b128 v132, a[120:123] offset:2048\n"
"ds_write_b128 v132, a[124:127] offset:3072\n"
"s_mov_b32 exec_hi, 0\n"
"ds_write_b128 v130, v[48:51] offset:4096\n"
"ds_write_b128 v131, v[72:75] offset:4096\n"
"ds_write_b128 v132, a[128:131] offset:4096\n"
"s_mov_b32 exec_hi, -1\n"
"s_mov_b32 exec_lo, 0\n"
"ds_write_b128 v130, v[52:55] offset:4096\n"
"ds_write_b128 v131, v[76:79] offset:4096\n"
"ds_write_b128 v132, a[132:135] offset:4096\n"
"v_mov_b32 v24, v28\n"
"v_mov_b32 v25, v29\n"
"v_mov_b32 v26, v30\n"
"v_mov_b32 v27, v31\n"
"s_mov_b32 exec_lo, -1\n"
"v_pk_add_f32 v[8:9], v[8:9], v[176:177]\n"
"v_pk_add_f32 v[10:11], v[10:11], v[178:179]\n"
"v_pk_add_f32 v[12:13], v[12:13], v[180:181]\n"
"v_pk_add_f32 v[14:15], v[14:15], v[182:183]\n"
"v_pk_add_f32 v[16:17], v[16:17], v[184:185]\n"
"v_pk_add_f32 v[18:19], v[18:19], v[186:187]\n"
"v_pk_add_f32 v[20:21], v[20:21], v[188:189]\n"
"v_pk_add_f32 v[22:23], v[22:23], v[190:191]\n"
"s_waitcnt lgkmcnt(0)\n"
"s_barrier\n"
"ds_read_b128 v[32:35], v129 offset:4096\n"
"ds_read_b128 v[36:39], v129 offset:9216\n"
"ds_read_b128 v[40:43], v129 offset:14336\n"
"ds_read_b128 v[44:47], v129 offset:0\n"
"ds_read_b128 v[48:51], v129 offset:5120\n"
"ds_read_b128 v[52:55], v129 offset:10240\n"
"ds_read_b128 v[56:59], v129 offset:1024\n"
"ds_read_b128 v[60:63], v129 offset:6144\n"
"ds_read_b128 v[64:67], v129 offset:11264\n"
"ds_read_b128 v[68:71], v129 offset:2048\n"
"ds_read_b128 v[72:75], v129 offset:7168\n"
"ds_read_b128 v[76:79], v129 offset:12288\n"
"ds_read_b128 v[152:155], v129 offset:3072\n"
"ds_read_b128 v[156:159], v129 offset:8192\n"
"ds_read_b128 v[160:163], v129 offset:13312\n"
"s_waitcnt lgkmcnt(12)\n"
"v_pk_add_f32 v[32:33], v[32:33], v[36:37]\n"
"v_pk_add_f32 v[24:25], v[24:25], v[40:41]\n"
"v_pk_add_f32 v[34:35], v[34:35], v[38:39]\n"
"v_pk_add_f32 v[26:27], v[26:27], v[42:43]\n"
"v_pk_add_f32 v[24:25], v[24:25], v[32:33]\n"
"v_pk_add_f32 v[26:27], v[26:27], v[34:35]\n"
"v_mov_b32 v28, v24\n"
"v_mov_b32 v29, v25\n"
"v_mov_b32 v30, v26\n"
"v_mov_b32 v31, v27\n"
"v_permlane32_swap_b32 v24, v28\n"
"v_permlane32_swap_b32 v25, v29\n"
"v_permlane32_swap_b32 v26, v30\n"
"v_permlane32_swap_b32 v27, v31\n"
"v_pk_add_f32 v[24:25], v[24:25], v[28:29]\n"
"v_pk_add_f32 v[24:25], v[24:25], v[192:193]\n"
"v_pk_add_f32 v[26:27], v[26:27], v[30:31]\n"
"v_pk_add_f32 v[26:27], v[26:27], v[194:195]\n"
"v_cvt_pk_bf16_f32 v172, v24, v25\n"
"v_cvt_pk_bf16_f32 v173, v26, v27\n"
"buffer_store_dwordx2 v[172:173], v245, s[60:63], s18 offen offset:128 sc1\n"
"s_waitcnt lgkmcnt(9)\n"
"v_pk_add_f32 v[44:45], v[44:45], v[48:49]\n"
"v_pk_add_f32 v[8:9], v[8:9], v[52:53]\n"
"v_pk_add_f32 v[46:47], v[46:47], v[50:51]\n"
"v_pk_add_f32 v[10:11], v[10:11], v[54:55]\n"
"v_pk_add_f32 v[8:9], v[8:9], v[44:45]\n"
"v_pk_add_f32 v[10:11], v[10:11], v[46:47]\n"
"v_cvt_pk_bf16_f32 v164, v8, v9\n"
"v_cvt_pk_bf16_f32 v165, v10, v11\n"
"s_waitcnt lgkmcnt(6)\n"
"v_pk_add_f32 v[56:57], v[56:57], v[60:61]\n"
"v_pk_add_f32 v[12:13], v[12:13], v[64:65]\n"
"v_pk_add_f32 v[58:59], v[58:59], v[62:63]\n"
"v_pk_add_f32 v[14:15], v[14:15], v[66:67]\n"
"v_pk_add_f32 v[12:13], v[12:13], v[56:57]\n"
"v_pk_add_f32 v[14:15], v[14:15], v[58:59]\n"
"v_cvt_pk_bf16_f32 v166, v12, v13\n"
"v_cvt_pk_bf16_f32 v167, v14, v15\n"
"buffer_store_dwordx4 v[164:167], v246, s[60:63], s18 offen offset:0 sc1\n"
"s_waitcnt lgkmcnt(3)\n"
"v_pk_add_f32 v[68:69], v[68:69], v[72:73]\n"
"v_pk_add_f32 v[16:17], v[16:17], v[76:77]\n"
"v_pk_add_f32 v[70:71], v[70:71], v[74:75]\n"
"v_pk_add_f32 v[18:19], v[18:19], v[78:79]\n"
"v_pk_add_f32 v[16:17], v[16:17], v[68:69]\n"
"v_pk_add_f32 v[18:19], v[18:19], v[70:71]\n"
"v_cvt_pk_bf16_f32 v168, v16, v17\n"
"v_cvt_pk_bf16_f32 v169, v18, v19\n"
"s_waitcnt lgkmcnt(0)\n"
"v_pk_add_f32 v[152:153], v[152:153], v[156:157]\n"
"v_pk_add_f32 v[20:21], v[20:21], v[160:161]\n"
"v_pk_add_f32 v[154:155], v[154:155], v[158:159]\n"
"v_pk_add_f32 v[22:23], v[22:23], v[162:163]\n"
"v_pk_add_f32 v[20:21], v[20:21], v[152:153]\n"
"v_pk_add_f32 v[22:23], v[22:23], v[154:155]\n"
"v_cvt_pk_bf16_f32 v170, v20, v21\n"
"v_cvt_pk_bf16_f32 v171, v22, v23\n"
"buffer_store_dwordx4 v[168:171], v246, s[60:63], s18 offen offset:64 sc1\n"
"kend_%=:\n"
  :: "s"(__builtin_amdgcn_kernarg_segment_ptr()), "s"(blockIdx.x), "v"(threadIdx.x), "s"((unsigned)(size_t)(__attribute__((address_space(3))) char*)smem)
  : "v1","v2","v3","v4","v5","v6","v7","v8","v9","v10","v11","v12","v13","v14","v15","v16",
"v17","v18","v19","v20","v21","v22","v23","v24","v25","v26","v27","v28","v29","v30","v31","v32",
"v33","v34","v35","v36","v37","v38","v39","v40","v41","v42","v43","v44","v45","v46","v47","v48",
"v49","v50","v51","v52","v53","v54","v55","v56","v57","v58","v59","v60","v61","v62","v63","v64",
"v65","v66","v67","v68","v69","v70","v71","v72","v73","v74","v75","v76","v77","v78","v79","v80",
"v81","v82","v83","v84","v85","v86","v87","v88","v89","v90","v91","v92","v93","v94","v95","v96",
"v97","v98","v99","v100","v101","v102","v103","v104","v105","v106","v107","v108","v109","v110","v111","v112",
"v113","v114","v115","v116","v117","v118","v119","v120","v121","v122","v123","v124","v125","v126","v127","v128",
"v129","v130","v131","v132","v133","v134","v135","v136","v137","v138","v139","v140","v141","v142","v143","v144",
"v145","v146","v147","v148","v149","v150","v151","v152","v153","v154","v155","v156","v157","v158","v159","v160",
"v161","v162","v163","v164","v165","v166","v167","v168","v169","v170","v171","v172","v173","v174","v175","v176",
"v177","v178","v179","v180","v181","v182","v183","v184","v185","v186","v187","v188","v189","v190","v191","v192",
"v193","v194","v195","v196","v197","v198","v199","v200","v201","v202","v203","v204","v205","v206","v207","v208",
"v209","v210","v211","v212","v213","v214","v215","v216","v217","v218","v219","v220","v221","v222","v223","v224",
"v225","v226","v227","v228","v229","v230","v231","v232","v233","v234","v235","v236","v237","v238","v239","v240",
"v241","v242","v243","v244","v245","v246","v247","v248","v249","v250","v251","v252","v253","v254","v255","a0",
"a1","a2","a3","a4","a5","a6","a7","a8","a9","a10","a11","a12","a13","a14","a15","a16",
"a17","a18","a19","a20","a21","a22","a23","a24","a25","a26","a27","a28","a29","a30","a31","a32",
"a33","a34","a35","a36","a37","a38","a39","a40","a41","a42","a43","a44","a45","a46","a47","a48",
"a49","a50","a51","a52","a53","a54","a55","a56","a57","a58","a59","a60","a61","a62","a63","a64",
"a65","a66","a67","a68","a69","a70","a71","a72","a73","a74","a75","a76","a77","a78","a79","a80",
"a81","a82","a83","a84","a85","a86","a87","a88","a89","a90","a91","a92","a93","a94","a95","a96",
"a97","a98","a99","a100","a101","a102","a103","a104","a105","a106","a107","a108","a109","a110","a111","a112",
"a113","a114","a115","a116","a117","a118","a119","a120","a121","a122","a123","a124","a125","a126","a127","a128",
"a129","a130","a131","a132","a133","a134","a135","a136","a137","a138","a139","a140","a141","a142","a143","a144",
"a145","a146","a147","a148","a149","a150","a151","a152","a153","a154","a155","a156","a157","a158","a159","a160",
"a161","a162","a163","a164","a165","a166","a167","a168","a169","a170","a171","a172","a173","a174","a175","a176",
"a177","a178","a179","a180","a181","a182","a183","a184","a185","a186","a187","a188","a189","a190","a191","a192",
"a193","a194","a195","a196","a197","a198","a199","a200","a201","a202","a203","a204","a205","a206","a207","a208",
"a209","a210","a211","a212","a213","a214","a215","a216","a217","a218","a219","a220","a221","a222","a223","a224",
"a225","a226","a227","a228","a229","a230","a231","a232","a233","a234","a235","a236","a237","a238","a239","a240",
"a241","a242","a243","a244","a245","a246","a247","a248","a249","a250","a251","a252","a253","a254","a255","s4",
"s5","s6","s7","s8","s9","s10","s11","s12","s13","s14","s15","s16","s17","s18","s19","s20",
"s21","s22","s23","s24","s25","s26","s27","s28","s29","s30","s31","s32","s33","s34","s35","s36",
"s37","s38","s39","s40","s41","s42","s43","s44","s45","s46","s47","s48","s49","s50","s51","s52",
"s53","s54","s55","s56","s57","s58","s59","s60","s61","s62","s63","s64","s65","s66","s67","s68",
"s69","s70","s71","s72","s73","s74","s75","s76","s77","s78","s79","s80","s81","s82","s83","s84",
"s85","s86","s87","s88","s89","s90","s91","s92","s93","s94","s95","s96","s97","s98","s99","s100",
"s101","vcc","scc","memory");
}

extern "C" void kernel_launch(void* const* d_in, const int* in_sizes, int n_in, void* d_out, int out_size, void* d_ws, size_t ws_size, hipStream_t stream) {
  fused_h3_gmlp<<<256, 256, 0, stream>>>(d_in[0], d_in[1], d_in[2], d_in[3], d_in[4], d_in[5], d_in[7], d_in[8], d_in[9], d_in[10], d_out, d_ws);
}
```
